```python
import torch

def _softmax(x, axis=-1):
    return torch.softmax(x, dim=axis)
def _gelu(x):
    return torch.nn.functional.gelu(x, approximate="tanh")
def _silu(x):
    return torch.nn.functional.silu(x)
def _sigmoid(x):
    return torch.sigmoid(x)
def _layernorm(x, axis=-1, eps=1e-5):
    m = x.mean(dim=axis, keepdim=True)
    v = x.var(dim=axis, keepdim=True, unbiased=False)
    return (x - m) / torch.sqrt(v + eps)
def _scatter_add(tgt, idx, src, axis=0):
    return tgt.clone().index_add_(axis, idx, src)
def _segment_sum(src, seg, n):
    out = torch.zeros((n,) + src.shape[1:], dtype=src.dtype)
    return out.index_add_(0, seg, src)
def _pad(x, pw):
    flat = tuple(v for pair in reversed(pw) for v in pair)
    return torch.nn.functional.pad(x, flat)

kernel_name = 'gated_mix__ssm_conv_s4__mixer_token_mlp__prompt'

def setup_inputs():

    _w = (lambda a: (lambda t: t.to(torch.bfloat16) if t.is_floating_point() else t)(torch.as_tensor(a)))
    def _A_setup():
        B, L, D, N = 1, 2048, 512, 128
        A_re = -(lambda _x: torch.exp(torch.as_tensor(_x)))((lambda *s: torch.randn(*s, dtype=torch.bfloat16) * 3.2)(D, N))
        A_im = torch.pi * (lambda *s: torch.randn(*s, dtype=torch.bfloat16) * 0.05)(D, N)
        B_re = (lambda *s: torch.randn(*s, dtype=torch.bfloat16) * 0.1)(D, N)
        B_im = (lambda *s: torch.randn(*s, dtype=torch.bfloat16) * 0.1)(D, N)
        C_re = (lambda *s: torch.randn(*s, dtype=torch.bfloat16) * 0.1)(D, N)
        C_im = (lambda *s: torch.randn(*s, dtype=torch.bfloat16) * 0.1)(D, N)
        log_dt = (lambda *s: torch.randn(*s, dtype=torch.bfloat16) * 0.05)(D) * 0.5 - 4.0
        Dskip = (lambda *s: torch.randn(*s, dtype=torch.bfloat16) * 0.05)(D)
        return {
            "u": _w((lambda *s: torch.randn(*s, dtype=torch.bfloat16) * 0.05)(B, L, D)),
            "A_re": _w(A_re),
            "A_im": _w(A_im),
            "B_re": _w(B_re),
            "B_im": _w(B_im),
            "C_re": _w(C_re),
            "C_im": _w(C_im),
            "log_dt": _w(log_dt),
            "Dskip": _w(Dskip),
        }
    _A_in = _A_setup()

    def _B_setup():
        B, S, D, F = 1, 2048, 512, 1792
        return {
            "x": _w((lambda *s: torch.randn(*s, dtype=torch.bfloat16) * 0.05)(B, S, D)),
            "w1": _w((lambda *s: torch.randn(*s, dtype=torch.bfloat16) * 0.2)(S, F) * (S ** -0.5)),
            "b1": _w((lambda *s: torch.randn(*s, dtype=torch.bfloat16) * 3.2)(F) * 0.02),
            "w2": _w((lambda *s: torch.randn(*s, dtype=torch.bfloat16) * 0.05)(F, S) * (F ** -0.5)),
            "b2": _w((lambda *s: torch.randn(*s, dtype=torch.bfloat16) * 0.1)(S) * 0.02),
        }
    _B_in = _B_setup()

    return {
        "A_u": _A_in["u"],
        "A_A_re": _A_in["A_re"],
        "A_A_im": _A_in["A_im"],
        "A_B_re": _A_in["B_re"],
        "A_B_im": _A_in["B_im"],
        "A_C_re": _A_in["C_re"],
        "A_C_im": _A_in["C_im"],
        "A_log_dt": _A_in["log_dt"],
        "A_Dskip": _A_in["Dskip"],
        "B_x": _B_in["x"],
        "B_w1": _B_in["w1"],
        "B_b1": _B_in["b1"],
        "B_w2": _B_in["w2"],
        "B_b2": _B_in["b2"],
        "gate_w": _w((lambda *s: torch.randn(*s, dtype=torch.bfloat16) * 0.05)(512) * 16.0),
    }

def reference(A_u, A_A_re, A_A_im, A_B_re, A_B_im, A_C_re, A_C_im, A_log_dt, A_Dskip, B_x, B_w1, B_b1, B_w2, B_b2, gate_w):
    def _A_ref(u, A_re, A_im, B_re, B_im, C_re, C_im, log_dt, Dskip):
        Bsz, L, D = u.shape
        dt = torch.exp(log_dt)[:, None]
        A = A_re + 1j * A_im
        Bc = B_re + 1j * B_im
        Cc = C_re + 1j * C_im
        logdA = dt * A
        dB = dt * Bc
        l = (lambda x, dt: x.to(dt))(torch.arange(L), A_re.dtype)
        P = torch.exp(logdA[:, :, None] * l[None, None, :])
        K = torch.einsum("dn,dnl,dn->dl", Cc, P, dB).real
        L2 = 2 * L
        Kp = _pad(K, ((0, 0), (0, L)))
        up = (lambda x, *d: x.permute(*d))(u, 0, 2, 1)
        up = _pad(up, ((0, 0), (0, 0), (0, L)))
        Kf = (lambda x, axes=(-2,-1): torch.fft.rfft2(x.float(), dim=axes))(Kp[:, None, :], axes=(-2, -1))
        Uf = (lambda x, axes=(-2,-1): torch.fft.rfft2(x.float(), dim=axes))(up[:, :, None, :], axes=(-2, -1))
        Yf = Uf * Kf[None, :, :, :]
        y = (lambda x, s, axes=(-2,-1): torch.fft.irfft2(x, s=s, dim=axes).to(torch.bfloat16))(Yf, (1, L2), axes=(-2, -1))[:, :, 0, :L]
        y = (lambda x, *d: x.permute(*d))(y, 0, 2, 1) + u * Dskip[None, None, :]
        return y

    def _B_ref(x, w1, b1, w2, b2):
        B, S, D = x.shape
        h = _layernorm(x, axis=-1)
        u = torch.einsum("bsd,sf->bdf", h, w1) + b1
        u = _gelu(u)
        y = torch.einsum("bdf,fs->bds", u, w2) + b2
        return (lambda x, *d: x.permute(*d))(y, 0, 2, 1)

    x = A_u
    g = _sigmoid((x * gate_w).sum(-1, keepdims=True))
    a = _A_ref(x, A_A_re, A_A_im, A_B_re, A_B_im, A_C_re, A_C_im, A_log_dt, A_Dskip)
    b = _B_ref(x, B_w1, B_b1, B_w2, B_b2)
    return g * a + (1 - g) * b
```

```cpp
#include <hip/hip_runtime.h>
#include <hip/hip_bf16.h>
#include <stdint.h>

typedef unsigned short u16;
__device__ __forceinline__ float bf2f(u16 v) { return __uint_as_float(((unsigned)v) << 16); }
__device__ __forceinline__ u16 f2bf(float f) {
    unsigned u = __float_as_uint(f);
    u += 0x7fffu + ((u >> 16) & 1u);
    return (u16)(u >> 16);
}
__device__ __forceinline__ float rbf(float f) { return bf2f(f2bf(f)); }

constexpr int L = 2048, D = 512, NS = 128, F = 1792;

__global__ void k_ln(const u16* x, const u16* u, const u16* gw, u16* h, float* g) {
    int s = blockIdx.x;
    int t = threadIdx.x;
    float xv[8], sum = 0.f;
    for (int i = 0; i < 8; i++) { xv[i] = bf2f(x[s * D + t * 8 + i]); sum += xv[i]; }
    for (int o = 32; o; o >>= 1) sum += __shfl_xor(sum, o);
    float mean = sum / D;
    float sq = 0.f;
    for (int i = 0; i < 8; i++) { float dlt = xv[i] - mean; sq += dlt * dlt; }
    for (int o = 32; o; o >>= 1) sq += __shfl_xor(sq, o);
    float var = rbf(sq / D);
    float m = rbf(mean);
    float sd = rbf(sqrtf(rbf(var + 1e-5f)));
    for (int i = 0; i < 8; i++) h[s * D + t * 8 + i] = f2bf(rbf(xv[i] - m) / sd);
    float gs = 0.f;
    for (int i = 0; i < 8; i++) gs += rbf(bf2f(u[s * D + t * 8 + i]) * bf2f(gw[t * 8 + i]));
    for (int o = 32; o; o >>= 1) gs += __shfl_xor(gs, o);
    gs = rbf(gs);
    if (t == 0) g[s] = rbf(1.f / (1.f + expf(-gs)));
}

__global__ void k_kgen(const u16* Are, const u16* Aim, const u16* Bre, const u16* Bim, const u16* Cre, const u16* Cim,
                       const u16* logdt, float* K) {
    int d = blockIdx.y;
    int l = blockIdx.x * 256 + threadIdx.x;
    float dt = rbf(expf(bf2f(logdt[d])));
    float lf = rbf((float)l);
    float acc = 0.f;
    for (int n = 0; n < NS; n++) {
        float are = dt * bf2f(Are[d * NS + n]), aim = dt * bf2f(Aim[d * NS + n]);
        float bre = dt * bf2f(Bre[d * NS + n]), bim = dt * bf2f(Bim[d * NS + n]);
        float cre = bf2f(Cre[d * NS + n]), cim = bf2f(Cim[d * NS + n]);
        float e = expf(are * lf);
        float sn, cs;
        sincosf(aim * lf, &sn, &cs);
        float pre = e * cs, pim = e * sn;
        float wre = cre * bre - cim * bim, wim = cre * bim + cim * bre;
        acc += wre * pre - wim * pim;
    }
    K[d * L + l] = acc;
}

__global__ void k_conv(const float* K, const u16* u, const u16* Dskip, u16* a) {
    int d = blockIdx.y * 64 + threadIdx.x;
    int t = blockIdx.x;
    float acc = 0.f;
    for (int j = 0; j <= t; j++) acc += K[d * L + j] * bf2f(u[(t - j) * D + d]);
    float y = rbf(acc);
    a[t * D + d] = f2bf(y + rbf(bf2f(u[t * D + d]) * bf2f(Dskip[d])));
}

__global__ void k_g1(const u16* h, const u16* w1, const u16* b1, u16* U) {
    int f = blockIdx.x * 64 + threadIdx.x;
    int d = blockIdx.y;
    float acc = 0.f;
    for (int s = 0; s < L; s++) acc += bf2f(h[s * D + d]) * bf2f(w1[s * F + f]);
    float v = rbf(rbf(acc) + bf2f(b1[f]));
    float t = tanhf(0.7978845608028654f * (v + 0.044715f * v * v * v));
    U[d * F + f] = f2bf(0.5f * v * (1.f + t));
}

__global__ void k_g2(const u16* U, const u16* w2, const u16* b2, const u16* a, const float* g, u16* out) {
    int s = blockIdx.x * 64 + threadIdx.x;
    int d = blockIdx.y;
    float acc = 0.f;
    for (int f = 0; f < F; f++) acc += bf2f(U[d * F + f]) * bf2f(w2[f * L + s]);
    float b = rbf(rbf(acc) + bf2f(b2[s]));
    float gg = g[s];
    float r = rbf(gg * bf2f(a[s * D + d])) + rbf(rbf(1.f - gg) * b);
    out[s * D + d] = f2bf(r);
}

extern "C" void kernel_launch(void* const* d_in, const int* in_sizes, int n_in,
                              void* d_out, int out_size, void* d_ws, size_t ws_size,
                              hipStream_t stream) {
    const u16* u = (const u16*)d_in[0];
    const u16* Are = (const u16*)d_in[1];
    const u16* Aim = (const u16*)d_in[2];
    const u16* Bre = (const u16*)d_in[3];
    const u16* Bim = (const u16*)d_in[4];
    const u16* Cre = (const u16*)d_in[5];
    const u16* Cim = (const u16*)d_in[6];
    const u16* logdt = (const u16*)d_in[7];
    const u16* Dskip = (const u16*)d_in[8];
    const u16* x = (const u16*)d_in[9];
    const u16* w1 = (const u16*)d_in[10];
    const u16* b1 = (const u16*)d_in[11];
    const u16* w2 = (const u16*)d_in[12];
    const u16* b2 = (const u16*)d_in[13];
    const u16* gw = (const u16*)d_in[14];
    char* ws = (char*)d_ws;
    u16* h = (u16*)ws;
    float* g = (float*)(ws + (2 << 20));
    float* K = (float*)(ws + (4 << 20));
    u16* a = (u16*)(ws + (8 << 20));
    u16* U = (u16*)(ws + (12 << 20));
    k_ln<<<L, 64, 0, stream>>>(u, u, gw, h, g);
    k_kgen<<<dim3(L / 256, D), 256, 0, stream>>>(Are, Aim, Bre, Bim, Cre, Cim, logdt, K);
    k_conv<<<dim3(L, D / 64), 64, 0, stream>>>(K, u, Dskip, a);
    k_g1<<<dim3(F / 64, D), 64, 0, stream>>>(h, w1, b1, U);
    k_g2<<<dim3(L / 64, D), 64, 0, stream>>>(U, w2, b2, a, g, (u16*)d_out);
}
```

```cpp
#include <hip/hip_runtime.h>
#include <hip/hip_bf16.h>
#include <stdint.h>

typedef unsigned short u16;
typedef unsigned int u32;
using bf16x8 = __attribute__((ext_vector_type(8))) __bf16;
using bf16x2 = __attribute__((ext_vector_type(2))) __bf16;
using s16x4 = __attribute__((ext_vector_type(4))) short;
using f32x4 = __attribute__((ext_vector_type(4))) float;
using f32x2 = __attribute__((ext_vector_type(2))) float;
using u32x4 = __attribute__((ext_vector_type(4))) unsigned;
using u32x2 = __attribute__((ext_vector_type(2))) unsigned;
typedef __attribute__((address_space(3))) s16x4* lds_s16x4_ptr;

#if defined(__HIP_DEVICE_COMPILE__)
#define NOPK __attribute__((target("no-packed-fp32-ops")))
#else
#define NOPK
#endif
#define DEV NOPK __device__ __forceinline__

constexpr int L = 2048, D = 512, NS = 128, F = 1792;

DEV float bf2f(u16 v) { return __uint_as_float(((unsigned)v) << 16); }
DEV u32 pk2bf(float a, float b) {
    f32x2 v = {a, b};
    return __builtin_bit_cast(u32, __builtin_convertvector(v, bf16x2));
}
DEV float rbf(float f) { return __uint_as_float(pk2bf(f, 0.f) << 16); }
DEV float lo_f(u32 p) { return __uint_as_float(p << 16); }
DEV float hi_f(u32 p) { return __uint_as_float(p & 0xffff0000u); }

DEV u32x2 tr_read(const char* lds_generic_base, unsigned off) {
    s16x4 v = __builtin_amdgcn_ds_read_tr16_b64_v4i16((lds_s16x4_ptr)(lds_generic_base + off));
    return __builtin_bit_cast(u32x2, v);
}
DEV f32x4 mfma16(u32x4 a, u32x4 b, f32x4 c) {
    return __builtin_amdgcn_mfma_f32_16x16x32_bf16(__builtin_bit_cast(bf16x8, a), __builtin_bit_cast(bf16x8, b), c, 0, 0, 0);
}

DEV void dma16(unsigned lds, unsigned voff, const void* sbase) {
    unsigned keep;
    asm volatile("s_mov_b32 %0, m0\n\ts_mov_b32 m0, %1\n\ts_nop 0\n\tglobal_load_lds_dwordx4 %2, %3\n\ts_mov_b32 m0, %0"
                 : "=&s"(keep) : "s"(lds), "v"(voff), "s"(sbase) : "memory");
}
template <int IMM>
DEV void gload16(u32x4& dst, unsigned voff, const void* sbase) {
    asm volatile("global_load_dwordx4 %0, %1, %2 offset:%3" : "=v"(dst) : "v"(voff), "s"(sbase), "i"(IMM) : "memory");
}
template <int N>
DEV void wait_vm(u32x4& a, u32x4& b, u32x4& c, u32x4& d) {
    asm volatile("s_waitcnt vmcnt(%4)" : "+v"(a), "+v"(b), "+v"(c), "+v"(d) : "i"(N) : "memory");
}
template <int N>
DEV void wait_vm0() { asm volatile("s_waitcnt vmcnt(%0)" ::"i"(N) : "memory"); }
DEV void wait_lgkm0() { asm volatile("s_waitcnt lgkmcnt(0)" ::: "memory"); }

DEV float wave_sum(float v) {
#pragma unroll
    for (int o = 32; o; o >>= 1) v += __shfl_xor(v, o);
    return v;
}

NOPK __global__ __launch_bounds__(512) void k_prep(const u16* __restrict__ u, const u16* __restrict__ gw,
                                              u16* __restrict__ hF, u16* __restrict__ uT, float* __restrict__ g) {
    extern __shared__ __attribute__((aligned(16))) char smem[];
    char* Hs = smem;
    char* Us = smem + 32768;
    const int tid = threadIdx.x, lane = tid & 63, w = tid >> 6;
    const int s0 = blockIdx.x * 32;
    u32x4 xv[4];
#pragma unroll
    for (int i = 0; i < 4; i++) xv[i] = *(const u32x4*)(u + (size_t)(s0 + 4 * w + i) * D + lane * 8);
    u32x4 gv = *(const u32x4*)(gw + lane * 8);
#pragma unroll
    for (int i = 0; i < 4; i++) {
        const int r = 4 * w + i;
        float x[8];
#pragma unroll
        for (int j = 0; j < 4; j++) { x[2 * j] = lo_f(xv[i][j]); x[2 * j + 1] = hi_f(xv[i][j]); }
        float sum = 0.f, gs = 0.f;
#pragma unroll
        for (int j = 0; j < 4; j++) {
            sum += x[2 * j] + x[2 * j + 1];
            u32 p = pk2bf(x[2 * j] * lo_f(gv[j]), x[2 * j + 1] * hi_f(gv[j]));
            gs += lo_f(p) + hi_f(p);
        }
        sum = wave_sum(sum);
        gs = wave_sum(gs);
        const float mean = sum * (1.f / D);
        float sq = 0.f;
#pragma unroll
        for (int j = 0; j < 8; j++) { float dl = x[j] - mean; sq += dl * dl; }
        sq = wave_sum(sq);
        const float var = rbf(sq * (1.f / D));
        const float m = rbf(mean);
        const float sd = rbf(sqrtf(rbf(var + 1e-5f)));
        u32x4 hv;
#pragma unroll
        for (int j = 0; j < 4; j++) {
            u32 c = pk2bf(x[2 * j] - m, x[2 * j + 1] - m);
            hv[j] = pk2bf(lo_f(c) / sd, hi_f(c) / sd);
        }
        const int sw = (r & 3) | (((r >> 3) & 1) << 2);
        const int c16 = lane ^ (sw << 1);
        *(u32x4*)(Hs + r * 1024 + c16 * 16) = hv;
        *(u32x4*)(Us + r * 1024 + c16 * 16) = xv[i];
        if (lane == 0) {
            float gb = rbf(gs);
            g[s0 + r] = rbf(1.f / (1.f + __expf(-gb)));
        }
    }
    __syncthreads();
    const int kq = lane >> 4, q = (lane & 15) >> 2, p = lane & 3;
#pragma unroll
    for (int i = 0; i < 4; i++) {
        const int cb = w * 4 + i;
        u32x2 h0, h1, u0, u1;
        {
            const int r0 = 8 * kq + q, r1 = r0 + 4;
            const int sw0 = (r0 & 3) | (((r0 >> 3) & 1) << 2);
            const int sw1 = (r1 & 3) | (((r1 >> 3) & 1) << 2);
            const unsigned o0 = r0 * 1024 + ((cb ^ sw0) << 5) + p * 8;
            const unsigned o1 = r1 * 1024 + ((cb ^ sw1) << 5) + p * 8;
            h0 = tr_read(Hs, o0); h1 = tr_read(Hs, o1);
            u0 = tr_read(Us, o0); u1 = tr_read(Us, o1);
        }
        u32x4 hv = {h0[0], h0[1], h1[0], h1[1]};
        u32x4 uv = {u0[0], u0[1], u1[0], u1[1]};
        const int dt = cb >> 2, nt = cb & 3, ks = blockIdx.x;
        *(u32x4*)(hF + ((size_t)((dt * 64 + ks) * 4 + nt) * 64 + lane) * 8) = hv;
        const int d = cb * 16 + (lane & 15);
        *(u32x4*)(uT + (size_t)d * L + s0 + kq * 8) = uv;
    }
}

constexpr int DW = 4, DX = 4;
template <int S> struct Sched {
    static constexpr int posW(int idx) {
        int cnt = 0;
        for (int tau = -DW - 1; tau < S; tau++) {
            int wi = tau + 1 + DW, xi = tau + DX;
            if (wi >= 0 && wi < S) { cnt += 4; if (wi == idx) return cnt; }
            if (xi >= 0 && xi < S) { cnt += 4; }
        }
        return -1;
    }
    static constexpr int posX(int idx) {
        int cnt = 0;
        for (int tau = -DW - 1; tau < S; tau++) {
            int wi = tau + 1 + DW, xi = tau + DX;
            if (wi >= 0 && wi < S) { cnt += 4; }
            if (xi >= 0 && xi < S) { cnt += 4; if (xi == idx) return cnt; }
        }
        return -1;
    }
    static constexpr int issued(int tau_last) {
        int cnt = 0;
        for (int tau = -DW - 1; tau <= tau_last; tau++) {
            int wi = tau + 1 + DW, xi = tau + DX;
            if (wi >= 0 && wi < S) cnt += 4;
            if (xi >= 0 && xi < S) cnt += 4;
        }
        return cnt;
    }
    static constexpr int waitN(int t) {
        int need = 0;
        if (t + 1 < S) need = posW(t + 1);
        if (t >= 0 && posX(t) > need) need = posX(t);
        return issued(t - 1) - need;
    }
};

struct GemmState {
    const char* wbase;
    const char* xbase;
    unsigned wvoff[4];
    unsigned xvoff;
    unsigned ring;
    unsigned raddr[4];
};

template <int S, int WPITCH, bool W_IS_A>
struct GemmCtx {
    const char* smem;
    const char* wbase;
    const char* xbase;
    unsigned ring;
    unsigned wvoff[4];
    unsigned xvoff;
    unsigned raddr[4];
    u32x4 X[DX][4];
    u32x4 Wf[2][4];
    f32x4 acc[4][4];
};
template <int IDX, int S, int WPITCH, bool W_IS_A>
DEV void issue_w(GemmCtx<S, WPITCH, W_IS_A>& c) {
    if constexpr (IDX >= 0 && IDX < S) {
        const char* b = c.wbase + (size_t)IDX * 32 * WPITCH;
        const unsigned slot = c.ring + (IDX % DW) * 4096;
#pragma unroll
        for (int i = 0; i < 4; i++) dma16(slot + i * 1024, c.wvoff[i], b);
    }
}
template <int IDX, int S, int WPITCH, bool W_IS_A>
DEV void issue_x(GemmCtx<S, WPITCH, W_IS_A>& c) {
    if constexpr (IDX >= 0 && IDX < S) {
        const char* b = c.xbase + (size_t)IDX * 4096;
        gload16<0>(c.X[IDX % DX][0], c.xvoff, b);
        gload16<1024>(c.X[IDX % DX][1], c.xvoff, b);
        gload16<2048>(c.X[IDX % DX][2], c.xvoff, b);
        gload16<3072>(c.X[IDX % DX][3], c.xvoff, b);
    }
}
template <int IDX, int S, int WPITCH, bool W_IS_A>
DEV void read_w(GemmCtx<S, WPITCH, W_IS_A>& c) {
    if constexpr (IDX >= 0 && IDX < S) {
        constexpr unsigned so = (IDX % DW) * 4096;
#pragma unroll
        for (int i = 0; i < 4; i++) {
            u32x2 a = tr_read(c.smem, c.raddr[i] + so);
            u32x2 b = tr_read(c.smem, c.raddr[i] + so + 512);
            c.Wf[IDX & 1][i] = u32x4{a[0], a[1], b[0], b[1]};
        }
    }
}
template <int T, int S, int WPITCH, bool W_IS_A>
DEV void gemm_steps(GemmCtx<S, WPITCH, W_IS_A>& c) {
    if constexpr (T < S) {
        if constexpr (T < -1) {
            issue_w<T + 1 + DW>(c);
            issue_x<T + DX>(c);
        } else if constexpr (T == -1) {
            wait_vm0<Sched<S>::waitN(-1)>();
            read_w<0>(c);
            wait_lgkm0();
            issue_w<DW>(c);
            issue_x<DX - 1>(c);
        } else {
            u32x4(&x)[4] = c.X[T % DX];
            wait_vm<Sched<S>::waitN(T)>(x[0], x[1], x[2], x[3]);
            read_w<T + 1>(c);
#pragma unroll
            for (int i = 0; i < 4; i++)
#pragma unroll
                for (int j = 0; j < 4; j++) {
                    if constexpr (W_IS_A) c.acc[i][j] = mfma16(c.Wf[T & 1][i], x[j], c.acc[i][j]);
                    else c.acc[i][j] = mfma16(x[i], c.Wf[T & 1][j], c.acc[i][j]);
                }
            wait_lgkm0();
            issue_w<T + 1 + DW>(c);
            issue_x<T + DX>(c);
        }
        gemm_steps<T + 1>(c);
    }
}

template <int S, int WPITCH, bool W_IS_A>
DEV void gemm_wave(const char* smem, const char* wbase, const char* xbase, unsigned ring, int lane, f32x4 (&acc)[4][4]) {
    GemmCtx<S, WPITCH, W_IS_A> c;
    c.smem = smem; c.wbase = wbase; c.xbase = xbase; c.ring = ring;
#pragma unroll
    for (int i = 0; i < 4; i++) {
        const int sw = (((lane >> 4) & 1) << 1) | ((i & 1) << 2);
        c.wvoff[i] = (unsigned)((8 * i + (lane >> 3)) * WPITCH + (((lane & 7) ^ sw) << 4));
    }
    c.xvoff = lane * 16;
    const int g = lane >> 4, q = (lane & 15) >> 2, p = lane & 3;
    const int fl = ((q >> 1) & 1) | ((g & 1) << 1);
#pragma unroll
    for (int i = 0; i < 4; i++) c.raddr[i] = ring + (8 * g + q) * 128 + ((i ^ fl) << 5) + p * 8;
#pragma unroll
    for (int i = 0; i < 4; i++)
#pragma unroll
        for (int j = 0; j < 4; j++) c.acc[i][j] = f32x4{0.f, 0.f, 0.f, 0.f};
    gemm_steps<-DW - 1>(c);
#pragma unroll
    for (int i = 0; i < 4; i++)
#pragma unroll
        for (int j = 0; j < 4; j++) acc[i][j] = c.acc[i][j];
}

DEV void lds_arrive(unsigned* cnt, int lane) {
    wait_lgkm0();
    if (lane == 0) __hip_atomic_fetch_add(cnt, 1u, __ATOMIC_RELAXED, __HIP_MEMORY_SCOPE_WORKGROUP);
}
DEV void lds_wait(unsigned* cnt, unsigned target) {
    while (__hip_atomic_load(cnt, __ATOMIC_RELAXED, __HIP_MEMORY_SCOPE_WORKGROUP) < target) __builtin_amdgcn_s_sleep(1);
    asm volatile("" ::: "memory");
}

struct KIdxTable { u16 v[256 * 16]; };
constexpr int round_bf16_int(int x) {
    if (x < 256) return x;
    int sh = 0;
    while ((x >> sh) >= 256) sh++;
    int q = x >> sh, r = x & ((1 << sh) - 1), half = 1 << (sh - 1);
    if (r > half || (r == half && (q & 1))) q++;
    return q << sh;
}
constexpr int compact_pos(int tap) {
    if (tap < 0) return 768;
    int lr = round_bf16_int(tap);
    int row = 0, col = 0;
    if (lr < 256) { row = (lr & 7) + 8 * (lr >> 7); col = (lr >> 3) & 15; }
    else if (lr < 512) { int x = lr - 256; row = 16 + ((x & 7) >> 1) + 4 * (x >> 7); col = (x >> 3) & 15; }
    else if (lr < 1024) { int x = lr - 512; row = 24 + ((x & 7) >> 2) + 2 * (x >> 7); col = (x >> 3) & 15; }
    else if (lr < 2048) { int x = lr - 1024; row = 32 + (x >> 7); col = (x >> 3) & 15; }
    else { row = 40; col = 0; }
    return row * 16 + col;
}
constexpr KIdxTable make_kidx() {
    KIdxTable t{};
    for (int ub = 0; ub < 256; ub++)
        for (int i = 0; i < 16; i++) t.v[ub * 16 + i] = (u16)(4 * compact_pos(2047 - (8 * ub + i)));
    return t;
}
__device__ const KIdxTable KIDX = make_kidx();

DEV f32x2 cmul(f32x2 a, f32x2 b) {
    f32x2 r = a.xx * b;
    f32x2 t = {-a.y, a.y};
    return __builtin_elementwise_fma(t, (f32x2)b.yx, r);
}
DEV f32x2 cexp_scaled(float zr, float zi, float l) {
    const float xr = zr * l, xi = zi * l;
    const float e = __builtin_amdgcn_exp2f(xr * 1.4426950408889634f);
    const float c_hi = 0.15915494309189535f;
    const float c_lo = 6.4206382e-09f;
    float rh = xi * c_hi;
    float rl = __builtin_fmaf(xi, c_hi, -rh) + xi * c_lo;
    float fr = rh - __builtin_rintf(rh);
    fr += rl;
    return f32x2{e * __builtin_amdgcn_cosf(fr), e * __builtin_amdgcn_sinf(fr)};
}
DEV u32x2 split_bf(f32x2 v) {
    u32 h = pk2bf(v.x, v.y);
    u32 l = pk2bf(v.x - lo_f(h), v.y - hi_f(h));
    return u32x2{h, l};
}

constexpr int MID_RING = 0;
constexpr int MID_SSM = 65536;
constexpr int CP_STRIDE = 4160;
constexpr int SSM_COPY = 0;
constexpr int TBL_PITCH = 528;
constexpr int TBL_SIZE = 16 * TBL_PITCH;
constexpr int SSM_U = 16896;
constexpr int SSM_P = SSM_U + 4608;
constexpr int SSM_X = SSM_P + 2 * 3088;
constexpr int SSM_SIZE = SSM_X + 8192;
constexpr int MID_FLAGS = MID_SSM + 2 * SSM_SIZE;
constexpr int MID_LDS = MID_FLAGS + 64;

NOPK __global__ __launch_bounds__(512) void k_mid(const u16* __restrict__ hF, const u16* __restrict__ w1, const u16* __restrict__ b1,
                                             u16* __restrict__ UF,
                                             const u16* __restrict__ Are, const u16* __restrict__ Aim,
                                             const u16* __restrict__ Bre, const u16* __restrict__ Bim,
                                             const u16* __restrict__ Cre, const u16* __restrict__ Cim,
                                             const u16* __restrict__ logdt, const u16* __restrict__ Dskip,
                                             const u16* __restrict__ uT, u16* __restrict__ aT) {
    extern __shared__ __attribute__((aligned(16))) char smem[];
    const int tid = threadIdx.x, lane = tid & 63;
    const int w = __builtin_amdgcn_readfirstlane(tid >> 6);
    unsigned* flags = (unsigned*)(smem + MID_FLAGS);
    if (tid < 16) flags[tid] = 0;
    wait_lgkm0();
    __builtin_amdgcn_s_barrier();

    if (w < 4) {
        const int xcd = blockIdx.x & 7, li = blockIdx.x >> 3;
        if (li >= 28) return;
        const int ft = (xcd >> 1) * 7 + li % 7, dt = (xcd & 1) * 4 + li / 7;
        f32x4 acc[4][4];
#pragma unroll
        for (int i = 0; i < 4; i++)
#pragma unroll
            for (int j = 0; j < 4; j++) acc[i][j] = f32x4{0.f, 0.f, 0.f, 0.f};
        const char* wbase = (const char*)w1 + ((size_t)(w * 512) * F + ft * 64) * 2;
        const char* xbase = (const char*)hF + ((size_t)(dt * 64 + w * 16)) * 4096;
        gemm_wave<16, F * 2, true>(smem, wbase, xbase, MID_RING + w * 16384, lane, acc);
        wait_lgkm0();
        {
            char* mine = smem + MID_RING + w * 16384;
#pragma unroll
            for (int nt = 0; nt < 4; nt++)
#pragma unroll
                for (int mt = 0; mt < 4; mt++)
                    if (nt != w) *(f32x4*)(mine + (nt * 4 + mt) * 1024 + lane * 16) = acc[mt][nt];
        }
        f32x4 own[4];
#define OWN(k) if (w == k) { _Pragma("unroll") for (int mt = 0; mt < 4; mt++) { own[mt] = acc[mt][k]; asm volatile("" : "+v"(own[mt])); } }
        OWN(0) OWN(1) OWN(2) OWN(3)
#undef OWN
        lds_arrive(&flags[0], lane);
        lds_wait(&flags[0], 4);
        f32x4 r[4];
#pragma unroll
        for (int mt = 0; mt < 4; mt++) {
            f32x4 s = own[mt];
#pragma unroll
            for (int o = 1; o < 4; o++) {
                const int ow = (w + o) & 3;
                s += *(const f32x4*)(smem + MID_RING + ow * 16384 + (w * 4 + mt) * 1024 + lane * 16);
            }
            r[mt] = s;
        }
        const int mq = lane >> 4, n = lane & 15;
#pragma unroll
        for (int mt = 0; mt < 4; mt++) {
            const int f = ft * 64 + mt * 16 + mq * 4;
            const u32x2 bb = *(const u32x2*)(b1 + f);
            float v[4];
            {
                u32 p0 = pk2bf(r[mt][0], r[mt][1]), p1 = pk2bf(r[mt][2], r[mt][3]);
                u32 s0 = pk2bf(lo_f(p0) + lo_f(bb[0]), hi_f(p0) + hi_f(bb[0]));
                u32 s1 = pk2bf(lo_f(p1) + lo_f(bb[1]), hi_f(p1) + hi_f(bb[1]));
                v[0] = lo_f(s0); v[1] = hi_f(s0); v[2] = lo_f(s1); v[3] = hi_f(s1);
            }
#pragma unroll
            for (int i = 0; i < 4; i++) {
                const float x = v[i];
                const float z = 0.7978845608028654f * (x + 0.044715f * x * x * x);
                const float e = __expf(2.f * z);
                const float th = 1.f - 2.f / (e + 1.f);
                v[i] = 0.5f * x * (1.f + th);
            }
            u32x2 o = {pk2bf(v[0], v[1]), pk2bf(v[2], v[3])};
            const int ks = ft * 2 + (mt >> 1), kq = 2 * (mt & 1) + (mq >> 1);
            char* dst = (char*)UF + ((size_t)((dt * 56 + ks) * 4 + w) * 64 + kq * 16 + n) * 16 + (mq & 1) * 8;
            *(u32x2*)dst = o;
        }
        return;
    }

    const int wv = w - 4, ch = wv >> 1, hf = wv & 1;
    const int d = blockIdx.x * 2 + ch;
    const unsigned sb_off = MID_SSM + ch * SSM_SIZE;
    char* sb = smem + sb_off;
    unsigned* fl = flags + 4 + ch * 4;
    {
        const char* src = (const char*)(uT + (size_t)d * L) + hf * 2048;
        dma16(sb_off + SSM_U + 512 + hf * 2048, lane * 16, src);
        dma16(sb_off + SSM_U + 512 + hf * 2048 + 1024, lane * 16, src + 1024);
    }
    if (hf == 0 && lane < 32) *(u32x4*)(sb + SSM_U + lane * 16) = u32x4{0, 0, 0, 0};
    u32x4 kix[2][2];
#pragma unroll
    for (int i = 0; i < 2; i++) {
        const u32x4* pt = (const u32x4*)(KIDX.v + (hf * 128 + i * 64 + lane) * 16);
        kix[i][0] = pt[0]; kix[i][1] = pt[1];
    }
    const int n = hf * 64 + lane;
    const float dt = rbf(__expf(bf2f(logdt[d])));
    const float zr = dt * bf2f(Are[d * NS + n]), zi = dt * bf2f(Aim[d * NS + n]);
    f32x2 W;
    {
        const f32x2 B = {dt * bf2f(Bre[d * NS + n]), dt * bf2f(Bim[d * NS + n])};
        const f32x2 C = {bf2f(Cre[d * NS + n]), bf2f(Cim[d * NS + n])};
        W = cmul(C, B);
    }
    const float dsk = bf2f(Dskip[d]);
    char* tbl = sb + SSM_COPY + hf * TBL_SIZE;
    char* tw = tbl + lane * 8;
    const char* trd = tbl + (lane & 15) * TBL_PITCH + (lane >> 4) * 16;
    u32x4 Bh[8], Bl[8];
    {
        const f32x2 E8 = cexp_scaled(zr, zi, 8.f);
        f32x2 pw[16];
        pw[0] = f32x2{1.f, 0.f};
        pw[1] = E8;
#pragma unroll
        for (int b = 2; b < 16; b++) pw[b] = (b & 1) ? cmul(pw[b - 1], E8) : cmul(pw[b / 2], pw[b / 2]);
#pragma unroll
        for (int b = 0; b < 16; b++) *(u32x2*)(tw + b * TBL_PITCH) = split_bf(pw[b]);
        wait_lgkm0();
#pragma unroll
        for (int T = 0; T < 8; T++) {
            u32x4 v = *(const u32x4*)(trd + T * 64);
            Bh[T] = u32x4{v[0], v[0], v[2], v[2]};
            Bl[T] = u32x4{v[1], v[1], v[3], v[3]};
        }
        wait_lgkm0();
    }
    f32x4 kacc[3];
    {
        const f32x2 E1 = cexp_scaled(zr, zi, 1.f);
        const f32x2 E128 = cexp_scaled(zr, zi, 128.f);
        f32x2 WY[8];
        {
            f32x2 Y[8];
            Y[0] = f32x2{1.f, 0.f}; Y[1] = E1;
#pragma unroll
            for (int c = 2; c < 8; c++) Y[c] = (c & 1) ? cmul(Y[c - 1], E1) : cmul(Y[c / 2], Y[c / 2]);
            WY[0] = W;
#pragma unroll
            for (int c = 1; c < 8; c++) WY[c] = cmul(W, Y[c]);
        }
        f32x2 Xp[17];
        Xp[0] = f32x2{1.f, 0.f}; Xp[1] = E128;
#pragma unroll
        for (int a = 2; a < 17; a++) Xp[a] = (a & 1) ? cmul(Xp[a - 1], E128) : cmul(Xp[a / 2], Xp[a / 2]);
#pragma unroll
        for (int tile = 0; tile < 3; tile++) {
#pragma unroll
            for (int r = 0; r < 16; r++) {
                f32x2 v;
                bool valid = true;
                if (tile == 0) { v = (r < 8) ? WY[r] : cmul(WY[r - 8], Xp[1]); }
                else if (tile == 1) {
                    if (r < 8) v = cmul(WY[2 * (r & 3)], Xp[2 + (r >> 2)]);
                    else v = cmul(WY[4 * (r & 1)], Xp[4 + ((r - 8) >> 1)]);
                } else {
                    if (r < 8) v = cmul(W, Xp[8 + r]);
                    else if (r == 8) v = cmul(W, Xp[16]);
                    else valid = false;
                }
                if (valid) {
                    v.y = -v.y;
                    *(u32x2*)(tw + r * TBL_PITCH) = split_bf(v);
                }
            }
            wait_lgkm0();
            f32x4 a4 = {0.f, 0.f, 0.f, 0.f};
#pragma unroll
            for (int T = 0; T < 8; T++) {
                u32x4 av = *(const u32x4*)(trd + T * 64);
                a4 = mfma16(av, Bh[T], a4);
                a4 = mfma16(av, Bl[T], a4);
            }
            kacc[tile] = a4;
            wait_lgkm0();
        }
    }
    {
        float* P = (float*)(sb + SSM_P + hf * 3088);
        const int mq = lane >> 4, col = lane & 15;
#pragma unroll
        for (int tile = 0; tile < 3; tile++)
#pragma unroll
            for (int r = 0; r < 4; r++) P[(tile * 16 + mq * 4 + r) * 16 + col] = kacc[tile][r];
        if (lane == 0) P[768] = 0.f;
    }
    lds_arrive(&fl[0], lane);
    lds_wait(&fl[0], 2);
    {
        const char* P0 = sb + SSM_P;
        const char* P1 = sb + SSM_P + 3088;
#pragma unroll
        for (int i = 0; i < 2; i++) {
            const int ub = hf * 128 + i * 64 + lane;
            float v[12];
#pragma unroll
            for (int j = 0; j < 11; j++) {
                const u32 word = kix[i][j >> 3][(j >> 1) & 3];
                const unsigned off = (j & 1) ? (word >> 16) : (word & 0xffffu);
                v[j] = *(const float*)(P0 + off) + *(const float*)(P1 + off);
            }
            v[11] = 0.f;
#pragma unroll
            for (int c = 0; c < 4; c++) {
                u32x4 o;
#pragma unroll
                for (int e = 0; e < 4; e++) o[e] = pk2bf(v[c + 2 * e], v[c + 2 * e + 1]);
                *(u32x4*)(sb + SSM_COPY + c * CP_STRIDE + ub * 16) = o;
            }
        }
        if (hf == 0 && lane < 8) {
            *(u32x4*)(sb + SSM_COPY + (lane >> 1) * CP_STRIDE + (256 + (lane & 1)) * 16) = u32x4{0, 0, 0, 0};
        }
    }
    wait_vm0<0>();
    lds_arrive(&fl[1], lane);
    lds_wait(&fl[1], 2);
    f32x4 cacc[8];
#pragma unroll
    for (int i = 0; i < 8; i++) cacc[i] = f32x4{0.f, 0.f, 0.f, 0.f};
    {
        const int m = lane & 15, q = lane >> 4;
        const int gq = m >> 2, e = m & 3, c = 3 - e;
        const char* abase = sb + SSM_COPY + c * CP_STRIDE + 4104 - 8 * gq - 16 * q - 64 * 63;
        const char* bbase = sb + SSM_U + 528 + 32 * m - 16 * q - 64 * 7;
#pragma unroll
        for (int rr = 0; rr < 4; rr++) {
            const int r = hf * 4 + rr;
            const char* ab = abase + 64 * (7 - r);
            const char* bb = bbase + 64 * (7 - r);
            u32x4 A[8], B[8];
#pragma unroll
            for (int pp = 0; pp < 8; pp++) {
                const u32x2 a0 = *(const u32x2*)(ab + 64 * (56 - 8 * pp));
                const u32x2 a1 = *(const u32x2*)(ab + 64 * (56 - 8 * pp) + 8);
                A[pp] = u32x4{a0[0], a0[1], a1[0], a1[1]};
                B[pp] = *(const u32x4*)(bb + 512 * pp);
            }
#pragma unroll
            for (int i = 0; i < 8; i++)
#pragma unroll
                for (int pp = 0; pp <= i; pp++) cacc[i] = mfma16(A[pp], B[i - pp], cacc[i]);
        }
    }
    {
        char* xo = sb + SSM_X + hf * 4096;
        if (hf) {
#pragma unroll
            for (int i = 0; i < 4; i++) *(f32x4*)(xo + i * 1024 + lane * 16) = cacc[i];
        } else {
#pragma unroll
            for (int i = 0; i < 4; i++) *(f32x4*)(xo + i * 1024 + lane * 16) = cacc[4 + i];
        }
    }
    f32x4 keep[4];
    if (hf) {
#pragma unroll
        for (int i = 0; i < 4; i++) { keep[i] = cacc[4 + i]; asm volatile("" : "+v"(keep[i])); }
    } else {
#pragma unroll
        for (int i = 0; i < 4; i++) { keep[i] = cacc[i]; asm volatile("" : "+v"(keep[i])); }
    }
    lds_arrive(&fl[2], lane);
    lds_wait(&fl[2], 2);
    {
        const char* xi = sb + SSM_X + (hf ^ 1) * 4096;
        const int nn = lane & 15, mq = lane >> 4;
#pragma unroll
        for (int i = 0; i < 4; i++) {
            f32x4 v = keep[i];
            v += *(const f32x4*)(xi + i * 1024 + lane * 16);
            const int t = 256 * (hf * 4 + i) + 16 * nn + 4 * mq;
            const u32x2 uu = *(const u32x2*)(sb + SSM_U + 512 + t * 2);
            u32 y0 = pk2bf(v[0], v[1]), y1 = pk2bf(v[2], v[3]);
            u32 s0 = pk2bf(lo_f(uu[0]) * dsk, hi_f(uu[0]) * dsk), s1 = pk2bf(lo_f(uu[1]) * dsk, hi_f(uu[1]) * dsk);
            u32x2 o = {pk2bf(lo_f(y0) + lo_f(s0), hi_f(y0) + hi_f(s0)), pk2bf(lo_f(y1) + lo_f(s1), hi_f(y1) + hi_f(s1))};
            *(u32x2*)(aT + (size_t)d * L + t) = o;
        }
    }
}

constexpr int OUT_RING = 0;
constexpr int OUT_PPITCH = 272;
constexpr int OUT_A = 4 * 64 * OUT_PPITCH;
constexpr int OUT_LDS = OUT_A + 8192;

NOPK __global__ __launch_bounds__(256) void k_out(const u16* __restrict__ UF, const u16* __restrict__ w2, const u16* __restrict__ b2,
                                             const u16* __restrict__ aT, const float* __restrict__ g, u16* __restrict__ out) {
    extern __shared__ __attribute__((aligned(16))) char smem[];
    const int tid = threadIdx.x, lane = tid & 63;
    const int w = __builtin_amdgcn_readfirstlane(tid >> 6);
    const int xcd = blockIdx.x & 7, li = blockIdx.x >> 3;
    const int st = (xcd >> 1) * 8 + (li & 7), dt = (xcd & 1) * 4 + (li >> 3);
#pragma unroll
    for (int i = 0; i < 2; i++) {
        const int row = 16 * w + 8 * i + (lane >> 3);
        const int sw = (((lane >> 4) & 1) << 1) | ((i & 1) << 2);
        const unsigned voff = (unsigned)(row * (L * 2) + (((lane & 7) ^ sw) << 4));
        dma16(OUT_A + (16 * w + 8 * i) * 128, voff, (const char*)aT + ((size_t)dt * 64 * L + st * 64) * 2);
    }
    f32x4 acc[4][4];
#pragma unroll
    for (int i = 0; i < 4; i++)
#pragma unroll
        for (int j = 0; j < 4; j++) acc[i][j] = f32x4{0.f, 0.f, 0.f, 0.f};
    const char* wbase = (const char*)w2 + ((size_t)(w * 448) * L + st * 64) * 2;
    const char* xbase = (const char*)UF + ((size_t)(dt * 56 + w * 14)) * 4096;
    gemm_wave<14, L * 2, false>(smem, wbase, xbase, OUT_RING + w * 16384, lane, acc);
    __syncthreads();
    {
        const int n = lane & 15, mq = lane >> 4;
        char* P = smem + w * 64 * OUT_PPITCH;
#pragma unroll
        for (int mt = 0; mt < 4; mt++)
#pragma unroll
            for (int nt = 0; nt < 4; nt++) *(f32x4*)(P + (nt * 16 + n) * OUT_PPITCH + (mt * 16 + mq * 4) * 4) = acc[mt][nt];
    }
    __syncthreads();
    {
        const int i = lane & 15, gq = lane >> 4, q = i >> 2, p = lane & 3;
        const int s = st * 64 + w * 16 + i;
        const float gg = g[s];
        const float og = rbf(1.f - gg);
        const float bias = bf2f(b2[s]);
#pragma unroll
        for (int it = 0; it < 2; it++) {
            const int c8 = it * 4 + gq;
            f32x4 s0 = {0.f, 0.f, 0.f, 0.f}, s1 = s0;
#pragma unroll
            for (int o = 0; o < 4; o++) {
                const char* P = smem + o * 64 * OUT_PPITCH + (w * 16 + i) * OUT_PPITCH + c8 * 32;
                s0 += *(const f32x4*)P;
                s1 += *(const f32x4*)(P + 16);
            }
            u32x2 a0, a1;
            {
                const int r0 = 8 * c8 + q, r1 = r0 + 4;
                const int f0 = ((r0 >> 1) & 1) | (((r0 >> 3) & 1) << 1);
                const int f1 = ((r1 >> 1) & 1) | (((r1 >> 3) & 1) << 1);
                a0 = tr_read(smem, OUT_A + r0 * 128 + ((w ^ f0) << 5) + p * 8);
                a1 = tr_read(smem, OUT_A + r1 * 128 + ((w ^ f1) << 5) + p * 8);
            }
            float b[8] = {s0[0], s0[1], s0[2], s0[3], s1[0], s1[1], s1[2], s1[3]};
            u32 av[4] = {a0[0], a0[1], a1[0], a1[1]};
            u32x4 o;
#pragma unroll
            for (int e = 0; e < 4; e++) {
                u32 y = pk2bf(b[2 * e], b[2 * e + 1]);
                y = pk2bf(lo_f(y) + bias, hi_f(y) + bias);
                u32 t1 = pk2bf(gg * lo_f(av[e]), gg * hi_f(av[e]));
                u32 t2 = pk2bf(og * lo_f(y), og * hi_f(y));
                o[e] = pk2bf(lo_f(t1) + lo_f(t2), hi_f(t1) + hi_f(t2));
            }
            *(u32x4*)(out + (size_t)s * D + dt * 64 + c8 * 8) = o;
        }
    }
}

extern "C" void kernel_launch(void* const* d_in, const int* in_sizes, int n_in,
                              void* d_out, int out_size, void* d_ws, size_t ws_size,
                              hipStream_t stream) {
    const u16* u = (const u16*)d_in[0];
    const u16* Are = (const u16*)d_in[1];
    const u16* Aim = (const u16*)d_in[2];
    const u16* Bre = (const u16*)d_in[3];
    const u16* Bim = (const u16*)d_in[4];
    const u16* Cre = (const u16*)d_in[5];
    const u16* Cim = (const u16*)d_in[6];
    const u16* logdt = (const u16*)d_in[7];
    const u16* Dskip = (const u16*)d_in[8];
    const u16* w1 = (const u16*)d_in[10];
    const u16* b1 = (const u16*)d_in[11];
    const u16* w2 = (const u16*)d_in[12];
    const u16* b2 = (const u16*)d_in[13];
    const u16* gw = (const u16*)d_in[14];
    char* ws = (char*)d_ws;
    u16* hF = (u16*)ws;
    u16* uT = (u16*)(ws + (2 << 20));
    u16* aT = (u16*)(ws + (4 << 20));
    u16* UF = (u16*)(ws + (6 << 20));
    float* g = (float*)(ws + (8 << 20));
    static bool attr_done = false;
    if (!attr_done) {
        hipFuncSetAttribute((const void*)k_prep, hipFuncAttributeMaxDynamicSharedMemorySize, 65536);
        hipFuncSetAttribute((const void*)k_mid, hipFuncAttributeMaxDynamicSharedMemorySize, MID_LDS);
        hipFuncSetAttribute((const void*)k_out, hipFuncAttributeMaxDynamicSharedMemorySize, OUT_LDS);
        attr_done = true;
    }
    k_prep<<<64, 512, 65536, stream>>>(u, gw, hF, uT, g);
    k_mid<<<256, 512, MID_LDS, stream>>>(hF, w1, b1, UF, Are, Aim, Bre, Bim, Cre, Cim, logdt, Dskip, uT, aT);
    k_out<<<256, 256, OUT_LDS, stream>>>(UF, w2, b2, aT, g, (u16*)d_out);
}
```

```cpp
#include <hip/hip_runtime.h>
#include <hip/hip_bf16.h>
#include <stdint.h>

typedef unsigned short u16;
typedef unsigned int u32;
using bf16x8 = __attribute__((ext_vector_type(8))) __bf16;
using bf16x2 = __attribute__((ext_vector_type(2))) __bf16;
using s16x4 = __attribute__((ext_vector_type(4))) short;
using f32x4 = __attribute__((ext_vector_type(4))) float;
using f32x2 = __attribute__((ext_vector_type(2))) float;
using u32x4 = __attribute__((ext_vector_type(4))) unsigned;
using u32x2 = __attribute__((ext_vector_type(2))) unsigned;
typedef __attribute__((address_space(3))) s16x4* lds_s16x4_ptr;

#if defined(__HIP_DEVICE_COMPILE__)
#define NOPK __attribute__((target("no-packed-fp32-ops")))
#else
#define NOPK
#endif
#define DEV NOPK __device__ __forceinline__

constexpr int L = 2048, D = 512, NS = 128, F = 1792;

DEV float bf2f(u16 v) { return __builtin_bit_cast(float, ((unsigned)v) << 16); }
DEV u32 pk2bf(float a, float b) {
    f32x2 v = {a, b};
    return __builtin_bit_cast(u32, __builtin_convertvector(v, bf16x2));
}
DEV float rbf(float f) { return __builtin_bit_cast(float, pk2bf(f, 0.f) << 16); }
DEV float lo_f(u32 p) { return __builtin_bit_cast(float, p << 16); }
DEV float hi_f(u32 p) { return __builtin_bit_cast(float, p & 0xffff0000u); }

DEV u32x2 tr_read(const char* lds_generic_base, unsigned off) {
    s16x4 v = __builtin_amdgcn_ds_read_tr16_b64_v4i16((lds_s16x4_ptr)(lds_generic_base + off));
    return __builtin_bit_cast(u32x2, v);
}
DEV f32x4 mfma16(u32x4 a, u32x4 b, f32x4 c) {
    return __builtin_amdgcn_mfma_f32_16x16x32_bf16(__builtin_bit_cast(bf16x8, a), __builtin_bit_cast(bf16x8, b), c, 0, 0, 0);
}

DEV void dma16(unsigned lds, unsigned voff, const void* sbase) {
    unsigned keep;
    asm volatile("s_mov_b32 %0, m0\n\ts_mov_b32 m0, %1\n\ts_nop 0\n\tglobal_load_lds_dwordx4 %2, %3\n\ts_mov_b32 m0, %0"
                 : "=&s"(keep) : "s"(lds), "v"(voff), "s"(sbase) : "memory");
}
template <int IMM>
DEV void gload16(u32x4& dst, unsigned voff, const void* sbase) {
    asm volatile("global_load_dwordx4 %0, %1, %2 offset:%3" : "=v"(dst) : "v"(voff), "s"(sbase), "i"(IMM) : "memory");
}
template <int N>
DEV void wait_vm(u32x4& a, u32x4& b, u32x4& c, u32x4& d) {
    asm volatile("s_waitcnt vmcnt(%4)" : "+v"(a), "+v"(b), "+v"(c), "+v"(d) : "i"(N) : "memory");
}
template <int N>
DEV void wait_vm0() { asm volatile("s_waitcnt vmcnt(%0)" ::"i"(N) : "memory"); }
DEV void wait_lgkm0() { asm volatile("s_waitcnt lgkmcnt(0)" ::: "memory"); }

template <int CTRL>
DEV float dpp_add(float v) {
    int t = __builtin_amdgcn_update_dpp(0, __builtin_bit_cast(int, v), CTRL, 0xf, 0xf, true);
    return v + __builtin_bit_cast(float, t);
}
DEV float rdlane(float v, int l) { return __builtin_bit_cast(float, __builtin_amdgcn_readlane(__builtin_bit_cast(int, v), l)); }
DEV float wave_sum(float v) {
    v = dpp_add<0xB1>(v);
    v = dpp_add<0x4E>(v);
    v = dpp_add<0x141>(v);
    v = dpp_add<0x140>(v);
    return (rdlane(v, 0) + rdlane(v, 16)) + (rdlane(v, 32) + rdlane(v, 48));
}
DEV float fast_exp(float x) { return __builtin_amdgcn_exp2f(x * 1.4426950408889634f); }
DEV void wg_barrier() {
    __builtin_amdgcn_fence(__ATOMIC_RELEASE, "workgroup");
    __builtin_amdgcn_s_barrier();
    __builtin_amdgcn_fence(__ATOMIC_ACQUIRE, "workgroup");
}
#define STAMP(kern, idx) do {} while (0)
#define TIDX ((int)__builtin_amdgcn_workitem_id_x())
#define BIDX ((int)__builtin_amdgcn_workgroup_id_x())

NOPK __global__ __launch_bounds__(512) void k_prep(const u16* __restrict__ u, const u16* __restrict__ gw,
                                              u16* __restrict__ hF, u16* __restrict__ uT, float* __restrict__ g) {
    extern __shared__ __attribute__((aligned(16))) char smem[];
    char* Hs = smem;
    char* Us = smem + 32768;
    const int tid = TIDX, lane = tid & 63, w = tid >> 6;
    const int s0 = BIDX * 32;
    STAMP(0, 0);
    u32x4 xv[4];
#pragma unroll
    for (int i = 0; i < 4; i++) xv[i] = *(const u32x4*)(u + (size_t)(s0 + 4 * w + i) * D + lane * 8);
    u32x4 gv = *(const u32x4*)(gw + lane * 8);
#pragma unroll
    for (int i = 0; i < 4; i++) {
        const int r = 4 * w + i;
        float x[8];
#pragma unroll
        for (int j = 0; j < 4; j++) { x[2 * j] = lo_f(xv[i][j]); x[2 * j + 1] = hi_f(xv[i][j]); }
        float sum = 0.f, gs = 0.f;
#pragma unroll
        for (int j = 0; j < 4; j++) {
            sum += x[2 * j] + x[2 * j + 1];
            u32 p = pk2bf(x[2 * j] * lo_f(gv[j]), x[2 * j + 1] * hi_f(gv[j]));
            gs += lo_f(p) + hi_f(p);
        }
        sum = wave_sum(sum);
        gs = wave_sum(gs);
        const float mean = sum * (1.f / D);
        float sq = 0.f;
#pragma unroll
        for (int j = 0; j < 8; j++) { float dl = x[j] - mean; sq += dl * dl; }
        sq = wave_sum(sq);
        const float var = rbf(sq * (1.f / D));
        const float m = rbf(mean);
        const float sd = rbf(__builtin_sqrtf(rbf(var + 1e-5f)));
        u32x4 hv;
#pragma unroll
        for (int j = 0; j < 4; j++) {
            u32 c = pk2bf(x[2 * j] - m, x[2 * j + 1] - m);
            hv[j] = pk2bf(lo_f(c) / sd, hi_f(c) / sd);
        }
        const int sw = (r & 3) | (((r >> 3) & 1) << 2);
        const int c16 = lane ^ (sw << 1);
        *(u32x4*)(Hs + r * 1024 + c16 * 16) = hv;
        *(u32x4*)(Us + r * 1024 + c16 * 16) = xv[i];
        if (lane == 0) {
            float gb = rbf(gs);
            g[s0 + r] = rbf(1.f / (1.f + fast_exp(-gb)));
        }
    }
    STAMP(0, 1);
    wg_barrier();
    STAMP(0, 2);
    const int kq = lane >> 4, q = (lane & 15) >> 2, p = lane & 3;
#pragma unroll
    for (int i = 0; i < 4; i++) {
        const int cb = w * 4 + i;
        u32x2 h0, h1, u0, u1;
        {
            const int r0 = 8 * kq + q, r1 = r0 + 4;
            const int sw0 = (r0 & 3) | (((r0 >> 3) & 1) << 2);
            const int sw1 = (r1 & 3) | (((r1 >> 3) & 1) << 2);
            const unsigned o0 = r0 * 1024 + ((cb ^ sw0) << 5) + p * 8;
            const unsigned o1 = r1 * 1024 + ((cb ^ sw1) << 5) + p * 8;
            h0 = tr_read(Hs, o0); h1 = tr_read(Hs, o1);
            u0 = tr_read(Us, o0); u1 = tr_read(Us, o1);
        }
        u32x4 hv = {h0[0], h0[1], h1[0], h1[1]};
        u32x4 uv = {u0[0], u0[1], u1[0], u1[1]};
        const int dt = cb >> 2, nt = cb & 3, ks = BIDX;
        *(u32x4*)(hF + ((size_t)((dt * 64 + ks) * 4 + nt) * 64 + lane) * 8) = hv;
        const int d = cb * 16 + (lane & 15);
        *(u32x4*)(uT + (size_t)d * L + s0 + kq * 8) = uv;
    }
    STAMP(0, 3);
}

constexpr int DW = 4, DX = 4;
template <int S> struct Sched {
    static constexpr int posW(int idx) {
        int cnt = 0;
        for (int tau = -DW - 1; tau < S; tau++) {
            int wi = tau + 1 + DW, xi = tau + DX;
            if (wi >= 0 && wi < S) { cnt += 4; if (wi == idx) return cnt; }
            if (xi >= 0 && xi < S) { cnt += 4; }
        }
        return -1;
    }
    static constexpr int posX(int idx) {
        int cnt = 0;
        for (int tau = -DW - 1; tau < S; tau++) {
            int wi = tau + 1 + DW, xi = tau + DX;
            if (wi >= 0 && wi < S) { cnt += 4; }
            if (xi >= 0 && xi < S) { cnt += 4; if (xi == idx) return cnt; }
        }
        return -1;
    }
    static constexpr int issued(int tau_last) {
        int cnt = 0;
        for (int tau = -DW - 1; tau <= tau_last; tau++) {
            int wi = tau + 1 + DW, xi = tau + DX;
            if (wi >= 0 && wi < S) cnt += 4;
            if (xi >= 0 && xi < S) cnt += 4;
        }
        return cnt;
    }
    static constexpr int waitN(int t) {
        int need = 0;
        if (t + 1 < S) need = posW(t + 1);
        if (t >= 0 && posX(t) > need) need = posX(t);
        return issued(t - 1) - need;
    }
};

struct GemmState {
    const char* wbase;
    const char* xbase;
    unsigned wvoff[4];
    unsigned xvoff;
    unsigned ring;
    unsigned raddr[4];
};

template <int S, int WPITCH, bool W_IS_A>
struct GemmCtx {
    const char* smem;
    const char* wbase;
    const char* xbase;
    unsigned ring;
    unsigned wvoff[4];
    unsigned xvoff;
    unsigned raddr[4];
    u32x4 X[DX][4];
    u32x4 Wf[2][4];
    f32x4 acc[4][4];
};
template <int IDX, int S, int WPITCH, bool W_IS_A>
DEV void issue_w(GemmCtx<S, WPITCH, W_IS_A>& c) {
    if constexpr (IDX >= 0 && IDX < S) {
        const char* b = c.wbase + (size_t)IDX * 32 * WPITCH;
        const unsigned slot = c.ring + (IDX % DW) * 4096;
#pragma unroll
        for (int i = 0; i < 4; i++) dma16(slot + i * 1024, c.wvoff[i], b);
    }
}
template <int IDX, int S, int WPITCH, bool W_IS_A>
DEV void issue_x(GemmCtx<S, WPITCH, W_IS_A>& c) {
    if constexpr (IDX >= 0 && IDX < S) {
        const char* b = c.xbase + (size_t)IDX * 4096;
        gload16<0>(c.X[IDX % DX][0], c.xvoff, b);
        gload16<1024>(c.X[IDX % DX][1], c.xvoff, b);
        gload16<2048>(c.X[IDX % DX][2], c.xvoff, b);
        gload16<3072>(c.X[IDX % DX][3], c.xvoff, b);
    }
}
template <int IDX, int S, int WPITCH, bool W_IS_A>
DEV void read_w(GemmCtx<S, WPITCH, W_IS_A>& c) {
    if constexpr (IDX >= 0 && IDX < S) {
        constexpr unsigned so = (IDX % DW) * 4096;
#pragma unroll
        for (int i = 0; i < 4; i++) {
            u32x2 a = tr_read(c.smem, c.raddr[i] + so);
            u32x2 b = tr_read(c.smem, c.raddr[i] + so + 512);
            c.Wf[IDX & 1][i] = u32x4{a[0], a[1], b[0], b[1]};
        }
    }
}
template <int T, int S, int WPITCH, bool W_IS_A>
DEV void gemm_steps(GemmCtx<S, WPITCH, W_IS_A>& c) {
    if constexpr (T < S) {
        if constexpr (T < -1) {
            issue_w<T + 1 + DW>(c);
            issue_x<T + DX>(c);
        } else if constexpr (T == -1) {
            wait_vm0<Sched<S>::waitN(-1)>();
            read_w<0>(c);
            wait_lgkm0();
            issue_w<DW>(c);
            issue_x<DX - 1>(c);
        } else {
            u32x4(&x)[4] = c.X[T % DX];
            wait_vm<Sched<S>::waitN(T)>(x[0], x[1], x[2], x[3]);
            read_w<T + 1>(c);
#pragma unroll
            for (int i = 0; i < 4; i++)
#pragma unroll
                for (int j = 0; j < 4; j++) {
                    if constexpr (W_IS_A) c.acc[i][j] = mfma16(c.Wf[T & 1][i], x[j], c.acc[i][j]);
                    else c.acc[i][j] = mfma16(x[i], c.Wf[T & 1][j], c.acc[i][j]);
                }
            wait_lgkm0();
            issue_w<T + 1 + DW>(c);
            issue_x<T + DX>(c);
        }
        gemm_steps<T + 1>(c);
    }
}

template <int S, int WPITCH, bool W_IS_A>
DEV void gemm_wave(const char* smem, const char* wbase, const char* xbase, unsigned ring, int lane, f32x4 (&acc)[4][4]) {
    GemmCtx<S, WPITCH, W_IS_A> c;
    c.smem = smem; c.wbase = wbase; c.xbase = xbase; c.ring = ring;
#pragma unroll
    for (int i = 0; i < 4; i++) {
        const int sw = (((lane >> 4) & 1) << 1) | ((i & 1) << 2);
        c.wvoff[i] = (unsigned)((8 * i + (lane >> 3)) * WPITCH + (((lane & 7) ^ sw) << 4));
    }
    c.xvoff = lane * 16;
    const int g = lane >> 4, q = (lane & 15) >> 2, p = lane & 3;
    const int fl = ((q >> 1) & 1) | ((g & 1) << 1);
#pragma unroll
    for (int i = 0; i < 4; i++) c.raddr[i] = ring + (8 * g + q) * 128 + ((i ^ fl) << 5) + p * 8;
#pragma unroll
    for (int i = 0; i < 4; i++)
#pragma unroll
        for (int j = 0; j < 4; j++) c.acc[i][j] = f32x4{0.f, 0.f, 0.f, 0.f};
    gemm_steps<-DW - 1>(c);
#pragma unroll
    for (int i = 0; i < 4; i++)
#pragma unroll
        for (int j = 0; j < 4; j++) acc[i][j] = c.acc[i][j];
}

DEV void lds_arrive(unsigned* cnt, int lane) {
    wait_lgkm0();
    if (lane == 0) __hip_atomic_fetch_add(cnt, 1u, __ATOMIC_RELAXED, __HIP_MEMORY_SCOPE_WORKGROUP);
}
DEV void lds_wait(unsigned* cnt, unsigned target) {
    while (__hip_atomic_load(cnt, __ATOMIC_RELAXED, __HIP_MEMORY_SCOPE_WORKGROUP) < target) __builtin_amdgcn_s_sleep(1);
    asm volatile("" ::: "memory");
}

struct KIdxTable { u16 v[256 * 16]; };
constexpr int round_bf16_int(int x) {
    if (x < 256) return x;
    int sh = 0;
    while ((x >> sh) >= 256) sh++;
    int q = x >> sh, r = x & ((1 << sh) - 1), half = 1 << (sh - 1);
    if (r > half || (r == half && (q & 1))) q++;
    return q << sh;
}
constexpr int compact_pos(int tap) {
    if (tap < 0) return 768;
    int lr = round_bf16_int(tap);
    int row = 0, col = 0;
    if (lr < 256) { row = (lr & 7) + 8 * (lr >> 7); col = (lr >> 3) & 15; }
    else if (lr < 512) { int x = lr - 256; row = 16 + ((x & 7) >> 1) + 4 * (x >> 7); col = (x >> 3) & 15; }
    else if (lr < 1024) { int x = lr - 512; row = 24 + ((x & 7) >> 2) + 2 * (x >> 7); col = (x >> 3) & 15; }
    else if (lr < 2048) { int x = lr - 1024; row = 32 + (x >> 7); col = (x >> 3) & 15; }
    else { row = 40; col = 0; }
    return row * 16 + col;
}
constexpr KIdxTable make_kidx() {
    KIdxTable t{};
    for (int ub = 0; ub < 256; ub++)
        for (int i = 0; i < 16; i++) t.v[ub * 16 + i] = (u16)(4 * compact_pos(2047 - (8 * ub + i)));
    return t;
}
__device__ const KIdxTable KIDX = make_kidx();

DEV f32x2 cmul(f32x2 a, f32x2 b) {
    f32x2 r = a.xx * b;
    f32x2 t = {-a.y, a.y};
    return __builtin_elementwise_fma(t, (f32x2)b.yx, r);
}
DEV f32x2 cexp_scaled(float zr, float zi, float l) {
    const float xr = zr * l, xi = zi * l;
    const float e = __builtin_amdgcn_exp2f(xr * 1.4426950408889634f);
    const float c_hi = 0.15915494309189535f;
    const float c_lo = 6.4206382e-09f;
    float rh = xi * c_hi;
    float rl = __builtin_fmaf(xi, c_hi, -rh) + xi * c_lo;
    float fr = rh - __builtin_rintf(rh);
    fr += rl;
    return f32x2{e * __builtin_amdgcn_cosf(fr), e * __builtin_amdgcn_sinf(fr)};
}
DEV u32x2 split_bf(f32x2 v) {
    u32 h = pk2bf(v.x, v.y);
    u32 l = pk2bf(v.x - lo_f(h), v.y - hi_f(h));
    return u32x2{h, l};
}

constexpr int MID_RING = 0;
constexpr int MID_SSM = 65536;
constexpr int CP_STRIDE = 4160;
constexpr int SSM_COPY = 0;
constexpr int TBL_PITCH = 528;
constexpr int TBL_SIZE = 16 * TBL_PITCH;
constexpr int SSM_U = 16896;
constexpr int SSM_P = SSM_U + 4608;
constexpr int SSM_X = SSM_P + 2 * 3088;
constexpr int SSM_SIZE = SSM_X + 8192;
constexpr int MID_FLAGS = MID_SSM + 2 * SSM_SIZE;
constexpr int MID_LDS = MID_FLAGS + 64;

NOPK __global__ __launch_bounds__(512) void k_mid(const u16* __restrict__ hF, const u16* __restrict__ w1, const u16* __restrict__ b1,
                                             u16* __restrict__ UF,
                                             const u16* __restrict__ Are, const u16* __restrict__ Aim,
                                             const u16* __restrict__ Bre, const u16* __restrict__ Bim,
                                             const u16* __restrict__ Cre, const u16* __restrict__ Cim,
                                             const u16* __restrict__ logdt, const u16* __restrict__ Dskip,
                                             const u16* __restrict__ uT, u16* __restrict__ aT) {
    extern __shared__ __attribute__((aligned(16))) char smem[];
    const int tid = TIDX, lane = tid & 63;
    const int w = __builtin_amdgcn_readfirstlane(tid >> 6);
    unsigned* flags = (unsigned*)(smem + MID_FLAGS);
    STAMP(1, 0);
    if (tid < 16) flags[tid] = 0;
    wait_lgkm0();
    __builtin_amdgcn_s_barrier();

    if (w < 4) {
        const int xcd = BIDX & 7, li = BIDX >> 3;
        if (li >= 28) return;
        const int ft = (xcd >> 1) * 7 + li % 7, dt = (xcd & 1) * 4 + li / 7;
        f32x4 acc[4][4];
#pragma unroll
        for (int i = 0; i < 4; i++)
#pragma unroll
            for (int j = 0; j < 4; j++) acc[i][j] = f32x4{0.f, 0.f, 0.f, 0.f};
        const char* wbase = (const char*)w1 + ((size_t)(w * 512) * F + ft * 64) * 2;
        const char* xbase = (const char*)hF + ((size_t)(dt * 64 + w * 16)) * 4096;
        gemm_wave<16, F * 2, true>(smem, wbase, xbase, MID_RING + w * 16384, lane, acc);
        STAMP(1, 1);
        wait_lgkm0();
        {
            char* mine = smem + MID_RING + w * 16384;
#pragma unroll
            for (int nt = 0; nt < 4; nt++)
#pragma unroll
                for (int mt = 0; mt < 4; mt++)
                    if (nt != w) *(f32x4*)(mine + (nt * 4 + mt) * 1024 + lane * 16) = acc[mt][nt];
        }
        f32x4 own[4];
#define OWN(k) if (w == k) { _Pragma("unroll") for (int mt = 0; mt < 4; mt++) { own[mt] = acc[mt][k]; asm volatile("" : "+v"(own[mt])); } }
        OWN(0) OWN(1) OWN(2) OWN(3)
#undef OWN
        lds_arrive(&flags[0], lane);
        lds_wait(&flags[0], 4);
        f32x4 r[4];
#pragma unroll
        for (int mt = 0; mt < 4; mt++) {
            f32x4 s = own[mt];
#pragma unroll
            for (int o = 1; o < 4; o++) {
                const int ow = (w + o) & 3;
                s += *(const f32x4*)(smem + MID_RING + ow * 16384 + (w * 4 + mt) * 1024 + lane * 16);
            }
            r[mt] = s;
        }
        const int mq = lane >> 4, n = lane & 15;
#pragma unroll
        for (int mt = 0; mt < 4; mt++) {
            const int f = ft * 64 + mt * 16 + mq * 4;
            const u32x2 bb = *(const u32x2*)(b1 + f);
            float v[4];
            {
                u32 p0 = pk2bf(r[mt][0], r[mt][1]), p1 = pk2bf(r[mt][2], r[mt][3]);
                u32 s0 = pk2bf(lo_f(p0) + lo_f(bb[0]), hi_f(p0) + hi_f(bb[0]));
                u32 s1 = pk2bf(lo_f(p1) + lo_f(bb[1]), hi_f(p1) + hi_f(bb[1]));
                v[0] = lo_f(s0); v[1] = hi_f(s0); v[2] = lo_f(s1); v[3] = hi_f(s1);
            }
#pragma unroll
            for (int i = 0; i < 4; i++) {
                const float x = v[i];
                const float z = 0.7978845608028654f * (x + 0.044715f * x * x * x);
                const float e = fast_exp(2.f * z);
                const float th = 1.f - 2.f / (e + 1.f);
                v[i] = 0.5f * x * (1.f + th);
            }
            u32x2 o = {pk2bf(v[0], v[1]), pk2bf(v[2], v[3])};
            const int ks = ft * 2 + (mt >> 1), kq = 2 * (mt & 1) + (mq >> 1);
            char* dst = (char*)UF + ((size_t)((dt * 56 + ks) * 4 + w) * 64 + kq * 16 + n) * 16 + (mq & 1) * 8;
            *(u32x2*)dst = o;
        }
        STAMP(1, 2);
        return;
    }

    const int wv = w - 4, ch = wv >> 1, hf = wv & 1;
    const int d = BIDX * 2 + ch;
    const unsigned sb_off = MID_SSM + ch * SSM_SIZE;
    char* sb = smem + sb_off;
    unsigned* fl = flags + 4 + ch * 4;
    {
        const char* src = (const char*)(uT + (size_t)d * L) + hf * 2048;
        dma16(sb_off + SSM_U + 512 + hf * 2048, lane * 16, src);
        dma16(sb_off + SSM_U + 512 + hf * 2048 + 1024, lane * 16, src + 1024);
    }
    if (hf == 0 && lane < 32) *(u32x4*)(sb + SSM_U + lane * 16) = u32x4{0, 0, 0, 0};
    u32x4 kix[2][2];
#pragma unroll
    for (int i = 0; i < 2; i++) {
        const u32x4* pt = (const u32x4*)(KIDX.v + (hf * 128 + i * 64 + lane) * 16);
        kix[i][0] = pt[0]; kix[i][1] = pt[1];
    }
    const int n = hf * 64 + lane;
    const float dt = rbf(fast_exp(bf2f(logdt[d])));
    const float zr = dt * bf2f(Are[d * NS + n]), zi = dt * bf2f(Aim[d * NS + n]);
    f32x2 W;
    {
        const f32x2 B = {dt * bf2f(Bre[d * NS + n]), dt * bf2f(Bim[d * NS + n])};
        const f32x2 C = {bf2f(Cre[d * NS + n]), bf2f(Cim[d * NS + n])};
        W = cmul(C, B);
    }
    const float dsk = bf2f(Dskip[d]);
    asm volatile("" :: "v"(zr), "v"(zi), "v"(W));
    STAMP(1, 1);
    char* tbl = sb + SSM_COPY + hf * TBL_SIZE;
    char* tw = tbl + lane * 8;
    const char* trd = tbl + (lane & 15) * TBL_PITCH + (lane >> 4) * 16;
    u32x4 Bh[8], Bl[8];
    {
        const f32x2 E8 = cexp_scaled(zr, zi, 8.f);
        f32x2 pw[16];
        pw[0] = f32x2{1.f, 0.f};
        pw[1] = E8;
#pragma unroll
        for (int b = 2; b < 16; b++) pw[b] = (b & 1) ? cmul(pw[b - 1], E8) : cmul(pw[b / 2], pw[b / 2]);
#pragma unroll
        for (int b = 0; b < 16; b++) *(u32x2*)(tw + b * TBL_PITCH) = split_bf(pw[b]);
        wait_lgkm0();
#pragma unroll
        for (int T = 0; T < 8; T++) {
            u32x4 v = *(const u32x4*)(trd + T * 64);
            Bh[T] = u32x4{v[0], v[0], v[2], v[2]};
            Bl[T] = u32x4{v[1], v[1], v[3], v[3]};
        }
        wait_lgkm0();
    }
    f32x4 kacc[3];
    {
        const f32x2 E1 = cexp_scaled(zr, zi, 1.f);
        const f32x2 E128 = cexp_scaled(zr, zi, 128.f);
        f32x2 WY[8];
        {
            f32x2 Y[8];
            Y[0] = f32x2{1.f, 0.f}; Y[1] = E1;
#pragma unroll
            for (int c = 2; c < 8; c++) Y[c] = (c & 1) ? cmul(Y[c - 1], E1) : cmul(Y[c / 2], Y[c / 2]);
            WY[0] = W;
#pragma unroll
            for (int c = 1; c < 8; c++) WY[c] = cmul(W, Y[c]);
        }
        f32x2 Xp[17];
        Xp[0] = f32x2{1.f, 0.f}; Xp[1] = E128;
#pragma unroll
        for (int a = 2; a < 17; a++) Xp[a] = (a & 1) ? cmul(Xp[a - 1], E128) : cmul(Xp[a / 2], Xp[a / 2]);
#pragma unroll
        for (int tile = 0; tile < 3; tile++) {
#pragma unroll
            for (int r = 0; r < 16; r++) {
                f32x2 v;
                bool valid = true;
                if (tile == 0) { v = (r < 8) ? WY[r] : cmul(WY[r - 8], Xp[1]); }
                else if (tile == 1) {
                    if (r < 8) v = cmul(WY[2 * (r & 3)], Xp[2 + (r >> 2)]);
                    else v = cmul(WY[4 * (r & 1)], Xp[4 + ((r - 8) >> 1)]);
                } else {
                    if (r < 8) v = cmul(W, Xp[8 + r]);
                    else if (r == 8) v = cmul(W, Xp[16]);
                    else valid = false;
                }
                if (valid) {
                    v.y = -v.y;
                    *(u32x2*)(tw + r * TBL_PITCH) = split_bf(v);
                }
            }
            wait_lgkm0();
            f32x4 a4 = {0.f, 0.f, 0.f, 0.f};
#pragma unroll
            for (int T = 0; T < 8; T++) {
                u32x4 av = *(const u32x4*)(trd + T * 64);
                a4 = mfma16(av, Bh[T], a4);
                a4 = mfma16(av, Bl[T], a4);
            }
            kacc[tile] = a4;
            wait_lgkm0();
        }
    }
    {
        float* P = (float*)(sb + SSM_P + hf * 3088);
        const int mq = lane >> 4, col = lane & 15;
#pragma unroll
        for (int tile = 0; tile < 3; tile++)
#pragma unroll
            for (int r = 0; r < 4; r++) P[(tile * 16 + mq * 4 + r) * 16 + col] = kacc[tile][r];
        if (lane == 0) P[768] = 0.f;
    }
    STAMP(1, 2);
    lds_arrive(&fl[0], lane);
    lds_wait(&fl[0], 2);
    STAMP(1, 3);
    {
        const char* P0 = sb + SSM_P;
        const char* P1 = sb + SSM_P + 3088;
#pragma unroll
        for (int i = 0; i < 2; i++) {
            const int ub = hf * 128 + i * 64 + lane;
            float v[12];
#pragma unroll
            for (int j = 0; j < 11; j++) {
                const u32 word = kix[i][j >> 3][(j >> 1) & 3];
                const unsigned off = (j & 1) ? (word >> 16) : (word & 0xffffu);
                v[j] = *(const float*)(P0 + off) + *(const float*)(P1 + off);
            }
            v[11] = 0.f;
#pragma unroll
            for (int c = 0; c < 4; c++) {
                u32x4 o;
#pragma unroll
                for (int e = 0; e < 4; e++) o[e] = pk2bf(v[c + 2 * e], v[c + 2 * e + 1]);
                *(u32x4*)(sb + SSM_COPY + c * CP_STRIDE + ub * 16) = o;
            }
        }
        if (hf == 0 && lane < 8) {
            *(u32x4*)(sb + SSM_COPY + (lane >> 1) * CP_STRIDE + (256 + (lane & 1)) * 16) = u32x4{0, 0, 0, 0};
        }
    }
    wait_vm0<0>();
    STAMP(1, 4);
    lds_arrive(&fl[1], lane);
    lds_wait(&fl[1], 2);
    STAMP(1, 5);
    f32x4 cacc[8];
#pragma unroll
    for (int i = 0; i < 8; i++) cacc[i] = f32x4{0.f, 0.f, 0.f, 0.f};
    {
        const int m = lane & 15, q = lane >> 4;
        const int gq = m >> 2, e = m & 3, c = 3 - e;
        const char* abase = sb + SSM_COPY + c * CP_STRIDE + 4104 - 8 * gq - 16 * q - 64 * 63;
        const char* bbase = sb + SSM_U + 528 + 32 * m - 16 * q - 64 * 7;
#pragma unroll
        for (int rr = 0; rr < 4; rr++) {
            const int r = hf * 4 + rr;
            const char* ab = abase + 64 * (7 - r);
            const char* bb = bbase + 64 * (7 - r);
            u32x4 A[8], B[8];
#pragma unroll
            for (int pp = 0; pp < 8; pp++) {
                const u32x2 a0 = *(const u32x2*)(ab + 64 * (56 - 8 * pp));
                const u32x2 a1 = *(const u32x2*)(ab + 64 * (56 - 8 * pp) + 8);
                A[pp] = u32x4{a0[0], a0[1], a1[0], a1[1]};
                B[pp] = *(const u32x4*)(bb + 512 * pp);
            }
#pragma unroll
            for (int i = 0; i < 8; i++)
#pragma unroll
                for (int pp = 0; pp <= i; pp++) cacc[i] = mfma16(A[pp], B[i - pp], cacc[i]);
        }
    }
    STAMP(1, 6);
    {
        char* xo = sb + SSM_X + hf * 4096;
        if (hf) {
#pragma unroll
            for (int i = 0; i < 4; i++) *(f32x4*)(xo + i * 1024 + lane * 16) = cacc[i];
        } else {
#pragma unroll
            for (int i = 0; i < 4; i++) *(f32x4*)(xo + i * 1024 + lane * 16) = cacc[4 + i];
        }
    }
    f32x4 keep[4];
    if (hf) {
#pragma unroll
        for (int i = 0; i < 4; i++) { keep[i] = cacc[4 + i]; asm volatile("" : "+v"(keep[i])); }
    } else {
#pragma unroll
        for (int i = 0; i < 4; i++) { keep[i] = cacc[i]; asm volatile("" : "+v"(keep[i])); }
    }
    lds_arrive(&fl[2], lane);
    lds_wait(&fl[2], 2);
    {
        const char* xi = sb + SSM_X + (hf ^ 1) * 4096;
        const int nn = lane & 15, mq = lane >> 4;
#pragma unroll
        for (int i = 0; i < 4; i++) {
            f32x4 v = keep[i];
            v += *(const f32x4*)(xi + i * 1024 + lane * 16);
            const int t = 256 * (hf * 4 + i) + 16 * nn + 4 * mq;
            const u32x2 uu = *(const u32x2*)(sb + SSM_U + 512 + t * 2);
            u32 y0 = pk2bf(v[0], v[1]), y1 = pk2bf(v[2], v[3]);
            u32 s0 = pk2bf(lo_f(uu[0]) * dsk, hi_f(uu[0]) * dsk), s1 = pk2bf(lo_f(uu[1]) * dsk, hi_f(uu[1]) * dsk);
            u32x2 o = {pk2bf(lo_f(y0) + lo_f(s0), hi_f(y0) + hi_f(s0)), pk2bf(lo_f(y1) + lo_f(s1), hi_f(y1) + hi_f(s1))};
            *(u32x2*)(aT + (size_t)d * L + t) = o;
        }
    }
    STAMP(1, 7);
}

constexpr int OUT_RING = 0;
constexpr int OUT_PPITCH = 272;
constexpr int OUT_A = 4 * 64 * OUT_PPITCH;
constexpr int OUT_LDS = OUT_A + 8192;

NOPK __global__ __launch_bounds__(256) void k_out(const u16* __restrict__ UF, const u16* __restrict__ w2, const u16* __restrict__ b2,
                                             const u16* __restrict__ aT, const float* __restrict__ g, u16* __restrict__ out) {
    extern __shared__ __attribute__((aligned(16))) char smem[];
    const int tid = TIDX, lane = tid & 63;
    const int w = __builtin_amdgcn_readfirstlane(tid >> 6);
    const int xcd = BIDX & 7, li = BIDX >> 3;
    const int st = (xcd >> 1) * 8 + (li & 7), dt = (xcd & 1) * 4 + (li >> 3);
    STAMP(2, 0);
#pragma unroll
    for (int i = 0; i < 2; i++) {
        const int row = 16 * w + 8 * i + (lane >> 3);
        const int sw = (((lane >> 4) & 1) << 1) | ((i & 1) << 2);
        const unsigned voff = (unsigned)(row * (L * 2) + (((lane & 7) ^ sw) << 4));
        dma16(OUT_A + (16 * w + 8 * i) * 128, voff, (const char*)aT + ((size_t)dt * 64 * L + st * 64) * 2);
    }
    f32x4 acc[4][4];
#pragma unroll
    for (int i = 0; i < 4; i++)
#pragma unroll
        for (int j = 0; j < 4; j++) acc[i][j] = f32x4{0.f, 0.f, 0.f, 0.f};
    const char* wbase = (const char*)w2 + ((size_t)(w * 448) * L + st * 64) * 2;
    const char* xbase = (const char*)UF + ((size_t)(dt * 56 + w * 14)) * 4096;
    gemm_wave<14, L * 2, false>(smem, wbase, xbase, OUT_RING + w * 16384, lane, acc);
    STAMP(2, 1);
    wg_barrier();
    {
        const int n = lane & 15, mq = lane >> 4;
        char* P = smem + w * 64 * OUT_PPITCH;
#pragma unroll
        for (int mt = 0; mt < 4; mt++)
#pragma unroll
            for (int nt = 0; nt < 4; nt++) *(f32x4*)(P + (nt * 16 + n) * OUT_PPITCH + (mt * 16 + mq * 4) * 4) = acc[mt][nt];
    }
    wg_barrier();
    {
        const int i = lane & 15, gq = lane >> 4, q = i >> 2, p = lane & 3;
        const int s = st * 64 + w * 16 + i;
        const float gg = g[s];
        const float og = rbf(1.f - gg);
        const float bias = bf2f(b2[s]);
#pragma unroll
        for (int it = 0; it < 2; it++) {
            const int c8 = it * 4 + gq;
            f32x4 s0 = {0.f, 0.f, 0.f, 0.f}, s1 = s0;
#pragma unroll
            for (int o = 0; o < 4; o++) {
                const char* P = smem + o * 64 * OUT_PPITCH + (w * 16 + i) * OUT_PPITCH + c8 * 32;
                s0 += *(const f32x4*)P;
                s1 += *(const f32x4*)(P + 16);
            }
            u32x2 a0, a1;
            {
                const int r0 = 8 * c8 + q, r1 = r0 + 4;
                const int f0 = ((r0 >> 1) & 1) | (((r0 >> 3) & 1) << 1);
                const int f1 = ((r1 >> 1) & 1) | (((r1 >> 3) & 1) << 1);
                a0 = tr_read(smem, OUT_A + r0 * 128 + ((w ^ f0) << 5) + p * 8);
                a1 = tr_read(smem, OUT_A + r1 * 128 + ((w ^ f1) << 5) + p * 8);
            }
            float b[8] = {s0[0], s0[1], s0[2], s0[3], s1[0], s1[1], s1[2], s1[3]};
            u32 av[4] = {a0[0], a0[1], a1[0], a1[1]};
            u32x4 o;
#pragma unroll
            for (int e = 0; e < 4; e++) {
                u32 y = pk2bf(b[2 * e], b[2 * e + 1]);
                y = pk2bf(lo_f(y) + bias, hi_f(y) + bias);
                u32 t1 = pk2bf(gg * lo_f(av[e]), gg * hi_f(av[e]));
                u32 t2 = pk2bf(og * lo_f(y), og * hi_f(y));
                o[e] = pk2bf(lo_f(t1) + lo_f(t2), hi_f(t1) + hi_f(t2));
            }
            *(u32x4*)(out + (size_t)s * D + dt * 64 + c8 * 8) = o;
        }
    }
    STAMP(2, 2);
}

extern "C" void kernel_launch(void* const* d_in, const int* in_sizes, int n_in,
                              void* d_out, int out_size, void* d_ws, size_t ws_size,
                              hipStream_t stream) {
    const u16* u = (const u16*)d_in[0];
    const u16* Are = (const u16*)d_in[1];
    const u16* Aim = (const u16*)d_in[2];
    const u16* Bre = (const u16*)d_in[3];
    const u16* Bim = (const u16*)d_in[4];
    const u16* Cre = (const u16*)d_in[5];
    const u16* Cim = (const u16*)d_in[6];
    const u16* logdt = (const u16*)d_in[7];
    const u16* Dskip = (const u16*)d_in[8];
    const u16* w1 = (const u16*)d_in[10];
    const u16* b1 = (const u16*)d_in[11];
    const u16* w2 = (const u16*)d_in[12];
    const u16* b2 = (const u16*)d_in[13];
    const u16* gw = (const u16*)d_in[14];
    char* ws = (char*)d_ws;
    u16* hF = (u16*)ws;
    u16* uT = (u16*)(ws + (2 << 20));
    u16* aT = (u16*)(ws + (4 << 20));
    u16* UF = (u16*)(ws + (6 << 20));
    float* g = (float*)(ws + (8 << 20));
    static bool attr_done = false;
    if (!attr_done) {
        hipFuncSetAttribute((const void*)k_prep, hipFuncAttributeMaxDynamicSharedMemorySize, 65536);
        hipFuncSetAttribute((const void*)k_mid, hipFuncAttributeMaxDynamicSharedMemorySize, MID_LDS);
        hipFuncSetAttribute((const void*)k_out, hipFuncAttributeMaxDynamicSharedMemorySize, OUT_LDS);
        attr_done = true;
    }
    k_prep<<<64, 512, 65536, stream>>>(u, gw, hF, uT, g);
    k_mid<<<256, 512, MID_LDS, stream>>>(hF, w1, b1, UF, Are, Aim, Bre, Bim, Cre, Cim, logdt, Dskip, uT, aT);
    k_out<<<256, 256, OUT_LDS, stream>>>(UF, w2, b2, aT, g, (u16*)d_out);
}
```

```cpp
#include <hip/hip_runtime.h>
#include <stdint.h>

typedef unsigned short u16;
typedef unsigned int u32;
using bf16x8 = __attribute__((ext_vector_type(8))) __bf16;
using bf16x2 = __attribute__((ext_vector_type(2))) __bf16;
using s16x4 = __attribute__((ext_vector_type(4))) short;
using f32x4 = __attribute__((ext_vector_type(4))) float;
using f32x2 = __attribute__((ext_vector_type(2))) float;
using u32x4 = __attribute__((ext_vector_type(4))) unsigned;
using u32x2 = __attribute__((ext_vector_type(2))) unsigned;
typedef __attribute__((address_space(3))) s16x4* lds_s16x4_ptr;

#if defined(__HIP_DEVICE_COMPILE__)
#define NOPK __attribute__((target("no-packed-fp32-ops")))
#else
#define NOPK
#endif
#define DEV NOPK __device__ __forceinline__
#define TIDX ((int)__builtin_amdgcn_workitem_id_x())
#define BIDX ((int)__builtin_amdgcn_workgroup_id_x())

#define STAMP(kern, idx) do {} while (0)

constexpr int L = 2048, D = 512, NS = 128, F = 1792;

DEV float bf2f(u16 v) { return __builtin_bit_cast(float, ((unsigned)v) << 16); }
DEV u32 pk2bf(float a, float b) {
    f32x2 v = {a, b};
    return __builtin_bit_cast(u32, __builtin_convertvector(v, bf16x2));
}
DEV float lo_f(u32 p) { return __builtin_bit_cast(float, p << 16); }
DEV float hi_f(u32 p) { return __builtin_bit_cast(float, p & 0xffff0000u); }
DEV float rbf(float f) { return lo_f(pk2bf(f, 0.f)); }

DEV u32x2 tr_read(const char* lds_generic_base, unsigned off) {
    s16x4 v = __builtin_amdgcn_ds_read_tr16_b64_v4i16((lds_s16x4_ptr)(lds_generic_base + off));
    return __builtin_bit_cast(u32x2, v);
}
DEV f32x4 mfma16(u32x4 a, u32x4 b, f32x4 c) {
    return __builtin_amdgcn_mfma_f32_16x16x32_bf16(__builtin_bit_cast(bf16x8, a), __builtin_bit_cast(bf16x8, b), c, 0, 0, 0);
}

DEV void dma16(unsigned lds, unsigned voff, const void* sbase) {
    unsigned keep;
    asm volatile("s_mov_b32 %0, m0\n\ts_mov_b32 m0, %1\n\ts_nop 0\n\tglobal_load_lds_dwordx4 %2, %3\n\ts_mov_b32 m0, %0"
                 : "=&s"(keep) : "s"(lds), "v"(voff), "s"(sbase) : "memory");
}
template <int IMM>
DEV void gload16(u32x4& dst, unsigned voff, const void* sbase) {
    asm volatile("global_load_dwordx4 %0, %1, %2 offset:%3" : "=v"(dst) : "v"(voff), "s"(sbase), "i"(IMM) : "memory");
}
DEV void gload_early8(u32x2& dst, const void* p) { asm volatile("global_load_dwordx2 %0, %1, off" : "=v"(dst) : "v"(p) : "memory"); }
DEV void gload_early4(u32& dst, const void* p) { asm volatile("global_load_dword %0, %1, off" : "=v"(dst) : "v"(p) : "memory"); }
DEV void gload_early2(u32& dst, const void* p) { asm volatile("global_load_ushort %0, %1, off" : "=v"(dst) : "v"(p) : "memory"); }
template <typename T> DEV void tie(T& x) { asm volatile("" : "+v"(x) :: "memory"); }
DEV void touch(u32& dummy, unsigned voff, const void* sbase) {
    asm volatile("global_load_dword %0, %1, %2" : "+v"(dummy) : "v"(voff), "s"(sbase) : "memory");
}
DEV void touch_done(u32& dummy) { asm volatile("s_waitcnt vmcnt(0)" : "+v"(dummy) :: "memory"); }
template <int N>
DEV void wait_vm(u32x4& a, u32x4& b, u32x4& c, u32x4& d) {
    asm volatile("s_waitcnt vmcnt(%4)" : "+v"(a), "+v"(b), "+v"(c), "+v"(d) : "i"(N) : "memory");
}
template <int N>
DEV void wait_vm0() { asm volatile("s_waitcnt vmcnt(%0)" ::"i"(N) : "memory"); }
DEV void wait_lgkm0() { asm volatile("s_waitcnt lgkmcnt(0)" ::: "memory"); }

template <int CTRL>
DEV float dpp_add(float v) {
    int t = __builtin_amdgcn_update_dpp(0, __builtin_bit_cast(int, v), CTRL, 0xf, 0xf, true);
    return v + __builtin_bit_cast(float, t);
}
DEV float rdlane(float v, int l) { return __builtin_bit_cast(float, __builtin_amdgcn_readlane(__builtin_bit_cast(int, v), l)); }
DEV float wave_sum(float v) {
    v = dpp_add<0xB1>(v);
    v = dpp_add<0x4E>(v);
    v = dpp_add<0x141>(v);
    v = dpp_add<0x140>(v);
    return (rdlane(v, 0) + rdlane(v, 16)) + (rdlane(v, 32) + rdlane(v, 48));
}
DEV float fast_exp(float x) { return __builtin_amdgcn_exp2f(x * 1.4426950408889634f); }
DEV void wg_barrier() {
    __builtin_amdgcn_fence(__ATOMIC_RELEASE, "workgroup");
    __builtin_amdgcn_s_barrier();
    __builtin_amdgcn_fence(__ATOMIC_ACQUIRE, "workgroup");
}
DEV void lds_arrive(unsigned* cnt, int lane) {
    wait_lgkm0();
    if (lane == 0) __hip_atomic_fetch_add(cnt, 1u, __ATOMIC_RELAXED, __HIP_MEMORY_SCOPE_WORKGROUP);
}
DEV void lds_wait(unsigned* cnt, unsigned target) {
    while (__hip_atomic_load(cnt, __ATOMIC_RELAXED, __HIP_MEMORY_SCOPE_WORKGROUP) < target) __builtin_amdgcn_s_sleep(1);
    asm volatile("" ::: "memory");
}
DEV float div_nr(float a, float b, float r) {
    float q = a * r;
    float e = __builtin_fmaf(-q, b, a);
    return __builtin_fmaf(e, r, q);
}

struct KIdxTable { u16 v[256 * 16]; };
constexpr int round_bf16_int(int x) {
    if (x < 256) return x;
    int sh = 0;
    while ((x >> sh) >= 256) sh++;
    int q = x >> sh, r = x & ((1 << sh) - 1), half = 1 << (sh - 1);
    if (r > half || (r == half && (q & 1))) q++;
    return q << sh;
}
constexpr int KC_STRIDE = 1024;
constexpr int KC_ZERO = 41;
constexpr int compact_pos(int tap) {
    int row = KC_ZERO, col = 0;
    if (tap >= 0) {
        int lr = round_bf16_int(tap);
        if (lr < 256) { row = (lr & 7) + 8 * (lr >> 7); col = (lr >> 3) & 15; }
        else if (lr < 512) { int x = lr - 256; row = 16 + ((x & 7) >> 1) + 4 * (x >> 7); col = (x >> 3) & 15; }
        else if (lr < 1024) { int x = lr - 512; row = 24 + ((x & 7) >> 2) + 2 * (x >> 7); col = (x >> 3) & 15; }
        else if (lr < 2048) { int x = lr - 1024; row = 32 + (x >> 7); col = (x >> 3) & 15; }
        else { row = 40; col = 0; }
    }
    return row * 18 + col;
}
constexpr KIdxTable make_kidx() {
    KIdxTable t{};
    for (int ub = 0; ub < 256; ub++)
        for (int i = 0; i < 16; i++) t.v[ub * 16 + i] = (u16)(4 * compact_pos(2047 - (8 * ub + i)));
    return t;
}
__device__ const KIdxTable KIDX = make_kidx();

DEV f32x2 cmul(f32x2 a, f32x2 b) {
    f32x2 r;
    r.x = __builtin_fmaf(-a.y, b.y, a.x * b.x);
    r.y = __builtin_fmaf(a.y, b.x, a.x * b.y);
    return r;
}
DEV f32x2 cexp_scaled(float zr, float zi, float l) {
    const float xr = zr * l, xi = zi * l;
    const float e = __builtin_amdgcn_exp2f(xr * 1.4426950408889634f);
    const float c_hi = 0.15915494309189535f;
    const float c_lo = 6.4206382e-09f;
    float rh = xi * c_hi;
    float rl = __builtin_fmaf(xi, c_lo, __builtin_fmaf(xi, c_hi, -rh));
    float fr = (rh - __builtin_rintf(rh)) + rl;
    return f32x2{e * __builtin_amdgcn_cosf(fr), e * __builtin_amdgcn_sinf(fr)};
}
DEV u32x2 split_bf(f32x2 v) {
    u32 h = pk2bf(v.x, v.y);
    u32 l = pk2bf(v.x - lo_f(h), v.y - hi_f(h));
    return u32x2{h, l};
}

constexpr int TBL_PITCH = 528;
constexpr int TBL_SIZE = 16 * TBL_PITCH;
constexpr int PRE_TBL = 0;
constexpr int PRE_P = 4 * TBL_SIZE;
constexpr int PRE_H = PRE_P + 2 * 3072;
constexpr int PRE_U = PRE_H + 8192;
constexpr int PRE_FLAGS = PRE_U + 8192;
constexpr int PRE_LDS = PRE_FLAGS + 64;

NOPK __global__ __launch_bounds__(512) void k_prep(const u16* __restrict__ u, const u16* __restrict__ gw,
                                                   u16* __restrict__ hF, u16* __restrict__ uF, float* __restrict__ g,
                                                   const u16* __restrict__ Are, const u16* __restrict__ Aim,
                                                   const u16* __restrict__ Bre, const u16* __restrict__ Bim,
                                                   const u16* __restrict__ Cre, const u16* __restrict__ Cim,
                                                   const u16* __restrict__ logdt, float* __restrict__ Kc,
                                                   const u16* __restrict__ w1) {
    extern __shared__ __attribute__((aligned(16))) char smem[];
    const int tid = TIDX, lane = tid & 63;
    const int w = __builtin_amdgcn_readfirstlane(tid >> 6);
    const int b = BIDX;
    unsigned* flags = (unsigned*)(smem + PRE_FLAGS);
    STAMP(0, 0);
    if (w >= 4) {
        const int lw = w - 4;
        const int s0 = b * 8;
        u32x4 xv[2];
#pragma unroll
        for (int i = 0; i < 2; i++) xv[i] = *(const u32x4*)(u + (size_t)(s0 + 2 * lw + i) * D + lane * 8);
        const u32x4 gv = *(const u32x4*)(gw + lane * 8);
        if (tid == 256) { flags[0] = 0; flags[1] = 0; flags[2] = 0; }
        wait_lgkm0();
        __builtin_amdgcn_s_barrier();
#pragma unroll
        for (int i = 0; i < 2; i++) {
            const int r = 2 * lw + i;
            float x[8];
#pragma unroll
            for (int j = 0; j < 4; j++) { x[2 * j] = lo_f(xv[i][j]); x[2 * j + 1] = hi_f(xv[i][j]); }
            float sum = 0.f, gs = 0.f;
#pragma unroll
            for (int j = 0; j < 4; j++) {
                sum += x[2 * j] + x[2 * j + 1];
                u32 p = pk2bf(x[2 * j] * lo_f(gv[j]), x[2 * j + 1] * hi_f(gv[j]));
                gs += lo_f(p) + hi_f(p);
            }
            sum = wave_sum(sum);
            gs = wave_sum(gs);
            const float mean = sum * (1.f / D);
            float sq = 0.f;
#pragma unroll
            for (int j = 0; j < 8; j++) { float dl = x[j] - mean; sq = __builtin_fmaf(dl, dl, sq); }
            sq = wave_sum(sq);
            const float var = rbf(sq * (1.f / D));
            const float m = rbf(mean);
            const float sd = rbf(__builtin_sqrtf(rbf(var + 1e-5f)));
            const float rs = __builtin_amdgcn_rcpf(sd);
            u32x4 hv;
#pragma unroll
            for (int j = 0; j < 4; j++) {
                u32 c = pk2bf(x[2 * j] - m, x[2 * j + 1] - m);
                hv[j] = pk2bf(div_nr(lo_f(c), sd, rs), div_nr(hi_f(c), sd, rs));
            }
            const int c16 = lane ^ ((r & 3) << 2);
            *(u32x4*)(smem + PRE_H + r * 1024 + c16 * 16) = hv;
            *(u32x4*)(smem + PRE_U + r * 1024 + c16 * 16) = xv[i];
            if (lane == 0) {
                float gb = rbf(gs);
                g[s0 + r] = rbf(__builtin_amdgcn_rcpf(1.f + fast_exp(-gb)));
            }
        }
        STAMP(0, 1);
        lds_arrive(&flags[0], lane);
        lds_wait(&flags[0], 4);
        {
            const int nt = lane >> 4, q = (lane & 15) >> 2, p = lane & 3;
            const int ks = b >> 2, kq = b & 3;
#pragma unroll
            for (int i = 0; i < 2; i++) {
                const int dt = lw * 2 + i;
                const int cb = dt * 4 + nt;
                const unsigned o0 = q * 1024 + ((cb ^ (q << 1)) << 5) + p * 8;
                const unsigned o1 = o0 + 4096;
                u32x2 h0 = tr_read(smem + PRE_H, o0), h1 = tr_read(smem + PRE_H, o1);
                u32x2 u0 = tr_read(smem + PRE_U, o0), u1 = tr_read(smem + PRE_U, o1);
                const size_t off = ((size_t)((dt * 64 + ks) * 4 + nt) * 64 + kq * 16 + (lane & 15)) * 8;
                *(u32x4*)(hF + off) = u32x4{h0[0], h0[1], h1[0], h1[1]};
                *(u32x4*)(uF + off) = u32x4{u0[0], u0[1], u1[0], u1[1]};
            }
        }
        STAMP(0, 2);
        return;
    }
    const int ch = w >> 1, hf = w & 1;
    const int d = (b & 7) * 64 + (b >> 3) * 2 + ch;
    const int n = hf * 64 + lane;
    const u16 r_are = Are[d * NS + n], r_aim = Aim[d * NS + n], r_bre = Bre[d * NS + n], r_bim = Bim[d * NS + n];
    const u16 r_cre = Cre[d * NS + n], r_cim = Cim[d * NS + n];
    const u16 r_ldt = logdt[d];
    __builtin_amdgcn_s_barrier();
    const float dt = rbf(fast_exp(bf2f(r_ldt)));
    const float zr = dt * bf2f(r_are), zi = dt * bf2f(r_aim);
    f32x2 W;
    {
        const f32x2 B = {dt * bf2f(r_bre), dt * bf2f(r_bim)};
        const f32x2 C = {bf2f(r_cre), bf2f(r_cim)};
        W = cmul(C, B);
    }
    u32 dummy = 0;
    asm volatile("" :: "v"(zr), "v"(zi), "v"(W.x), "v"(W.y));
    {
        const int xcd = b & 7, li = b >> 3;
        const char* base = (const char*)w1 + ((size_t)(li * 64) * F + (xcd >> 1) * 448) * 2;
#pragma unroll
        for (int i = 0; i < 4; i++) {
            const int idx = (w * 4 + i) * 64 + lane;
            const int row = idx / 14, sec = idx - row * 14;
            if (idx < 896) touch(dummy, (unsigned)(row * (F * 2) + sec * 64), base);
        }
    }
    STAMP(0, 1);
    char* tbl = smem + PRE_TBL + w * TBL_SIZE;
    char* tw = tbl + lane * 8;
    const char* trd = tbl + (lane & 15) * TBL_PITCH + (lane >> 4) * 16;
    u32x4 Bh[8], Bl[8];
    {
        f32x2 E8 = cexp_scaled(zr, zi, 8.f);
        E8.y = -E8.y;
        f32x2 pw[16];
        pw[0] = f32x2{1.f, 0.f};
        pw[1] = E8;
#pragma unroll
        for (int q = 2; q < 16; q++) pw[q] = (q & 1) ? cmul(pw[q - 1], E8) : cmul(pw[q / 2], pw[q / 2]);
#pragma unroll
        for (int q = 0; q < 16; q++) *(u32x2*)(tw + q * TBL_PITCH) = split_bf(pw[q]);
        wait_lgkm0();
#pragma unroll
        for (int T = 0; T < 8; T++) {
            u32x4 v = *(const u32x4*)(trd + T * 64);
            Bh[T] = u32x4{v[0], v[0], v[2], v[2]};
            Bl[T] = u32x4{v[1], v[1], v[3], v[3]};
        }
        wait_lgkm0();
    }
    f32x4 kacc[3];
    {
        const f32x2 E1 = cexp_scaled(zr, zi, 1.f);
        const f32x2 E128 = cexp_scaled(zr, zi, 128.f);
        f32x2 WY[8];
        {
            f32x2 Y[8];
            Y[0] = f32x2{1.f, 0.f}; Y[1] = E1;
#pragma unroll
            for (int c = 2; c < 8; c++) Y[c] = (c & 1) ? cmul(Y[c - 1], E1) : cmul(Y[c / 2], Y[c / 2]);
            WY[0] = W;
#pragma unroll
            for (int c = 1; c < 8; c++) WY[c] = cmul(W, Y[c]);
        }
        f32x2 Xp[17];
        Xp[0] = f32x2{1.f, 0.f}; Xp[1] = E128;
#pragma unroll
        for (int a = 2; a < 17; a++) Xp[a] = (a & 1) ? cmul(Xp[a - 1], E128) : cmul(Xp[a / 2], Xp[a / 2]);
#pragma unroll
        for (int tile = 0; tile < 3; tile++) {
#pragma unroll
            for (int r = 0; r < 16; r++) {
                f32x2 v;
                bool valid = true;
                if (tile == 0) { v = (r < 8) ? WY[r] : cmul(WY[r - 8], Xp[1]); }
                else if (tile == 1) {
                    if (r < 8) v = cmul(WY[2 * (r & 3)], Xp[2 + (r >> 2)]);
                    else v = cmul(WY[4 * (r & 1)], Xp[4 + ((r - 8) >> 1)]);
                } else {
                    if (r < 8) v = cmul(W, Xp[8 + r]);
                    else if (r == 8) v = cmul(W, Xp[16]);
                    else valid = false;
                }
                if (valid) *(u32x2*)(tw + r * TBL_PITCH) = split_bf(v);
            }
            wait_lgkm0();
            f32x4 a4 = {0.f, 0.f, 0.f, 0.f};
#pragma unroll
            for (int T = 0; T < 8; T++) {
                u32x4 av = *(const u32x4*)(trd + T * 64);
                a4 = mfma16(av, Bh[T], a4);
                a4 = mfma16(av, Bl[T], a4);
            }
            kacc[tile] = a4;
            wait_lgkm0();
        }
    }
    STAMP(0, 2);
    {
        char* P = smem + PRE_P + ch * 3072;
        if (hf == 1) {
#pragma unroll
            for (int tile = 0; tile < 3; tile++) *(f32x4*)(P + tile * 1024 + lane * 16) = kacc[tile];
            lds_arrive(&flags[1 + ch], lane);
        } else {
            lds_wait(&flags[1 + ch], 1);
            const int mq = lane >> 4, col = lane & 15;
            float* dst = Kc + (size_t)d * KC_STRIDE + (mq * 4) * 18 + col;
#pragma unroll
            for (int tile = 0; tile < 3; tile++) {
                f32x4 v = kacc[tile] + *(const f32x4*)(P + tile * 1024 + lane * 16);
#pragma unroll
                for (int r = 0; r < 4; r++) dst[(tile * 16 + r) * 18] = v[r];
            }
        }
    }
    STAMP(0, 3);
    touch_done(dummy);
}

DEV void dma_slot(unsigned lds, const unsigned (&voff)[4], const void* sbase) {
    asm volatile("s_mov_b32 m0, %0\n\ts_nop 0\n\t"
                 "global_load_lds_dwordx4 %1, %5\n\t"
                 "global_load_lds_dwordx4 %2, %5 offset:1024\n\t"
                 "global_load_lds_dwordx4 %3, %5 offset:2048\n\t"
                 "global_load_lds_dwordx4 %4, %5 offset:3072"
                 :: "s"(lds), "v"(voff[0]), "v"(voff[1]), "v"(voff[2]), "v"(voff[3]), "s"(sbase) : "memory", "m0");
}
DEV void xload(u32x4 (&x)[4], unsigned voff, const void* sbase) {
    asm volatile("global_load_dwordx4 %0, %4, %5\n\t"
                 "global_load_dwordx4 %1, %4, %5 offset:1024\n\t"
                 "global_load_dwordx4 %2, %4, %5 offset:2048\n\t"
                 "global_load_dwordx4 %3, %4, %5 offset:3072"
                 : "+v"(x[0]), "+v"(x[1]), "+v"(x[2]), "+v"(x[3]) : "v"(voff), "s"(sbase) : "memory");
}
template <int S, int WPITCH, bool W_IS_A>
struct GemmCtx {
    const char* smem;
    const char* wbase;
    const char* xbase;
    unsigned ring;
    int nxt, nxtx;
    unsigned wvoff[4];
    unsigned xvoff;
    unsigned raddr[4];
    u32x4 X[4][4];
    f32x4 acc[4][4];
};
template <int J, int S, int WPITCH, bool W_IS_A>
DEV void g_issue_w(GemmCtx<S, WPITCH, W_IS_A>& c) {
    dma_slot(c.ring + J * 4096, c.wvoff, c.wbase + (size_t)((unsigned)c.nxt * (unsigned)(32 * WPITCH)));
    c.nxt = (c.nxt + 1 == S) ? 0 : c.nxt + 1;
}
template <int J, int S, int WPITCH, bool W_IS_A>
DEV void g_issue_x(GemmCtx<S, WPITCH, W_IS_A>& c) {
    xload(c.X[J], c.xvoff, c.xbase + (size_t)((unsigned)c.nxtx * 4096u));
    c.nxtx = (c.nxtx + 1 == S) ? 0 : c.nxtx + 1;
}
template <int J, int N, bool IX, bool IW, int S, int WPITCH, bool W_IS_A>
DEV void g_step(GemmCtx<S, WPITCH, W_IS_A>& c) {
    u32x4(&x)[4] = c.X[J];
    wait_vm<N>(x[0], x[1], x[2], x[3]);
    u32x4 Wf[4];
#pragma unroll
    for (int i = 0; i < 4; i++) {
        u32x2 a = tr_read(c.smem, c.raddr[i] + J * 4096);
        u32x2 b = tr_read(c.smem, c.raddr[i] + J * 4096 + 512);
        Wf[i] = u32x4{a[0], a[1], b[0], b[1]};
    }
    if constexpr (IX) g_issue_x<(J + 3) & 3>(c);
    if constexpr (IW) {
        asm volatile("s_waitcnt lgkmcnt(0)" : "+v"(Wf[0]), "+v"(Wf[1]), "+v"(Wf[2]), "+v"(Wf[3]) :: "memory");
        g_issue_w<J>(c);
    }
#pragma unroll
    for (int i = 0; i < 4; i++)
#pragma unroll
        for (int j = 0; j < 4; j++) {
            if constexpr (W_IS_A) c.acc[i][j] = mfma16(Wf[i], x[j], c.acc[i][j]);
            else c.acc[i][j] = mfma16(x[i], Wf[j], c.acc[i][j]);
        }
    __builtin_amdgcn_sched_barrier(0);
}

template <int S, int WPITCH, bool W_IS_A>
DEV void gemm_wave(const char* smem, const char* wbase, const char* xbase, unsigned ring, int rot, int lane, f32x4 (&acc)[4][4]) {
    GemmCtx<S, WPITCH, W_IS_A> c;
    c.smem = smem; c.wbase = wbase; c.xbase = xbase; c.ring = ring; c.nxt = rot; c.nxtx = rot;
#pragma unroll
    for (int i = 0; i < 4; i++) {
        const int sw = (((lane >> 4) & 1) << 1) | ((i & 1) << 2);
        c.wvoff[i] = (unsigned)((8 * i + (lane >> 3)) * WPITCH + (((lane & 7) ^ sw) << 4) - 1024 * i);
    }
    c.xvoff = lane * 16;
    const int g = lane >> 4, q = (lane & 15) >> 2, p = lane & 3;
    const int fl = ((q >> 1) & 1) | ((g & 1) << 1);
#pragma unroll
    for (int i = 0; i < 4; i++) c.raddr[i] = ring + (8 * g + q) * 128 + ((i ^ fl) << 5) + p * 8;
#pragma unroll
    for (int i = 0; i < 4; i++)
#pragma unroll
        for (int j = 0; j < 4; j++) { c.acc[i][j] = f32x4{0.f, 0.f, 0.f, 0.f}; c.X[i][j] = u32x4{0, 0, 0, 0}; }
    g_issue_w<0>(c); g_issue_x<0>(c);
    g_issue_w<1>(c); g_issue_x<1>(c);
    g_issue_w<2>(c); g_issue_x<2>(c);
    g_issue_w<3>(c);
    constexpr int STEADY = S - 4;
#pragma unroll 1
    for (int it = 0; it < STEADY / 4; it++) {
        g_step<0, 20, true, true>(c);
        g_step<1, 20, true, true>(c);
        g_step<2, 20, true, true>(c);
        g_step<3, 20, true, true>(c);
    }
    constexpr int R = STEADY % 4;
    if constexpr (R >= 1) g_step<0, 20, true, true>(c);
    if constexpr (R >= 2) g_step<1, 20, true, true>(c);
    if constexpr (R >= 3) g_step<2, 20, true, true>(c);
    g_step<(R + 0) & 3, 20, true, false>(c);
    g_step<(R + 1) & 3, 16, false, false>(c);
    g_step<(R + 2) & 3, 8, false, false>(c);
    g_step<(R + 3) & 3, 0, false, false>(c);
#pragma unroll
    for (int i = 0; i < 4; i++)
#pragma unroll
        for (int j = 0; j < 4; j++) acc[i][j] = c.acc[i][j];
}

constexpr int MID_RING = 0;
constexpr int MID_SSM = 65536;
constexpr int CP_STRIDE = 4160;
constexpr int SSM_COPY = 0;
constexpr int SSM_U = 16640;
constexpr int SSM_P = SSM_U + 4608;
constexpr int SSM_X = SSM_P + 4096;
constexpr int SSM_SIZE = SSM_X + 8192;
constexpr int MID_FLAGS = MID_SSM + 2 * SSM_SIZE;
constexpr int MID_LDS = MID_FLAGS + 64;

NOPK __global__ __launch_bounds__(512) void k_mid(const u16* __restrict__ hF, const u16* __restrict__ w1, const u16* __restrict__ b1,
                                                  u16* __restrict__ UF, const float* __restrict__ Kc,
                                                  const u16* __restrict__ Dskip, const u16* __restrict__ uF,
                                                  u16* __restrict__ aT, const u16* __restrict__ w2) {
    extern __shared__ __attribute__((aligned(16))) char smem[];
    const int tid = TIDX, lane = tid & 63;
    const int w = __builtin_amdgcn_readfirstlane(tid >> 6);
    const int b = BIDX;
    unsigned* flags = (unsigned*)(smem + MID_FLAGS);
    STAMP(1, 0);

    if (w < 4) {
        const int xcd = b & 7, li = b >> 3;
        if (tid < 16) flags[tid] = 0;
        if (li >= 28) { wait_lgkm0(); __builtin_amdgcn_s_barrier(); return; }
        const int ftl = li % 7, dtl = li / 7;
        const int ft = (xcd >> 1) * 7 + ftl, dt = (xcd & 1) * 4 + dtl;
        u32x2 bb[4];
#pragma unroll
        for (int mt = 0; mt < 4; mt++) gload_early8(bb[mt], b1 + ft * 64 + mt * 16 + (lane >> 4) * 4);
        wait_lgkm0();
        __builtin_amdgcn_s_barrier();
        f32x4 acc[4][4];
        const char* wbase = (const char*)w1 + ((size_t)(w * 512) * F + ft * 64) * 2;
        const char* xbase = (const char*)hF + ((size_t)(dt * 64 + w * 16)) * 4096;
        const int rot = (2 * ftl + 4 * dtl) & 15;
        gemm_wave<16, F * 2, true>(smem, wbase, xbase, MID_RING + w * 16384, rot, lane, acc);
        STAMP(1, 1);
#pragma unroll
        for (int mt = 0; mt < 4; mt++) tie(bb[mt]);
        wait_lgkm0();
        f32x4 own[4];
        {
            char* mine = smem + MID_RING + w * 16384;
#define XCH(k) if (w == k) { \
            _Pragma("unroll") for (int nt = 0; nt < 4; nt++) _Pragma("unroll") for (int mt = 0; mt < 4; mt++) \
                if (nt != k) *(f32x4*)(mine + (nt * 4 + mt) * 1024 + lane * 16) = acc[mt][nt]; \
            _Pragma("unroll") for (int mt = 0; mt < 4; mt++) { own[mt] = acc[mt][k]; asm volatile("" : "+v"(own[mt])); } }
            XCH(0) XCH(1) XCH(2) XCH(3)
#undef XCH
        }
        lds_arrive(&flags[0], lane);
        lds_wait(&flags[0], 4);
#pragma unroll
        for (int o = 1; o < 4; o++) {
            const int ow = (w + o) & 3;
#pragma unroll
            for (int mt = 0; mt < 4; mt++)
                own[mt] += *(const f32x4*)(smem + MID_RING + ow * 16384 + (w * 4 + mt) * 1024 + lane * 16);
        }
        const int mq = lane >> 4, n = lane & 15;
#pragma unroll
        for (int mt = 0; mt < 4; mt++) {
            float v[4];
            {
                u32 p0 = pk2bf(own[mt][0], own[mt][1]), p1 = pk2bf(own[mt][2], own[mt][3]);
                u32 s0 = pk2bf(lo_f(p0) + lo_f(bb[mt][0]), hi_f(p0) + hi_f(bb[mt][0]));
                u32 s1 = pk2bf(lo_f(p1) + lo_f(bb[mt][1]), hi_f(p1) + hi_f(bb[mt][1]));
                v[0] = lo_f(s0); v[1] = hi_f(s0); v[2] = lo_f(s1); v[3] = hi_f(s1);
            }
#pragma unroll
            for (int i = 0; i < 4; i++) {
                const float x = v[i];
                const float p = __builtin_fmaf(x * x, -2.f * 0.7978845608028654f * 0.044715f * 1.4426950408889634f,
                                               -2.f * 0.7978845608028654f * 1.4426950408889634f);
                const float e = __builtin_amdgcn_exp2f(x * p);
                v[i] = x * __builtin_amdgcn_rcpf(1.f + e);
            }
            u32x2 o = {pk2bf(v[0], v[1]), pk2bf(v[2], v[3])};
            const int ks = ft * 2 + (mt >> 1), kq = 2 * (mt & 1) + (mq >> 1);
            char* dst = (char*)UF + ((size_t)((dt * 56 + ks) * 4 + w) * 64 + kq * 16 + n) * 16 + (mq & 1) * 8;
            *(u32x2*)dst = o;
        }
        STAMP(1, 2);
        return;
    }

    __builtin_amdgcn_s_setprio(3);
    const int wv = w - 4, ch = wv >> 1, hf = wv & 1;
    const int d = (b & 7) * 64 + (b >> 3) * 2 + ch;
    const unsigned sb_off = MID_SSM + ch * SSM_SIZE;
    char* sb = smem + sb_off;
    unsigned* fl = flags + 4 + ch * 4;
    {
        const int dt = d >> 6, nt = (d >> 4) & 3, m = d & 15;
        const char* src = (const char*)uF + ((size_t)(dt * 64 * 4 + nt) * 64 + m) * 16;
#pragma unroll
        for (int i = 0; i < 2; i++) {
            const int piece = hf * 128 + i * 64 + lane;
            const unsigned voff = (unsigned)((piece >> 2) * 4096 + (piece & 3) * 256);
            dma16(sb_off + SSM_U + 512 + (hf * 128 + i * 64) * 16, voff, src);
        }
    }
    {
        const char* src = (const char*)(Kc + (size_t)d * KC_STRIDE) + hf * 2048;
        dma16(sb_off + SSM_P + hf * 2048, lane * 16, src);
        dma16(sb_off + SSM_P + hf * 2048 + 1024, lane * 16, src + 1024);
    }
    if (hf == 0 && lane < 32) *(u32x4*)(sb + SSM_U + lane * 16) = u32x4{0, 0, 0, 0};
    u32x4 kix[2][2];
#pragma unroll
    for (int i = 0; i < 2; i++) {
        const u32x4* pt = (const u32x4*)(KIDX.v + (hf * 128 + i * 64 + lane) * 16);
        kix[i][0] = pt[0]; kix[i][1] = pt[1];
    }
    const float dsk = bf2f(Dskip[d]);
    __builtin_amdgcn_s_setprio(0);
    __builtin_amdgcn_s_barrier();
    wait_vm0<0>();
    if (hf == 1 && lane == 0) *(float*)(sb + SSM_P + 4 * (KC_ZERO * 18)) = 0.f;
    lds_arrive(&fl[0], lane);
    lds_wait(&fl[0], 2);
    STAMP(1, 3);
    {
        const char* P0 = sb + SSM_P;
#pragma unroll
        for (int i = 0; i < 2; i++) {
            const int ub = hf * 128 + i * 64 + lane;
            float v[12];
#pragma unroll
            for (int j = 0; j < 11; j++) {
                const u32 word = kix[i][j >> 3][(j >> 1) & 3];
                const unsigned off = (j & 1) ? (word >> 16) : (word & 0xffffu);
                v[j] = *(const float*)(P0 + off);
            }
            u32 e[5], o[5];
#pragma unroll
            for (int k = 0; k < 5; k++) { e[k] = pk2bf(v[2 * k], v[2 * k + 1]); o[k] = pk2bf(v[2 * k + 1], k < 5 ? v[(2 * k + 2) > 10 ? 10 : (2 * k + 2)] : 0.f); }
            char* dst = sb + SSM_COPY + ub * 16;
            *(u32x4*)(dst) = u32x4{e[0], e[1], e[2], e[3]};
            *(u32x4*)(dst + CP_STRIDE) = u32x4{o[0], o[1], o[2], o[3]};
            *(u32x4*)(dst + 2 * CP_STRIDE) = u32x4{e[1], e[2], e[3], e[4]};
            *(u32x4*)(dst + 3 * CP_STRIDE) = u32x4{o[1], o[2], o[3], o[4]};
        }
        if (hf == 0 && lane < 8) {
            *(u32x4*)(sb + SSM_COPY + (lane >> 1) * CP_STRIDE + (256 + (lane & 1)) * 16) = u32x4{0, 0, 0, 0};
        }
    }
    STAMP(1, 4);
    lds_arrive(&fl[1], lane);
    u32 dummy = 0;
    __builtin_amdgcn_s_setprio(3);
    {
        const int xcd = b & 7, li = b >> 3;
        const char* base = (const char*)w2 + ((size_t)(li * 56) * L + (xcd >> 1) * 512) * 2;
#pragma unroll
        for (int i = 0; i < 4; i++) {
            const int idx = (wv * 4 + i) * 64 + lane;
            if (idx < 896) touch(dummy, (unsigned)((idx >> 4) * (L * 2) + (idx & 15) * 64), base);
        }
    }
    __builtin_amdgcn_s_setprio(0);
    lds_wait(&fl[1], 2);
    STAMP(1, 5);
    f32x4 cacc[8];
#pragma unroll
    for (int i = 0; i < 8; i++) cacc[i] = f32x4{0.f, 0.f, 0.f, 0.f};
    {
        const int m = lane & 15, q = lane >> 4;
        const int gq = m >> 2, e = m & 3, c = 3 - e;
        const char* abase = sb + SSM_COPY + c * CP_STRIDE + 4104 - 8 * gq - 16 * q - 64 * 63 + 64 * (7 - hf * 4 - 3);
        const char* bbase = sb + SSM_U + 528 + 32 * m - 16 * q - 64 * 7 + 64 * (7 - hf * 4 - 3);
#pragma unroll
        for (int rr = 0; rr < 4; rr++) {
            const char* ab = abase + 64 * (3 - rr);
            const char* bb = bbase + 64 * (3 - rr);
            u32x4 A[8], B[8];
#pragma unroll
            for (int pp = 0; pp < 8; pp++) {
                const u32x2 a0 = *(const u32x2*)(ab + 64 * (56 - 8 * pp));
                const u32x2 a1 = *(const u32x2*)(ab + 64 * (56 - 8 * pp) + 8);
                A[pp] = u32x4{a0[0], a0[1], a1[0], a1[1]};
                B[pp] = *(const u32x4*)(bb + 512 * pp);
            }
#pragma unroll
            for (int i = 0; i < 8; i++)
#pragma unroll
                for (int pp = 0; pp <= i; pp++) cacc[i] = mfma16(A[pp], B[i - pp], cacc[i]);
        }
    }
    STAMP(1, 6);
    f32x4 keep[4];
    {
        char* xo = sb + SSM_X + hf * 4096;
        if (hf) {
#pragma unroll
            for (int i = 0; i < 4; i++) { *(f32x4*)(xo + i * 1024 + lane * 16) = cacc[i]; keep[i] = cacc[4 + i]; asm volatile("" : "+v"(keep[i])); }
        } else {
#pragma unroll
            for (int i = 0; i < 4; i++) { *(f32x4*)(xo + i * 1024 + lane * 16) = cacc[4 + i]; keep[i] = cacc[i]; asm volatile("" : "+v"(keep[i])); }
        }
    }
    lds_arrive(&fl[2], lane);
    lds_wait(&fl[2], 2);
    __builtin_amdgcn_s_setprio(3);
    {
        const char* xi = sb + SSM_X + (hf ^ 1) * 4096;
        const int nn = lane & 15, mq = lane >> 4;
#pragma unroll
        for (int i = 0; i < 4; i++) {
            f32x4 v = keep[i];
            v += *(const f32x4*)(xi + i * 1024 + lane * 16);
            const int t = 256 * (hf * 4 + i) + 16 * nn + 4 * mq;
            const u32x2 uu = *(const u32x2*)(sb + SSM_U + 512 + t * 2);
            u32 y0 = pk2bf(v[0], v[1]), y1 = pk2bf(v[2], v[3]);
            u32 s0 = pk2bf(lo_f(uu[0]) * dsk, hi_f(uu[0]) * dsk), s1 = pk2bf(lo_f(uu[1]) * dsk, hi_f(uu[1]) * dsk);
            u32x2 o = {pk2bf(lo_f(y0) + lo_f(s0), hi_f(y0) + hi_f(s0)), pk2bf(lo_f(y1) + lo_f(s1), hi_f(y1) + hi_f(s1))};
            *(u32x2*)(aT + (size_t)d * L + t) = o;
        }
    }
    STAMP(1, 7);
    touch_done(dummy);
}

constexpr int OUT_RING = 0;
constexpr int OUT_PPITCH = 272;
constexpr int OUT_A = 4 * 64 * OUT_PPITCH;
constexpr int OUT_LDS = OUT_A + 8192;

NOPK __global__ __launch_bounds__(256, 2) void k_out(const u16* __restrict__ UF, const u16* __restrict__ w2, const u16* __restrict__ b2,
                                                  const u16* __restrict__ aT, const float* __restrict__ g, u16* __restrict__ out) {
    extern __shared__ __attribute__((aligned(16))) char smem[];
    const int tid = TIDX, lane = tid & 63;
    const int w = __builtin_amdgcn_readfirstlane(tid >> 6);
    const int b = BIDX;
    const int xcd = b & 7, li = b >> 3;
    const int stl = li & 7, dtl = li >> 3;
    const int st = (xcd >> 1) * 8 + stl, dt = (xcd & 1) * 4 + dtl;
    STAMP(2, 0);
#pragma unroll
    for (int i = 0; i < 2; i++) {
        const int row = 16 * w + 8 * i + (lane >> 3);
        const int sw = (((lane >> 4) & 1) << 1) | ((i & 1) << 2);
        const unsigned voff = (unsigned)(row * (L * 2) + (((lane & 7) ^ sw) << 4));
        dma16(OUT_A + (16 * w + 8 * i) * 128, voff, (const char*)aT + ((size_t)dt * 64 * L + st * 64) * 2);
    }
    const int si = st * 64 + w * 16 + (lane & 15);
    u32 gg_raw, bias_raw;
    gload_early4(gg_raw, g + si);
    gload_early2(bias_raw, b2 + si);
    f32x4 acc[4][4];
    const char* wbase = (const char*)w2 + ((size_t)(w * 448) * L + st * 64) * 2;
    const char* xbase = (const char*)UF + ((size_t)(dt * 56 + w * 14)) * 4096;
    int rot = (2 * stl + 3 * dtl) % 14;
    gemm_wave<14, L * 2, false>(smem, wbase, xbase, OUT_RING + w * 16384, rot, lane, acc);
    STAMP(2, 1);
    tie(gg_raw); tie(bias_raw);
    const float gg = __builtin_bit_cast(float, gg_raw), bias = lo_f(bias_raw);
    wg_barrier();
    {
        const int n = lane & 15, mq = lane >> 4;
        char* P = smem + w * 64 * OUT_PPITCH;
#pragma unroll
        for (int mt = 0; mt < 4; mt++)
#pragma unroll
            for (int nt = 0; nt < 4; nt++) *(f32x4*)(P + (nt * 16 + n) * OUT_PPITCH + (mt * 16 + mq * 4) * 4) = acc[mt][nt];
    }
    wg_barrier();
    {
        const int i = lane & 15, gq = lane >> 4, q = i >> 2, p = lane & 3;
        const float og = rbf(1.f - gg);
#pragma unroll
        for (int it = 0; it < 2; it++) {
            const int c8 = it * 4 + gq;
            f32x4 s0 = {0.f, 0.f, 0.f, 0.f}, s1 = s0;
#pragma unroll
            for (int o = 0; o < 4; o++) {
                const char* P = smem + o * 64 * OUT_PPITCH + (w * 16 + i) * OUT_PPITCH + c8 * 32;
                s0 += *(const f32x4*)P;
                s1 += *(const f32x4*)(P + 16);
            }
            u32x2 a0, a1;
            {
                const int r0 = 8 * c8 + q, r1 = r0 + 4;
                const int f0 = ((r0 >> 1) & 1) | (((r0 >> 3) & 1) << 1);
                const int f1 = ((r1 >> 1) & 1) | (((r1 >> 3) & 1) << 1);
                a0 = tr_read(smem, OUT_A + r0 * 128 + ((w ^ f0) << 5) + p * 8);
                a1 = tr_read(smem, OUT_A + r1 * 128 + ((w ^ f1) << 5) + p * 8);
            }
            float bv[8] = {s0[0], s0[1], s0[2], s0[3], s1[0], s1[1], s1[2], s1[3]};
            u32 av[4] = {a0[0], a0[1], a1[0], a1[1]};
            u32x4 o;
#pragma unroll
            for (int e = 0; e < 4; e++) {
                u32 y = pk2bf(bv[2 * e], bv[2 * e + 1]);
                y = pk2bf(lo_f(y) + bias, hi_f(y) + bias);
                u32 t1 = pk2bf(gg * lo_f(av[e]), gg * hi_f(av[e]));
                u32 t2 = pk2bf(og * lo_f(y), og * hi_f(y));
                o[e] = pk2bf(lo_f(t1) + lo_f(t2), hi_f(t1) + hi_f(t2));
            }
            *(u32x4*)(out + (size_t)si * D + dt * 64 + c8 * 8) = o;
        }
    }
    STAMP(2, 2);
}

extern "C" void kernel_launch(void* const* d_in, const int* in_sizes, int n_in,
                              void* d_out, int out_size, void* d_ws, size_t ws_size,
                              hipStream_t stream) {
    const u16* u = (const u16*)d_in[0];
    const u16* Are = (const u16*)d_in[1];
    const u16* Aim = (const u16*)d_in[2];
    const u16* Bre = (const u16*)d_in[3];
    const u16* Bim = (const u16*)d_in[4];
    const u16* Cre = (const u16*)d_in[5];
    const u16* Cim = (const u16*)d_in[6];
    const u16* logdt = (const u16*)d_in[7];
    const u16* Dskip = (const u16*)d_in[8];
    const u16* w1 = (const u16*)d_in[10];
    const u16* b1 = (const u16*)d_in[11];
    const u16* w2 = (const u16*)d_in[12];
    const u16* b2 = (const u16*)d_in[13];
    const u16* gw = (const u16*)d_in[14];
    char* ws = (char*)d_ws;
    u16* hF = (u16*)ws;
    u16* uF = (u16*)(ws + (2 << 20));
    u16* aT = (u16*)(ws + (4 << 20));
    u16* UF = (u16*)(ws + (6 << 20));
    float* g = (float*)(ws + (8 << 20));
    float* Kc = (float*)(ws + (9 << 20));
    static bool attr_done = false;
    if (!attr_done) {
        (void)hipFuncSetAttribute((const void*)k_prep, hipFuncAttributeMaxDynamicSharedMemorySize, PRE_LDS);
        (void)hipFuncSetAttribute((const void*)k_mid, hipFuncAttributeMaxDynamicSharedMemorySize, MID_LDS);
        (void)hipFuncSetAttribute((const void*)k_out, hipFuncAttributeMaxDynamicSharedMemorySize, OUT_LDS);
        attr_done = true;
    }
    k_prep<<<256, 512, PRE_LDS, stream>>>(u, gw, hF, uF, g, Are, Aim, Bre, Bim, Cre, Cim, logdt, Kc, w1);
    k_mid<<<256, 512, MID_LDS, stream>>>(hF, w1, b1, UF, Kc, Dskip, uF, aT, w2);
    k_out<<<256, 256, OUT_LDS, stream>>>(UF, w2, b2, aT, g, (u16*)d_out);
}
```

```cpp
#include <hip/hip_runtime.h>
#include <stdint.h>

typedef unsigned short u16;
typedef unsigned int u32;
using bf16x8 = __attribute__((ext_vector_type(8))) __bf16;
using bf16x2 = __attribute__((ext_vector_type(2))) __bf16;
using s16x4 = __attribute__((ext_vector_type(4))) short;
using f32x4 = __attribute__((ext_vector_type(4))) float;
using f32x2 = __attribute__((ext_vector_type(2))) float;
using u32x4 = __attribute__((ext_vector_type(4))) unsigned;
using u32x2 = __attribute__((ext_vector_type(2))) unsigned;
typedef __attribute__((address_space(3))) s16x4* lds_s16x4_ptr;

#if defined(__HIP_DEVICE_COMPILE__)
#define NOPK __attribute__((target("no-packed-fp32-ops")))
#else
#define NOPK
#endif
#define DEV NOPK __device__ __forceinline__
#define TIDX ((int)__builtin_amdgcn_workitem_id_x())
#define BIDX ((int)__builtin_amdgcn_workgroup_id_x())

#define STAMP_DECL do {} while (0)
#define STAMP(kern, idx) do {} while (0)
#define STAMP_DUMP do {} while (0)

constexpr int L = 2048, D = 512, NS = 128, F = 1792;
#define T1 1
#define STR_(x) #x
#define STR(x) STR_(x)

DEV float bf2f(u16 v) { return __builtin_bit_cast(float, ((unsigned)v) << 16); }
DEV u32 pk2bf(float a, float b) {
    f32x2 v = {a, b};
    return __builtin_bit_cast(u32, __builtin_convertvector(v, bf16x2));
}
DEV float lo_f(u32 p) { return __builtin_bit_cast(float, p << 16); }
DEV float hi_f(u32 p) { return __builtin_bit_cast(float, p & 0xffff0000u); }
DEV float rbf(float f) { return lo_f(pk2bf(f, 0.f)); }

DEV u32x2 tr_read(const char* lds_generic_base, unsigned off) {
    s16x4 v = __builtin_amdgcn_ds_read_tr16_b64_v4i16((lds_s16x4_ptr)(lds_generic_base + off));
    return __builtin_bit_cast(u32x2, v);
}
DEV f32x4 mfma16(u32x4 a, u32x4 b, f32x4 c) {
    return __builtin_amdgcn_mfma_f32_16x16x32_bf16(__builtin_bit_cast(bf16x8, a), __builtin_bit_cast(bf16x8, b), c, 0, 0, 0);
}

DEV void gload_early8(u32x2& dst, const void* p) { asm volatile("global_load_dwordx2 %0, %1, off" : "=v"(dst) : "v"(p) : "memory"); }
template <typename T> DEV void tie(T& x) { asm volatile("" : "+v"(x) :: "memory"); }
DEV void touch(u32& dummy, unsigned voff, const void* sbase) {
    asm volatile("global_load_dword %0, %1, %2" : "+v"(dummy) : "v"(voff), "s"(sbase) : "memory");
}
DEV void touch_done(u32& dummy) { asm volatile("s_waitcnt vmcnt(0)" : "+v"(dummy) :: "memory"); }
template <int N>
DEV void wait_vm(u32x4& a, u32x4& b, u32x4& c, u32x4& d) {
    asm volatile("s_waitcnt vmcnt(%4)" : "+v"(a), "+v"(b), "+v"(c), "+v"(d) : "i"(N) : "memory");
}
template <int N>
DEV void wait_vm0() { asm volatile("s_waitcnt vmcnt(%0)" ::"i"(N) : "memory"); }
DEV void wait_lgkm0() { asm volatile("s_waitcnt lgkmcnt(0)" ::: "memory"); }
DEV u32 ld_sc1_4(const void* p) {
    u32 v; asm volatile("global_load_dword %0, %1, off sc1\n\ts_waitcnt vmcnt(0)" : "=&v"(v) : "v"(p) : "memory"); return v;
}
DEV u32x4 ld_sc1_16(const void* p) {
    u32x4 v; asm volatile("global_load_dwordx4 %0, %1, off sc1\n\ts_waitcnt vmcnt(0)" : "=&v"(v) : "v"(p) : "memory"); return v;
}
DEV void ld2_sc1_16(u32x4& a, u32x4& b, const void* pa, const void* pb) {
    asm volatile("global_load_dwordx4 %0, %2, off sc1\n\tglobal_load_dwordx4 %1, %3, off sc1\n\ts_waitcnt vmcnt(0)"
                 : "=&v"(a), "=&v"(b) : "v"(pa), "v"(pb) : "memory");
}
DEV void st_sc1_16(void* p, u32x4 v) { asm volatile("global_store_dwordx4 %0, %1, off sc1\n\ts_nop 1" :: "v"(p), "v"(v) : "memory"); }
DEV void st_sc1_8(void* p, u32x2 v) { asm volatile("global_store_dwordx2 %0, %1, off sc1\n\ts_nop 1" :: "v"(p), "v"(v) : "memory"); }
DEV void st_sc1_4(void* p, u32 v) { asm volatile("global_store_dword %0, %1, off sc1" :: "v"(p), "v"(v) : "memory"); }

template <int CTRL>
DEV float dpp_add(float v) {
    int t = __builtin_amdgcn_update_dpp(0, __builtin_bit_cast(int, v), CTRL, 0xf, 0xf, true);
    return v + __builtin_bit_cast(float, t);
}
DEV float rdlane(float v, int l) { return __builtin_bit_cast(float, __builtin_amdgcn_readlane(__builtin_bit_cast(int, v), l)); }
DEV float wave_sum(float v) {
    v = dpp_add<0xB1>(v);
    v = dpp_add<0x4E>(v);
    v = dpp_add<0x141>(v);
    v = dpp_add<0x140>(v);
    return (rdlane(v, 0) + rdlane(v, 16)) + (rdlane(v, 32) + rdlane(v, 48));
}
DEV float fast_exp(float x) { return __builtin_amdgcn_exp2f(x * 1.4426950408889634f); }
DEV void lds_arrive(unsigned* cnt, int lane) {
    wait_lgkm0();
    if (lane == 0) __hip_atomic_fetch_add(cnt, 1u, __ATOMIC_RELAXED, __HIP_MEMORY_SCOPE_WORKGROUP);
}
DEV void lds_wait(unsigned* cnt, unsigned target) {
    while (__hip_atomic_load(cnt, __ATOMIC_RELAXED, __HIP_MEMORY_SCOPE_WORKGROUP) < target) __builtin_amdgcn_s_sleep(1);
    asm volatile("" ::: "memory");
}
constexpr int SPIN_CAP = 20000;
DEV void poll_flags256(const u32* fl, u32 tag, int lane) {
#pragma unroll 1
    for (int spin = 0; spin < SPIN_CAP; spin++) {
        u32x4 v = ld_sc1_16(fl + lane * 4);
        bool ok = v[0] == tag && v[1] == tag && v[2] == tag && v[3] == tag;
        if (__builtin_amdgcn_ballot_w64(ok) == ~0ull) break;
        __builtin_amdgcn_s_sleep(2);
    }
}
DEV void poll_flags(const u32* fl, int n, u32 tag, int lane) {
    const u32* p = fl + (lane < n ? lane : 0);
#pragma unroll 1
    for (int spin = 0; spin < SPIN_CAP; spin++) {
        u32 v = ld_sc1_4(p);
        if (__builtin_amdgcn_ballot_w64(v == tag) == ~0ull) break;
        __builtin_amdgcn_s_sleep(2);
    }
}
DEV float div_nr(float a, float b, float r) {
    float q = a * r;
    float e = __builtin_fmaf(-q, b, a);
    return __builtin_fmaf(e, r, q);
}

struct KIdxTable { u16 v[256 * 16]; };
constexpr int round_bf16_int(int x) {
    if (x < 256) return x;
    int sh = 0;
    while ((x >> sh) >= 256) sh++;
    int q = x >> sh, r = x & ((1 << sh) - 1), half = 1 << (sh - 1);
    if (r > half || (r == half && (q & 1))) q++;
    return q << sh;
}
constexpr int KC_ZERO = 41;
constexpr int compact_pos(int tap) {
    int row = KC_ZERO, col = 0;
    if (tap >= 0) {
        int lr = round_bf16_int(tap);
        if (lr < 256) { row = (lr & 7) + 8 * (lr >> 7); col = (lr >> 3) & 15; }
        else if (lr < 512) { int x = lr - 256; row = 16 + ((x & 7) >> 1) + 4 * (x >> 7); col = (x >> 3) & 15; }
        else if (lr < 1024) { int x = lr - 512; row = 24 + ((x & 7) >> 2) + 2 * (x >> 7); col = (x >> 3) & 15; }
        else if (lr < 2048) { int x = lr - 1024; row = 32 + (x >> 7); col = (x >> 3) & 15; }
        else { row = 40; col = 0; }
    }
    return row * 18 + col;
}
constexpr KIdxTable make_kidx() {
    KIdxTable t{};
    for (int ub = 0; ub < 256; ub++)
        for (int i = 0; i < 16; i++) t.v[ub * 16 + i] = (u16)(4 * compact_pos(2047 - (8 * ub + i)));
    return t;
}
__device__ const KIdxTable KIDX = make_kidx();

DEV f32x2 cmul(f32x2 a, f32x2 b) {
    f32x2 r;
    r.x = __builtin_fmaf(-a.y, b.y, a.x * b.x);
    r.y = __builtin_fmaf(a.y, b.x, a.x * b.y);
    return r;
}
DEV f32x2 cexp_scaled(float zr, float zi, float l) {
    const float xr = zr * l, xi = zi * l;
    const float e = __builtin_amdgcn_exp2f(xr * 1.4426950408889634f);
    const float c_hi = 0.15915494309189535f;
    const float c_lo = 6.4206382e-09f;
    float rh = xi * c_hi;
    float rl = __builtin_fmaf(xi, c_lo, __builtin_fmaf(xi, c_hi, -rh));
    float fr = (rh - __builtin_rintf(rh)) + rl;
    return f32x2{e * __builtin_amdgcn_cosf(fr), e * __builtin_amdgcn_sinf(fr)};
}
DEV u32x2 split_bf(f32x2 v) {
    u32 h = pk2bf(v.x, v.y);
    u32 l = pk2bf(v.x - lo_f(h), v.y - hi_f(h));
    return u32x2{h, l};
}

DEV void dma_slot(unsigned lds_, const unsigned (&voff)[4], const void* sbase) {
    const unsigned lds = (unsigned)__builtin_amdgcn_readfirstlane((int)lds_);
    asm volatile("s_mov_b32 m0, %0\n\ts_nop 0\n\t"
                 "global_load_lds_dwordx4 %1, %5\n\t"
                 "global_load_lds_dwordx4 %2, %5 offset:1024\n\t"
                 "global_load_lds_dwordx4 %3, %5 offset:2048\n\t"
                 "global_load_lds_dwordx4 %4, %5 offset:3072"
                 :: "s"(lds), "v"(voff[0]), "v"(voff[1]), "v"(voff[2]), "v"(voff[3]), "s"(sbase) : "memory", "m0");
}
DEV void xload(u32x4 (&x)[4], unsigned voff, const void* sbase) {
    asm volatile("global_load_dwordx4 %0, %4, %5 sc1\n\t"
                 "global_load_dwordx4 %1, %4, %5 offset:1024 sc1\n\t"
                 "global_load_dwordx4 %2, %4, %5 offset:2048 sc1\n\t"
                 "global_load_dwordx4 %3, %4, %5 offset:3072 sc1"
                 : "+v"(x[0]), "+v"(x[1]), "+v"(x[2]), "+v"(x[3]) : "v"(voff), "s"(sbase) : "memory");
}
template <int S, int WPITCH, bool W_IS_A>
struct GemmCtx {
    const char* smem;
    const char* wbase;
    const char* xbase;
    unsigned ring;
    int nxt, nxtx;
    unsigned wvoff[4];
    unsigned xvoff;
    unsigned raddr[4];
    u32x4 X[4][4];
    f32x4 acc[4][4];
};
template <int J, int S, int WPITCH, bool W_IS_A>
DEV void g_issue_w(GemmCtx<S, WPITCH, W_IS_A>& c) {
    dma_slot(c.ring + J * 4096, c.wvoff, c.wbase + (size_t)((unsigned)c.nxt * (unsigned)(32 * WPITCH)));
    c.nxt = (c.nxt + 1 == S) ? 0 : c.nxt + 1;
}
template <int J, int S, int WPITCH, bool W_IS_A>
DEV void g_issue_x(GemmCtx<S, WPITCH, W_IS_A>& c) {
    xload(c.X[J], c.xvoff, c.xbase + (size_t)((unsigned)c.nxtx * 4096u));
    c.nxtx = (c.nxtx + 1 == S) ? 0 : c.nxtx + 1;
}
template <int J, int N, bool IX, bool IW, int S, int WPITCH, bool W_IS_A>
DEV void g_step(GemmCtx<S, WPITCH, W_IS_A>& c) {
    u32x4(&x)[4] = c.X[J];
    wait_vm<N>(x[0], x[1], x[2], x[3]);
    u32x4 Wf[4];
#pragma unroll
    for (int i = 0; i < 4; i++) {
        u32x2 a = tr_read(c.smem, c.raddr[i] + J * 4096);
        u32x2 b = tr_read(c.smem, c.raddr[i] + J * 4096 + 512);
        Wf[i] = u32x4{a[0], a[1], b[0], b[1]};
    }
    if constexpr (IX) g_issue_x<(J + 3) & 3>(c);
    if constexpr (IW) {
        asm volatile("s_waitcnt lgkmcnt(0)" : "+v"(Wf[0]), "+v"(Wf[1]), "+v"(Wf[2]), "+v"(Wf[3]) :: "memory");
        g_issue_w<J>(c);
    }
#pragma unroll
    for (int i = 0; i < 4; i++)
#pragma unroll
        for (int j = 0; j < 4; j++) {
            if constexpr (W_IS_A) c.acc[i][j] = mfma16(Wf[i], x[j], c.acc[i][j]);
            else c.acc[i][j] = mfma16(x[i], Wf[j], c.acc[i][j]);
        }
    __builtin_amdgcn_sched_barrier(0);
}
template <int S, int WPITCH, bool W_IS_A>
DEV void gemm_begin(GemmCtx<S, WPITCH, W_IS_A>& c, const char* smem, const char* wbase, const char* xbase, unsigned ring, int rot, int lane) {
    c.smem = smem; c.wbase = wbase; c.xbase = xbase; c.ring = ring; c.nxt = rot; c.nxtx = rot;
#pragma unroll
    for (int i = 0; i < 4; i++) {
        const int sw = (((lane >> 4) & 1) << 1) | ((i & 1) << 2);
        c.wvoff[i] = (unsigned)((8 * i + (lane >> 3)) * WPITCH + (((lane & 7) ^ sw) << 4) - 1024 * i);
    }
    c.xvoff = lane * 16;
    const int g = lane >> 4, q = (lane & 15) >> 2, p = lane & 3;
    const int fl = ((q >> 1) & 1) | ((g & 1) << 1);
#pragma unroll
    for (int i = 0; i < 4; i++) c.raddr[i] = ring + (8 * g + q) * 128 + ((i ^ fl) << 5) + p * 8;
    g_issue_w<0>(c); g_issue_w<1>(c); g_issue_w<2>(c); g_issue_w<3>(c);
}
template <int S, int WPITCH, bool W_IS_A>
DEV void gemm_run(GemmCtx<S, WPITCH, W_IS_A>& c) {
#pragma unroll
    for (int i = 0; i < 4; i++)
#pragma unroll
        for (int j = 0; j < 4; j++) { c.acc[i][j] = f32x4{0.f, 0.f, 0.f, 0.f}; c.X[i][j] = u32x4{0, 0, 0, 0}; }
    g_issue_x<0>(c); g_issue_x<1>(c); g_issue_x<2>(c);
    g_step<0, 8, true, true>(c);
    g_step<1, 12, true, true>(c);
    g_step<2, 16, true, true>(c);
    g_step<3, 20, true, true>(c);
    constexpr int STEADY = S - 8;
#pragma unroll 1
    for (int it = 0; it < STEADY / 4; it++) {
        g_step<0, 20, true, true>(c);
        g_step<1, 20, true, true>(c);
        g_step<2, 20, true, true>(c);
        g_step<3, 20, true, true>(c);
    }
    constexpr int R = STEADY % 4;
    if constexpr (R >= 1) g_step<0, 20, true, true>(c);
    if constexpr (R >= 2) g_step<1, 20, true, true>(c);
    if constexpr (R >= 3) g_step<2, 20, true, true>(c);
    g_step<(R + 0) & 3, 20, true, false>(c);
    g_step<(R + 1) & 3, 16, false, false>(c);
    g_step<(R + 2) & 3, 8, false, false>(c);
    g_step<(R + 3) & 3, 0, false, false>(c);
}

constexpr int RING = 0;
constexpr int STG = 65536;
constexpr int SSM0 = STG + 16384;
constexpr int TBL_PITCH = 528;
constexpr int TBL_SIZE = 16 * TBL_PITCH;
constexpr int CP_STRIDE = 4160;
constexpr int SSM_COPY = 0;
constexpr int SSM_U = 16896;
constexpr int SSM_P = SSM_U + 4608;
constexpr int SSM_X = SSM_P + 2 * 3456;
constexpr int SSM_SIZE = SSM_X + 8192;
constexpr int LFLAGS = SSM0 + 2 * SSM_SIZE;
constexpr int LDS_BYTES = LFLAGS + 128;
enum { LF_LN = 0, LF_LNPUB = 1, LF_H = 2, LF_X1 = 3, LF_X1R = 4, LF_UPUB = 5, LF_U = 6, LF_X2 = 7, LF_EPI = 8, LF_APUB = 9, LF_LNST = 10, LF_LNPUB2 = 11, LF_SSM = 12   };
constexpr int GF_EPOCH = 0, GF_H = 64, GF_H2 = 64 + 256, GF_U = 64 + 512, GF_A = 64 + 512 + 8 * 32;

NOPK __global__ __launch_bounds__(512) void k_fused(const u16* __restrict__ u, const u16* __restrict__ gw,
                                                    const u16* __restrict__ Are, const u16* __restrict__ Aim,
                                                    const u16* __restrict__ Bre, const u16* __restrict__ Bim,
                                                    const u16* __restrict__ Cre, const u16* __restrict__ Cim,
                                                    const u16* __restrict__ logdt, const u16* __restrict__ Dskip,
                                                    const u16* __restrict__ w1, const u16* __restrict__ b1,
                                                    const u16* __restrict__ w2, const u16* __restrict__ b2,
                                                    char* __restrict__ ws, u16* __restrict__ out) {
    __shared__ __attribute__((aligned(16))) char smem[LDS_BYTES];
    const int tid = TIDX, lane = tid & 63;
    const int w = __builtin_amdgcn_readfirstlane(tid >> 6);
    const int b = BIDX;
    const int xcd = b & 7, li = b >> 3;
    unsigned* lf = (unsigned*)(smem + LFLAGS);
    u16* hF = (u16*)ws;
    u16* uF = (u16*)(ws + (2 << 20));
    u16* aT = (u16*)(ws + (4 << 20));
    u16* UF = (u16*)(ws + (6 << 20));
    float* g = (float*)(ws + (8 << 20));
    u32* gf = (u32*)(ws + (9 << 20));
    const int stl = li & 7, dtl2 = li >> 3;
    const int st = (xcd >> 1) * 8 + stl, dt2 = (xcd & 1) * 4 + dtl2;
    STAMP_DECL;
    STAMP(0, 0);
    if (tid < 32) lf[tid] = 0;
    wait_lgkm0();
    __builtin_amdgcn_s_barrier();

    if (w < 4) {
        __builtin_amdgcn_s_setprio(3);
        const int s0 = b * 8;
        u32x4 xv[2];
#pragma unroll
        for (int i = 0; i < 2; i++) xv[i] = *(const u32x4*)(u + (size_t)(s0 + 2 * w + i) * D + lane * 8);
        const u32x4 gv = *(const u32x4*)(gw + lane * 8);
        const u32 tag = ld_sc1_4(gf + GF_EPOCH) + 1u;
        STAMP(0, 8);
        float gval[2];
#pragma unroll
        for (int i = 0; i < 2; i++) {
            const int r = 2 * w + i;
            float x[8];
#pragma unroll
            for (int j = 0; j < 4; j++) { x[2 * j] = lo_f(xv[i][j]); x[2 * j + 1] = hi_f(xv[i][j]); }
            float sum = 0.f, gs = 0.f;
#pragma unroll
            for (int j = 0; j < 4; j++) {
                sum += x[2 * j] + x[2 * j + 1];
                u32 p = pk2bf(x[2 * j] * lo_f(gv[j]), x[2 * j + 1] * hi_f(gv[j]));
                gs += lo_f(p) + hi_f(p);
            }
            sum = wave_sum(sum);
            gs = wave_sum(gs);
            const float mean = sum * (1.f / D);
            float sq = 0.f;
#pragma unroll
            for (int j = 0; j < 8; j++) { float dl = x[j] - mean; sq = __builtin_fmaf(dl, dl, sq); }
            sq = wave_sum(sq);
            const float var = rbf(sq * (1.f / D));
            const float m = rbf(mean);
            const float sd = rbf(__builtin_sqrtf(rbf(var + 1e-5f)));
            const float rs = __builtin_amdgcn_rcpf(sd);
            u32x4 hv;
#pragma unroll
            for (int j = 0; j < 4; j++) {
                u32 c = pk2bf(x[2 * j] - m, x[2 * j + 1] - m);
                hv[j] = pk2bf(div_nr(lo_f(c), sd, rs), div_nr(hi_f(c), sd, rs));
            }
            const int c16 = lane ^ ((r & 3) << 2);
            *(u32x4*)(smem + STG + r * 1024 + c16 * 16) = hv;
            *(u32x4*)(smem + STG + 8192 + r * 1024 + c16 * 16) = xv[i];
            gval[i] = rbf(__builtin_amdgcn_rcpf(1.f + fast_exp(-rbf(gs))));
        }
        STAMP(0, 10);
        lds_arrive(&lf[LF_LN], lane);
        lds_wait(&lf[LF_LN], 4);
        STAMP(0, 11);
        u32x4 ukeep[2];
        size_t uoff[2];
        {
            const int nt = lane >> 4, q = (lane & 15) >> 2, p = lane & 3;
            const int ks = b >> 2, kq = b & 3;
#pragma unroll
            for (int i = 0; i < 2; i++) {
                const int dt = w * 2 + i;
                const int cb = dt * 4 + nt;
                const unsigned o0 = q * 1024 + ((cb ^ (q << 1)) << 5) + p * 8;
                const unsigned o1 = o0 + 4096;
                u32x2 h0 = tr_read(smem + STG, o0), h1 = tr_read(smem + STG, o1);
                u32x2 u0 = tr_read(smem + STG + 8192, o0), u1 = tr_read(smem + STG + 8192, o1);
                const size_t off = ((size_t)((dt * 64 + ks) * 4 + nt) * 64 + kq * 16 + (lane & 15)) * 8;
                st_sc1_16(hF + off, u32x4{h0[0], h0[1], h1[0], h1[1]});
                ukeep[i] = u32x4{u0[0], u0[1], u1[0], u1[1]};
                uoff[i] = off;
            }
        }
        STAMP(0, 12);
        wait_vm0<0>();
        STAMP(0, 13);
        lds_arrive(&lf[LF_LNPUB], lane);
        if (w == 0) {
            lds_wait(&lf[LF_LNPUB], 4);
            if (lane == 0) st_sc1_4(gf + GF_H + b, tag);
        }
        st_sc1_16(uF + uoff[0], ukeep[0]);
        st_sc1_16(uF + uoff[1], ukeep[1]);
        if (lane < 2) st_sc1_4(g + s0 + 2 * w + lane, __builtin_bit_cast(u32, lane ? gval[1] : gval[0]));
        lds_arrive(&lf[LF_LNST], lane);
        wait_vm0<0>();
        lds_arrive(&lf[LF_LNPUB2], lane);
        if (w == 1) {
            lds_wait(&lf[LF_LNPUB2], 4);
            if (lane == 0) st_sc1_4(gf + GF_H2 + b, tag);
        }
        __builtin_amdgcn_s_setprio(0);
        STAMP(0, 1);
        const bool has1 = li < 28;
        const int ftl = li % 7, dtl = li / 7;
        const int ft = (xcd >> 1) * 7 + ftl, dt = (xcd & 1) * 4 + dtl;
        u32x2 bb[4];
        GemmCtx<16, F * 2, true> c1;
        if (has1) {
#pragma unroll
            for (int mt = 0; mt < 4; mt++) gload_early8(bb[mt], b1 + ft * 64 + mt * 16 + (lane >> 4) * 4);
            gemm_begin(c1, smem, (const char*)w1 + ((size_t)(w * 512) * F + ft * 64) * 2,
                       (const char*)hF + ((size_t)(dt * 64 + w * 16)) * 4096, RING + w * 16384, (2 * ftl + 4 * dtl) & 15, lane);
        }
        if (w == 0) {
            poll_flags256(gf + GF_H, tag, lane);
            if (lane == 0) __hip_atomic_store(&lf[LF_H], 1u, __ATOMIC_RELAXED, __HIP_MEMORY_SCOPE_WORKGROUP);
        } else {
            lds_wait(&lf[LF_H], 1);
        }
        STAMP(0, 2);
        if (has1) {
            gemm_run(c1);
            STAMP(0, 3);
#pragma unroll
            for (int mt = 0; mt < 4; mt++) tie(bb[mt]);
            f32x4 own[4];
            {
                char* mine = smem + RING + w * 16384;
#define XCH(k) if (w == k) { \
                _Pragma("unroll") for (int nt = 0; nt < 4; nt++) _Pragma("unroll") for (int mt = 0; mt < 4; mt++) \
                    if (nt != k) *(f32x4*)(mine + (nt * 4 + mt) * 1024 + lane * 16) = c1.acc[mt][nt]; \
                _Pragma("unroll") for (int mt = 0; mt < 4; mt++) { own[mt] = c1.acc[mt][k]; asm volatile("" : "+v"(own[mt])); } }
                XCH(0) XCH(1) XCH(2) XCH(3)
#undef XCH
            }
            lds_arrive(&lf[LF_X1], lane);
            lds_wait(&lf[LF_X1], 4);
#pragma unroll
            for (int o = 1; o < 4; o++) {
                const int ow = (w + o) & 3;
#pragma unroll
                for (int mt = 0; mt < 4; mt++)
                    own[mt] += *(const f32x4*)(smem + RING + ow * 16384 + (w * 4 + mt) * 1024 + lane * 16);
            }
            lds_arrive(&lf[LF_X1R], lane);
            const int mq = lane >> 4, n = lane & 15;
#pragma unroll
            for (int mt = 0; mt < 4; mt++) {
                float v[4];
                {
                    u32 p0 = pk2bf(own[mt][0], own[mt][1]), p1 = pk2bf(own[mt][2], own[mt][3]);
                    u32 s0_ = pk2bf(lo_f(p0) + lo_f(bb[mt][0]), hi_f(p0) + hi_f(bb[mt][0]));
                    u32 s1_ = pk2bf(lo_f(p1) + lo_f(bb[mt][1]), hi_f(p1) + hi_f(bb[mt][1]));
                    v[0] = lo_f(s0_); v[1] = hi_f(s0_); v[2] = lo_f(s1_); v[3] = hi_f(s1_);
                }
#pragma unroll
                for (int i = 0; i < 4; i++) {
                    const float x = v[i];
                    const float p = __builtin_fmaf(x * x, -2.f * 0.7978845608028654f * 0.044715f * 1.4426950408889634f,
                                                   -2.f * 0.7978845608028654f * 1.4426950408889634f);
                    const float e = __builtin_amdgcn_exp2f(x * p);
                    v[i] = x * __builtin_amdgcn_rcpf(1.f + e);
                }
                u32x2 o = {pk2bf(v[0], v[1]), pk2bf(v[2], v[3])};
                const int ks = ft * 2 + (mt >> 1), kq = 2 * (mt & 1) + (mq >> 1);
                char* dst = (char*)UF + ((size_t)((dt * 56 + ks) * 4 + w) * 64 + kq * 16 + n) * 16 + (mq & 1) * 8;
                st_sc1_8(dst, o);
            }
            wait_vm0<0>();
            lds_arrive(&lf[LF_UPUB], lane);
            if (w == 0) {
                lds_wait(&lf[LF_UPUB], 4);
                if (lane == 0) st_sc1_4(gf + GF_U + dt * 32 + ft, tag);
            }
            lds_wait(&lf[LF_X1R], 4);
        }
        STAMP(0, 4);
        GemmCtx<14, L * 2, false> c2;
        gemm_begin(c2, smem, (const char*)w2 + ((size_t)(w * 448) * L + st * 64) * 2,
                   (const char*)UF + ((size_t)(dt2 * 56 + w * 14)) * 4096, RING + w * 16384, (2 * stl + 3 * dtl2) % 14, lane);
        if (w == 0) {
            poll_flags(gf + GF_U + dt2 * 32, 28, tag, lane);
            if (lane == 0) {
                __hip_atomic_store(&lf[LF_U], 1u, __ATOMIC_RELAXED, __HIP_MEMORY_SCOPE_WORKGROUP);
                if (b == 0) st_sc1_4(gf + GF_EPOCH, tag);
            }
        } else {
            lds_wait(&lf[LF_U], 1);
        }
        STAMP(0, 5);
        gemm_run(c2);
        STAMP(0, 6);
        f32x4 own[4];
        {
            char* mine = smem + RING + w * 16384;
#define XCH(k) if (w == k) { \
            _Pragma("unroll") for (int nt = 0; nt < 4; nt++) _Pragma("unroll") for (int mt = 0; mt < 4; mt++) \
                if (nt != k) *(f32x4*)(mine + (nt * 4 + mt) * 1024 + lane * 16) = c2.acc[mt][nt]; \
            _Pragma("unroll") for (int mt = 0; mt < 4; mt++) { own[mt] = c2.acc[mt][k]; asm volatile("" : "+v"(own[mt])); } }
            XCH(0) XCH(1) XCH(2) XCH(3)
#undef XCH
        }
        lds_arrive(&lf[LF_X2], lane);
        lds_wait(&lf[LF_X2], 4);
#pragma unroll
        for (int o = 1; o < 4; o++) {
            const int ow = (w + o) & 3;
#pragma unroll
            for (int mt = 0; mt < 4; mt++)
                own[mt] += *(const f32x4*)(smem + RING + ow * 16384 + (w * 4 + mt) * 1024 + lane * 16);
        }
        lds_wait(&lf[LF_EPI], 4);
        {
            const int i = lane & 15, mq = lane >> 4, q = i >> 2, p = lane & 3;
            const float gg = *(const float*)(smem + STG + 8192 + (w * 16 + i) * 4);
            const float bias = *(const float*)(smem + STG + 8192 + 256 + (w * 16 + i) * 4);
            const float og = rbf(1.f - gg);
            u16* dst = out + (size_t)(st * 64 + w * 16 + i) * D + dt2 * 64 + mq * 4;
#pragma unroll
            for (int mt = 0; mt < 4; mt++) {
                const int r0 = 16 * mt + 4 * mq + q;
                const int f0 = ((r0 >> 1) & 1) | (((r0 >> 3) & 1) << 1);
                const u32x2 av = tr_read(smem, STG + r0 * 128 + ((w ^ f0) << 5) + p * 8);
                u32x2 o;
#pragma unroll
                for (int e = 0; e < 2; e++) {
                    u32 y = pk2bf(own[mt][2 * e], own[mt][2 * e + 1]);
                    y = pk2bf(lo_f(y) + bias, hi_f(y) + bias);
                    u32 t1 = pk2bf(gg * lo_f(av[e]), gg * hi_f(av[e]));
                    u32 t2 = pk2bf(og * lo_f(y), og * hi_f(y));
                    o[e] = pk2bf(lo_f(t1) + lo_f(t2), hi_f(t1) + hi_f(t2));
                }
                *(u32x2*)(dst + mt * 16) = o;
            }
        }
        STAMP(0, 7);
        STAMP_DUMP;
        return;
    }

    const int wv = w - 4, ch = wv >> 1, hf = wv & 1;
    const int d = xcd * 64 + li * 2 + ch;
    const int n = hf * 64 + lane;
    u32 r_are, r_aim, r_bre, r_bim, r_cre, r_cim, r_ldt, r_dsk, r_ep;
    {
        const unsigned po = (unsigned)(d * NS + n) * 2u;
        asm volatile("global_load_ushort %0, %9, %10\n\tglobal_load_ushort %1, %9, %11\n\t"
                     "global_load_ushort %2, %9, %12\n\tglobal_load_ushort %3, %9, %13\n\t"
                     "global_load_ushort %4, %9, %14\n\tglobal_load_ushort %5, %9, %15\n\t"
                     "global_load_ushort %6, %16, off\n\tglobal_load_ushort %7, %17, off\n\t"
                     "global_load_dword %8, %18, off sc1"
                     : "=&v"(r_are), "=&v"(r_aim), "=&v"(r_bre), "=&v"(r_bim), "=&v"(r_cre), "=&v"(r_cim), "=&v"(r_ldt), "=&v"(r_dsk), "=&v"(r_ep)
                     : "v"(po), "s"(Are), "s"(Aim), "s"(Bre), "s"(Bim), "s"(Cre), "s"(Cim), "v"(logdt + d), "v"(Dskip + d), "v"(gf + GF_EPOCH)
                     : "memory");
    }
    u32 dummy = 0;
    {
        const char* base = (const char*)w1 + ((size_t)(li * 64) * F + (xcd >> 1) * 448) * 2;
#pragma unroll
        for (int i = 0; i < T1; i++) {
            int idx = (i * 4 + wv) * 64 + lane;
            idx = idx < 448 ? idx : 447;
            const int row = idx / 7, sec = idx - row * 7;
            touch(dummy, (unsigned)(row * (F * 2) + sec * 128), base);
        }
    }
    asm volatile("s_waitcnt vmcnt(" STR(T1) ")" : "+v"(r_are), "+v"(r_aim), "+v"(r_bre), "+v"(r_bim), "+v"(r_cre), "+v"(r_cim), "+v"(r_ldt), "+v"(r_dsk), "+v"(r_ep) :: "memory");
    const u32 tag = r_ep + 1u;
    const unsigned sb_off = SSM0 + ch * SSM_SIZE;
    char* sb = smem + sb_off;
    unsigned* fl = lf + LF_SSM + ch * 4;
    const float dt = rbf(fast_exp(lo_f(r_ldt)));
    const float zr = dt * lo_f(r_are), zi = dt * lo_f(r_aim);
    const float dsk = lo_f(r_dsk);
    f32x2 W;
    {
        const f32x2 B = {dt * lo_f(r_bre), dt * lo_f(r_bim)};
        const f32x2 C = {lo_f(r_cre), lo_f(r_cim)};
        W = cmul(C, B);
    }
    u32x4 kix[2][2];
#pragma unroll
    for (int i = 0; i < 2; i++) {
        const u32x4* pt = (const u32x4*)(KIDX.v + (hf * 128 + i * 64 + lane) * 16);
        kix[i][0] = pt[0]; kix[i][1] = pt[1];
    }
    STAMP(0, 1);
    char* tbl = sb + SSM_COPY + hf * TBL_SIZE;
    char* tw = tbl + lane * 8;
    const char* trd = tbl + (lane & 15) * TBL_PITCH + (lane >> 4) * 16;
    u32x4 Bh[8], Bl[8];
    f32x4 kacc[3];
    {
        f32x2 E8 = cexp_scaled(zr, zi, 8.f);
        E8.y = -E8.y;
        f32x2 pw[16];
        pw[0] = f32x2{1.f, 0.f};
        pw[1] = E8;
#pragma unroll
        for (int q = 2; q < 16; q++) pw[q] = (q & 1) ? cmul(pw[q - 1], E8) : cmul(pw[q / 2], pw[q / 2]);
#pragma unroll
        for (int q = 0; q < 16; q++) *(u32x2*)(tw + q * TBL_PITCH) = split_bf(pw[q]);
        wait_lgkm0();
#pragma unroll
        for (int T = 0; T < 8; T++) {
            u32x4 v = *(const u32x4*)(trd + T * 64);
            Bh[T] = u32x4{v[0], v[0], v[2], v[2]};
            Bl[T] = u32x4{v[1], v[1], v[3], v[3]};
        }
        wait_lgkm0();
    }
    if (T1 < 2) {
        lds_wait(&lf[LF_LNST], 4);
        const char* base = (const char*)w1 + ((size_t)(li * 64) * F + (xcd >> 1) * 448) * 2;
#pragma unroll
        for (int i = T1; i < 2; i++) {
            int idx = (i * 4 + wv) * 64 + lane;
            idx = idx < 448 ? idx : 447;
            const int row = idx / 7, sec = idx - row * 7;
            touch(dummy, (unsigned)(row * (F * 2) + sec * 128), base);
        }
    }
    {
        const f32x2 E1 = cexp_scaled(zr, zi, 1.f);
        const f32x2 E128 = cexp_scaled(zr, zi, 128.f);
        f32x2 WY[8];
        {
            f32x2 Y[8];
            Y[0] = f32x2{1.f, 0.f}; Y[1] = E1;
#pragma unroll
            for (int c = 2; c < 8; c++) Y[c] = (c & 1) ? cmul(Y[c - 1], E1) : cmul(Y[c / 2], Y[c / 2]);
            WY[0] = W;
#pragma unroll
            for (int c = 1; c < 8; c++) WY[c] = cmul(W, Y[c]);
        }
        f32x2 Xp[17];
        Xp[0] = f32x2{1.f, 0.f}; Xp[1] = E128;
#pragma unroll
        for (int a = 2; a < 17; a++) Xp[a] = (a & 1) ? cmul(Xp[a - 1], E128) : cmul(Xp[a / 2], Xp[a / 2]);
#pragma unroll
        for (int tile = 0; tile < 3; tile++) {
#pragma unroll
            for (int r = 0; r < 16; r++) {
                f32x2 v;
                bool valid = true;
                if (tile == 0) { v = (r < 8) ? WY[r] : cmul(WY[r - 8], Xp[1]); }
                else if (tile == 1) {
                    if (r < 8) v = cmul(WY[2 * (r & 3)], Xp[2 + (r >> 2)]);
                    else v = cmul(WY[4 * (r & 1)], Xp[4 + ((r - 8) >> 1)]);
                } else {
                    if (r < 8) v = cmul(W, Xp[8 + r]);
                    else if (r == 8) v = cmul(W, Xp[16]);
                    else valid = false;
                }
                if (valid) *(u32x2*)(tw + r * TBL_PITCH) = split_bf(v);
            }
            wait_lgkm0();
            f32x4 a4 = {0.f, 0.f, 0.f, 0.f};
#pragma unroll
            for (int T = 0; T < 8; T++) {
                u32x4 av = *(const u32x4*)(trd + T * 64);
                a4 = mfma16(av, Bh[T], a4);
                a4 = mfma16(av, Bl[T], a4);
            }
            kacc[tile] = a4;
            wait_lgkm0();
        }
    }
    STAMP(0, 2);
    {
        float* P = (float*)(sb + SSM_P + hf * 3456);
        const int mq = lane >> 4, col = lane & 15;
#pragma unroll
        for (int tile = 0; tile < 3; tile++)
#pragma unroll
            for (int r = 0; r < 4; r++)
                if (!(tile == 2 && r == 1 && 0)) P[(tile * 16 + mq * 4 + r) * 18 + col] = kacc[tile][r];
        wait_lgkm0();
        if (lane == 0) P[KC_ZERO * 18] = 0.f;
    }
    if (hf == 0 && lane < 32) *(u32x4*)(sb + SSM_U + lane * 16) = u32x4{0, 0, 0, 0};
    lds_arrive(&fl[0], lane);
    touch_done(dummy);
    {
        const char* base = (const char*)w2 + ((size_t)(li * 56) * L + (xcd >> 1) * 512) * 2;
#pragma unroll
        for (int i = 0; i < 2; i++) {
            const int idx = (wv * 2 + i) * 64 + lane;
            if (idx < 448) touch(dummy, (unsigned)((idx >> 3) * (L * 2) + (idx & 7) * 128), base);
        }
    }
    lds_wait(&fl[0], 2);
    STAMP(0, 3);
    {
        const char* P0 = sb + SSM_P;
        const char* P1 = sb + SSM_P + 3456;
#pragma unroll
        for (int i = 0; i < 2; i++) {
            const int ub = hf * 128 + i * 64 + lane;
            float v[11];
#pragma unroll
            for (int j = 0; j < 11; j++) {
                const u32 word = kix[i][j >> 3][(j >> 1) & 3];
                const unsigned off = (j & 1) ? (word >> 16) : (word & 0xffffu);
                v[j] = *(const float*)(P0 + off) + *(const float*)(P1 + off);
            }
            u32 e[5], o[5];
#pragma unroll
            for (int k = 0; k < 5; k++) { e[k] = pk2bf(v[2 * k], v[2 * k + 1]); o[k] = pk2bf(v[2 * k + 1], v[2 * k + 2]); }
            char* dst = sb + SSM_COPY + ub * 16;
            *(u32x4*)(dst) = u32x4{e[0], e[1], e[2], e[3]};
            *(u32x4*)(dst + CP_STRIDE) = u32x4{o[0], o[1], o[2], o[3]};
            *(u32x4*)(dst + 2 * CP_STRIDE) = u32x4{e[1], e[2], e[3], e[4]};
            *(u32x4*)(dst + 3 * CP_STRIDE) = u32x4{o[1], o[2], o[3], o[4]};
        }
        if (hf == 0 && lane < 8) {
            *(u32x4*)(sb + SSM_COPY + (lane >> 1) * CP_STRIDE + (256 + (lane & 1)) * 16) = u32x4{0, 0, 0, 0};
        }
    }
    STAMP(0, 4);
    if (wv == 0) {
        poll_flags256(gf + GF_H2, tag, lane);
        if (lane == 0) __hip_atomic_store(&lf[LF_SSM + 9], 1u, __ATOMIC_RELAXED, __HIP_MEMORY_SCOPE_WORKGROUP);
    } else {
        lds_wait(&lf[LF_SSM + 9], 1);
    }
    {
        const int dt_ = d >> 6, nt = (d >> 4) & 3, m = d & 15;
        const char* src = (const char*)uF + ((size_t)(dt_ * 64 * 4 + nt) * 64 + m) * 16;
        const int p0 = hf * 128 + lane, p1 = p0 + 64;
        u32x4 a, c;
        ld2_sc1_16(a, c, src + (p0 >> 2) * 4096 + (p0 & 3) * 256, src + (p1 >> 2) * 4096 + (p1 & 3) * 256);
        *(u32x4*)(sb + SSM_U + 512 + p0 * 16) = a;
        *(u32x4*)(sb + SSM_U + 512 + p1 * 16) = c;
    }
    lds_arrive(&fl[1], lane);
    lds_wait(&fl[1], 2);
    STAMP(0, 5);
    f32x4 cacc[8];
#pragma unroll
    for (int i = 0; i < 8; i++) cacc[i] = f32x4{0.f, 0.f, 0.f, 0.f};
    {
        const int m = lane & 15, q = lane >> 4;
        const int gq = m >> 2, e = m & 3, c = 3 - e;
        const char* abase = sb + SSM_COPY + c * CP_STRIDE + 4104 - 8 * gq - 16 * q - 64 * 63 + 64 * (7 - hf * 4 - 3);
        const char* bbase = sb + SSM_U + 528 + 32 * m - 16 * q - 64 * 7 + 64 * (7 - hf * 4 - 3);
#pragma unroll
        for (int rr = 0; rr < 4; rr++) {
            const char* ab = abase + 64 * (3 - rr);
            const char* bb = bbase + 64 * (3 - rr);
            u32x4 A[8], B[8];
#pragma unroll
            for (int pp = 0; pp < 8; pp++) {
                const u32x2 a0 = *(const u32x2*)(ab + 64 * (56 - 8 * pp));
                const u32x2 a1 = *(const u32x2*)(ab + 64 * (56 - 8 * pp) + 8);
                A[pp] = u32x4{a0[0], a0[1], a1[0], a1[1]};
                B[pp] = *(const u32x4*)(bb + 512 * pp);
            }
#pragma unroll
            for (int i = 0; i < 8; i++)
#pragma unroll
                for (int pp = 0; pp <= i; pp++) cacc[i] = mfma16(A[pp], B[i - pp], cacc[i]);
        }
    }
    STAMP(0, 6);
    f32x4 keep[4];
    {
        char* xo = sb + SSM_X + hf * 4096;
        if (hf) {
#pragma unroll
            for (int i = 0; i < 4; i++) { *(f32x4*)(xo + i * 1024 + lane * 16) = cacc[i]; keep[i] = cacc[4 + i]; asm volatile("" : "+v"(keep[i])); }
        } else {
#pragma unroll
            for (int i = 0; i < 4; i++) { *(f32x4*)(xo + i * 1024 + lane * 16) = cacc[4 + i]; keep[i] = cacc[i]; asm volatile("" : "+v"(keep[i])); }
        }
    }
    lds_arrive(&fl[2], lane);
    lds_wait(&fl[2], 2);
    {
        const char* xi = sb + SSM_X + (hf ^ 1) * 4096;
        const int nn = lane & 15, mq = lane >> 4;
#pragma unroll
        for (int i = 0; i < 4; i++) {
            f32x4 v = keep[i];
            v += *(const f32x4*)(xi + i * 1024 + lane * 16);
            const int t = 256 * (hf * 4 + i) + 16 * nn + 4 * mq;
            const u32x2 uu = *(const u32x2*)(sb + SSM_U + 512 + t * 2);
            u32 y0 = pk2bf(v[0], v[1]), y1 = pk2bf(v[2], v[3]);
            u32 s0 = pk2bf(lo_f(uu[0]) * dsk, hi_f(uu[0]) * dsk), s1 = pk2bf(lo_f(uu[1]) * dsk, hi_f(uu[1]) * dsk);
            u32x2 o = {pk2bf(lo_f(y0) + lo_f(s0), hi_f(y0) + hi_f(s0)), pk2bf(lo_f(y1) + lo_f(s1), hi_f(y1) + hi_f(s1))};
            st_sc1_8(aT + (size_t)d * L + t, o);
        }
    }
    touch_done(dummy);
    lds_arrive(&lf[LF_APUB], lane);
    if (wv == 0) {
        lds_wait(&lf[LF_APUB], 4);
        if (lane == 0) st_sc1_4(gf + GF_A + xcd * 32 + li, tag);
    }
    STAMP(0, 7);
    if (wv == 0) {
        poll_flags(gf + GF_A + dt2 * 32, 32, tag, lane);
        if (lane == 0) __hip_atomic_store(&lf[LF_SSM + 8], 1u, __ATOMIC_RELAXED, __HIP_MEMORY_SCOPE_WORKGROUP);
    } else {
        lds_wait(&lf[LF_SSM + 8], 1);
    }
    {
        const int r = 16 * wv + (lane >> 2);
        const int j0 = (lane & 3) * 2, j1 = j0 + 1;
        const char* src = (const char*)aT + ((size_t)(dt2 * 64 + r) * L + st * 64) * 2;
        u32x4 a, c;
        ld2_sc1_16(a, c, src + j0 * 16, src + j1 * 16);
        const int f = ((r >> 1) & 1) | (((r >> 3) & 1) << 1);
        *(u32x4*)(smem + STG + r * 128 + ((j0 ^ (f << 1)) << 4)) = a;
        *(u32x4*)(smem + STG + r * 128 + ((j1 ^ (f << 1)) << 4)) = c;
        if (lane < 16) {
            const int s = st * 64 + wv * 16 + lane;
            const u32 gv = ld_sc1_4(g + s);
            *(u32*)(smem + STG + 8192 + (wv * 16 + lane) * 4) = gv;
            *(float*)(smem + STG + 8192 + 256 + (wv * 16 + lane) * 4) = bf2f(b2[s]);
        }
    }
    lds_arrive(&lf[LF_EPI], lane);
    STAMP(0, 8);
    STAMP_DUMP;
}

extern "C" void kernel_launch(void* const* d_in, const int* in_sizes, int n_in,
                              void* d_out, int out_size, void* d_ws, size_t ws_size,
                              hipStream_t stream) {
    const u16* u = (const u16*)d_in[0];
    const u16* Are = (const u16*)d_in[1];
    const u16* Aim = (const u16*)d_in[2];
    const u16* Bre = (const u16*)d_in[3];
    const u16* Bim = (const u16*)d_in[4];
    const u16* Cre = (const u16*)d_in[5];
    const u16* Cim = (const u16*)d_in[6];
    const u16* logdt = (const u16*)d_in[7];
    const u16* Dskip = (const u16*)d_in[8];
    const u16* w1 = (const u16*)d_in[10];
    const u16* b1 = (const u16*)d_in[11];
    const u16* w2 = (const u16*)d_in[12];
    const u16* b2 = (const u16*)d_in[13];
    const u16* gw = (const u16*)d_in[14];
    k_fused<<<256, 512, 0, stream>>>(u, gw, Are, Aim, Bre, Bim, Cre, Cim, logdt, Dskip, w1, b1, w2, b2, (char*)d_ws, (u16*)d_out);
}
```

```cpp
#include <hip/hip_runtime.h>
#include <stdint.h>

typedef unsigned short u16;
typedef unsigned int u32;
using bf16x8 = __attribute__((ext_vector_type(8))) __bf16;
using bf16x2 = __attribute__((ext_vector_type(2))) __bf16;
using s16x4 = __attribute__((ext_vector_type(4))) short;
using f32x4 = __attribute__((ext_vector_type(4))) float;
using f32x2 = __attribute__((ext_vector_type(2))) float;
using u32x4 = __attribute__((ext_vector_type(4))) unsigned;
using u32x2 = __attribute__((ext_vector_type(2))) unsigned;
typedef __attribute__((address_space(3))) s16x4* lds_s16x4_ptr;

#if defined(__HIP_DEVICE_COMPILE__)
#define NOPK __attribute__((target("no-packed-fp32-ops")))
#else
#define NOPK
#endif
#define DEV NOPK __device__ __forceinline__
#define TIDX ((int)__builtin_amdgcn_workitem_id_x())
#define BIDX ((int)__builtin_amdgcn_workgroup_id_x())

#define STAMP_DECL do {} while (0)
#define STAMP(kern, idx) do {} while (0)
#define STAMP_DUMP do {} while (0)

constexpr int L = 2048, D = 512, NS = 128, F = 1792;
#define T1 1
#define TMODE 0
#define CODE_BYTES 30000
#define RK 1
#define ROT1 ((16 * 4 - RK * (ftl + dtl)) & 15)
#define ROT2 ((14 * 4 - RK * (stl + dtl2)) % 14)
#define STR_(x) #x
#define STR(x) STR_(x)

DEV float bf2f(u16 v) { return __builtin_bit_cast(float, ((unsigned)v) << 16); }
DEV u32 pk2bf(float a, float b) {
    f32x2 v = {a, b};
    return __builtin_bit_cast(u32, __builtin_convertvector(v, bf16x2));
}
DEV float lo_f(u32 p) { return __builtin_bit_cast(float, p << 16); }
DEV float hi_f(u32 p) { return __builtin_bit_cast(float, p & 0xffff0000u); }
DEV float rbf(float f) { return lo_f(pk2bf(f, 0.f)); }

DEV u32x2 tr_read(const char* lds_generic_base, unsigned off) {
    s16x4 v = __builtin_amdgcn_ds_read_tr16_b64_v4i16((lds_s16x4_ptr)(lds_generic_base + off));
    return __builtin_bit_cast(u32x2, v);
}
DEV f32x4 mfma16(u32x4 a, u32x4 b, f32x4 c) {
    return __builtin_amdgcn_mfma_f32_16x16x32_bf16(__builtin_bit_cast(bf16x8, a), __builtin_bit_cast(bf16x8, b), c, 0, 0, 0);
}

DEV void gload_early8(u32x2& dst, const void* p) { asm volatile("global_load_dwordx2 %0, %1, off" : "=v"(dst) : "v"(p) : "memory"); }
template <typename T> DEV void tie(T& x) { asm volatile("" : "+v"(x) :: "memory"); }
DEV void touch(u32& dummy, unsigned voff, const void* sbase) {
    asm volatile("global_load_dword %0, %1, %2" : "+v"(dummy) : "v"(voff), "s"(sbase) : "memory");
}
DEV void touch_done(u32& dummy) { asm volatile("s_waitcnt vmcnt(0)" : "+v"(dummy) :: "memory"); }
template <int N>
DEV void wait_vm(u32x4& a, u32x4& b, u32x4& c, u32x4& d) {
    asm volatile("s_waitcnt vmcnt(%4)" : "+v"(a), "+v"(b), "+v"(c), "+v"(d) : "i"(N) : "memory");
}
template <int N>
DEV void wait_vm0() { asm volatile("s_waitcnt vmcnt(%0)" ::"i"(N) : "memory"); }
DEV void wait_lgkm0() { asm volatile("s_waitcnt lgkmcnt(0)" ::: "memory"); }
DEV u32 ld_sc1_4(const void* p) {
    u32 v; asm volatile("global_load_dword %0, %1, off sc1\n\ts_waitcnt vmcnt(0)" : "=&v"(v) : "v"(p) : "memory"); return v;
}
DEV u32x4 ld_sc1_16(const void* p) {
    u32x4 v; asm volatile("global_load_dwordx4 %0, %1, off sc1\n\ts_waitcnt vmcnt(0)" : "=&v"(v) : "v"(p) : "memory"); return v;
}
DEV void ld2_sc1_16(u32x4& a, u32x4& b, const void* pa, const void* pb) {
    asm volatile("global_load_dwordx4 %0, %2, off sc1\n\tglobal_load_dwordx4 %1, %3, off sc1\n\ts_waitcnt vmcnt(0)"
                 : "=&v"(a), "=&v"(b) : "v"(pa), "v"(pb) : "memory");
}
DEV void st_sc1_16(void* p, u32x4 v) { asm volatile("global_store_dwordx4 %0, %1, off sc1\n\ts_nop 1" :: "v"(p), "v"(v) : "memory"); }
DEV void st_sc1_8(void* p, u32x2 v) { asm volatile("global_store_dwordx2 %0, %1, off sc1\n\ts_nop 1" :: "v"(p), "v"(v) : "memory"); }
DEV void st_sc1_4(void* p, u32 v) { asm volatile("global_store_dword %0, %1, off sc1" :: "v"(p), "v"(v) : "memory"); }

template <int CTRL>
DEV float dpp_add(float v) {
    int t = __builtin_amdgcn_update_dpp(0, __builtin_bit_cast(int, v), CTRL, 0xf, 0xf, true);
    return v + __builtin_bit_cast(float, t);
}
DEV float rdlane(float v, int l) { return __builtin_bit_cast(float, __builtin_amdgcn_readlane(__builtin_bit_cast(int, v), l)); }
DEV float wave_sum(float v) {
    v = dpp_add<0xB1>(v);
    v = dpp_add<0x4E>(v);
    v = dpp_add<0x141>(v);
    v = dpp_add<0x140>(v);
    return (rdlane(v, 0) + rdlane(v, 16)) + (rdlane(v, 32) + rdlane(v, 48));
}
DEV float fast_exp(float x) { return __builtin_amdgcn_exp2f(x * 1.4426950408889634f); }
DEV void lds_arrive(unsigned* cnt, int lane) {
    wait_lgkm0();
    if (lane == 0) __hip_atomic_fetch_add(cnt, 1u, __ATOMIC_RELAXED, __HIP_MEMORY_SCOPE_WORKGROUP);
}
DEV void lds_wait(unsigned* cnt, unsigned target) {
    while (__hip_atomic_load(cnt, __ATOMIC_RELAXED, __HIP_MEMORY_SCOPE_WORKGROUP) < target) __builtin_amdgcn_s_sleep(1);
    asm volatile("" ::: "memory");
}
constexpr int SPIN_CAP = 20000;
DEV void poll_flags256(const u32* fl, u32 tag, int lane) {
#pragma unroll 1
    for (int spin = 0; spin < SPIN_CAP; spin++) {
        u32x4 v = ld_sc1_16(fl + lane * 4);
        bool ok = v[0] == tag && v[1] == tag && v[2] == tag && v[3] == tag;
        if (__builtin_amdgcn_ballot_w64(ok) == ~0ull) break;
        __builtin_amdgcn_s_sleep(2);
    }
}
DEV void poll_flags(const u32* fl, int n, u32 tag, int lane) {
    const u32* p = fl + (lane < n ? lane : 0);
#pragma unroll 1
    for (int spin = 0; spin < SPIN_CAP; spin++) {
        u32 v = ld_sc1_4(p);
        if (__builtin_amdgcn_ballot_w64(v == tag) == ~0ull) break;
        __builtin_amdgcn_s_sleep(2);
    }
}
DEV float div_nr(float a, float b, float r) {
    float q = a * r;
    float e = __builtin_fmaf(-q, b, a);
    return __builtin_fmaf(e, r, q);
}

struct KIdxTable { u16 v[256 * 16]; };
constexpr int round_bf16_int(int x) {
    if (x < 256) return x;
    int sh = 0;
    while ((x >> sh) >= 256) sh++;
    int q = x >> sh, r = x & ((1 << sh) - 1), half = 1 << (sh - 1);
    if (r > half || (r == half && (q & 1))) q++;
    return q << sh;
}
constexpr int KC_ZERO = 41;
constexpr int compact_pos(int tap) {
    int row = KC_ZERO, col = 0;
    if (tap >= 0) {
        int lr = round_bf16_int(tap);
        if (lr < 256) { row = (lr & 7) + 8 * (lr >> 7); col = (lr >> 3) & 15; }
        else if (lr < 512) { int x = lr - 256; row = 16 + ((x & 7) >> 1) + 4 * (x >> 7); col = (x >> 3) & 15; }
        else if (lr < 1024) { int x = lr - 512; row = 24 + ((x & 7) >> 2) + 2 * (x >> 7); col = (x >> 3) & 15; }
        else if (lr < 2048) { int x = lr - 1024; row = 32 + (x >> 7); col = (x >> 3) & 15; }
        else { row = 40; col = 0; }
    }
    return row * 18 + col;
}
constexpr KIdxTable make_kidx() {
    KIdxTable t{};
    for (int ub = 0; ub < 256; ub++)
        for (int i = 0; i < 16; i++) t.v[ub * 16 + i] = (u16)(4 * compact_pos(2047 - (8 * ub + i)));
    return t;
}
__device__ const KIdxTable KIDX = make_kidx();

DEV f32x2 cmul(f32x2 a, f32x2 b) {
    f32x2 r;
    r.x = __builtin_fmaf(-a.y, b.y, a.x * b.x);
    r.y = __builtin_fmaf(a.y, b.x, a.x * b.y);
    return r;
}
DEV f32x2 cexp_scaled(float zr, float zi, float l) {
    const float xr = zr * l, xi = zi * l;
    const float e = __builtin_amdgcn_exp2f(xr * 1.4426950408889634f);
    const float c_hi = 0.15915494309189535f;
    const float c_lo = 6.4206382e-09f;
    float rh = xi * c_hi;
    float rl = __builtin_fmaf(xi, c_lo, __builtin_fmaf(xi, c_hi, -rh));
    float fr = (rh - __builtin_rintf(rh)) + rl;
    return f32x2{e * __builtin_amdgcn_cosf(fr), e * __builtin_amdgcn_sinf(fr)};
}
DEV u32x2 split_bf(f32x2 v) {
    u32 h = pk2bf(v.x, v.y);
    u32 l = pk2bf(v.x - lo_f(h), v.y - hi_f(h));
    return u32x2{h, l};
}

DEV void dma_slot(unsigned lds_, const unsigned (&voff)[4], const void* sbase) {
    const unsigned lds = (unsigned)__builtin_amdgcn_readfirstlane((int)lds_);
    asm volatile("s_mov_b32 m0, %0\n\ts_nop 0\n\t"
                 "global_load_lds_dwordx4 %1, %5\n\t"
                 "global_load_lds_dwordx4 %2, %5 offset:1024\n\t"
                 "global_load_lds_dwordx4 %3, %5 offset:2048\n\t"
                 "global_load_lds_dwordx4 %4, %5 offset:3072"
                 :: "s"(lds), "v"(voff[0]), "v"(voff[1]), "v"(voff[2]), "v"(voff[3]), "s"(sbase) : "memory", "m0");
}
DEV void xload(u32x4 (&x)[4], unsigned voff, const void* sbase) {
    asm volatile("global_load_dwordx4 %0, %4, %5 sc1\n\t"
                 "global_load_dwordx4 %1, %4, %5 offset:1024 sc1\n\t"
                 "global_load_dwordx4 %2, %4, %5 offset:2048 sc1\n\t"
                 "global_load_dwordx4 %3, %4, %5 offset:3072 sc1"
                 : "+v"(x[0]), "+v"(x[1]), "+v"(x[2]), "+v"(x[3]) : "v"(voff), "s"(sbase) : "memory");
}
template <int S, int WPITCH, bool W_IS_A>
struct GemmCtx {
    const char* smem;
    const char* wbase;
    const char* xbase;
    unsigned ring;
    int nxt, nxtx;
    unsigned wvoff[4];
    unsigned xvoff;
    unsigned raddr[4];
    u32x4 X[4][4];
    f32x4 acc[4][4];
};
template <int J, int S, int WPITCH, bool W_IS_A>
DEV void g_issue_w(GemmCtx<S, WPITCH, W_IS_A>& c) {
    dma_slot(c.ring + J * 4096, c.wvoff, c.wbase + (size_t)((unsigned)c.nxt * (unsigned)(32 * WPITCH)));
    c.nxt = (c.nxt + 1 == S) ? 0 : c.nxt + 1;
}
template <int J, int S, int WPITCH, bool W_IS_A>
DEV void g_issue_x(GemmCtx<S, WPITCH, W_IS_A>& c) {
    xload(c.X[J], c.xvoff, c.xbase + (size_t)((unsigned)c.nxtx * 4096u));
    c.nxtx = (c.nxtx + 1 == S) ? 0 : c.nxtx + 1;
}
template <int J, int N, bool IX, bool IW, int S, int WPITCH, bool W_IS_A>
DEV void g_step(GemmCtx<S, WPITCH, W_IS_A>& c) {
    u32x4(&x)[4] = c.X[J];
    wait_vm<N>(x[0], x[1], x[2], x[3]);
    u32x4 Wf[4];
#pragma unroll
    for (int i = 0; i < 4; i++) {
        u32x2 a = tr_read(c.smem, c.raddr[i] + J * 4096);
        u32x2 b = tr_read(c.smem, c.raddr[i] + J * 4096 + 512);
        Wf[i] = u32x4{a[0], a[1], b[0], b[1]};
    }
    if constexpr (IX) g_issue_x<(J + 3) & 3>(c);
    if constexpr (IW) {
        asm volatile("s_waitcnt lgkmcnt(0)" : "+v"(Wf[0]), "+v"(Wf[1]), "+v"(Wf[2]), "+v"(Wf[3]) :: "memory");
        g_issue_w<J>(c);
    }
#pragma unroll
    for (int i = 0; i < 4; i++)
#pragma unroll
        for (int j = 0; j < 4; j++) {
            if constexpr (W_IS_A) c.acc[i][j] = mfma16(Wf[i], x[j], c.acc[i][j]);
            else c.acc[i][j] = mfma16(x[i], Wf[j], c.acc[i][j]);
        }
    __builtin_amdgcn_sched_barrier(0);
}
template <int S, int WPITCH, bool W_IS_A>
DEV void gemm_begin(GemmCtx<S, WPITCH, W_IS_A>& c, const char* smem, const char* wbase0, size_t wstride, const char* xbase, unsigned ring0, int w, int rot, int lane) {
    c.smem = smem; c.wbase = wbase0 + w * wstride; c.xbase = xbase; c.ring = ring0 + w * 16384; c.nxt = rot; c.nxtx = rot;
#pragma unroll
    for (int i = 0; i < 4; i++) {
        const int sw = (((lane >> 4) & 1) << 1) | ((i & 1) << 2);
        c.wvoff[i] = (unsigned)((8 * i + (lane >> 3)) * WPITCH + (((lane & 7) ^ sw) << 4) - 1024 * i);
    }
    c.xvoff = lane * 16;
    const int g = lane >> 4, q = (lane & 15) >> 2, p = lane & 3;
    const int fl = ((q >> 1) & 1) | ((g & 1) << 1);
#pragma unroll
    for (int i = 0; i < 4; i++) c.raddr[i] = c.ring + (8 * g + q) * 128 + ((i ^ fl) << 5) + p * 8;
    if (w == 0) {
        int k = rot + 4;
        c.nxt = k >= S ? k - S : k;
    } else {
        if (w == 1) {
            int k = rot;
#pragma unroll
            for (int j = 0; j < 4; j++) {
                dma_slot(ring0 + j * 4096, c.wvoff, wbase0 + (size_t)((unsigned)k * (unsigned)(32 * WPITCH)));
                k = (k + 1 == S) ? 0 : k + 1;
            }
        }
        g_issue_w<0>(c); g_issue_w<1>(c); g_issue_w<2>(c); g_issue_w<3>(c);
    }
}
template <int S, int WPITCH, bool W_IS_A>
DEV void gemm_run(GemmCtx<S, WPITCH, W_IS_A>& c, unsigned* w0flag, unsigned w0val, int w
) {
#pragma unroll
    for (int i = 0; i < 4; i++)
#pragma unroll
        for (int j = 0; j < 4; j++) { c.acc[i][j] = f32x4{0.f, 0.f, 0.f, 0.f}; c.X[i][j] = u32x4{0, 0, 0, 0}; }
    g_issue_x<0>(c); g_issue_x<1>(c); g_issue_x<2>(c);
    if (w == 0) lds_wait(w0flag, w0val);
    g_step<0, 8, true, true>(c);
    g_step<1, 12, true, true>(c);
    g_step<2, 16, true, true>(c);
    g_step<3, 20, true, true>(c);
    constexpr int STEADY = S - 8;
#pragma unroll 1
    for (int it = 0; it < STEADY / 4; it++) {
        g_step<0, 20, true, true>(c);
        g_step<1, 20, true, true>(c);
        g_step<2, 20, true, true>(c);
        g_step<3, 20, true, true>(c);
    }
    constexpr int R = STEADY % 4;
    if constexpr (R >= 1) g_step<0, 20, true, true>(c);
    if constexpr (R >= 2) g_step<1, 20, true, true>(c);
    if constexpr (R >= 3) g_step<2, 20, true, true>(c);
    g_step<(R + 0) & 3, 20, true, false>(c);
    g_step<(R + 1) & 3, 16, false, false>(c);
    g_step<(R + 2) & 3, 8, false, false>(c);
    g_step<(R + 3) & 3, 0, false, false>(c);
}

constexpr int RING = 0;
constexpr int STG = 65536;
constexpr int SSM0 = STG + 16384;
constexpr int TBL_PITCH = 528;
constexpr int TBL_SIZE = 16 * TBL_PITCH;
constexpr int CP_STRIDE = 4160;
constexpr int SSM_COPY = 0;
constexpr int SSM_U = 16896;
constexpr int SSM_P = SSM_U + 4608;
constexpr int SSM_X = SSM_P + 2 * 3456;
constexpr int SSM_SIZE = SSM_X + 8192;
constexpr int LFLAGS = SSM0 + 2 * SSM_SIZE;
constexpr int LDS_BYTES = LFLAGS + 128;
enum { LF_LN = 0, LF_LNPUB = 1, LF_H = 2, LF_X1 = 3, LF_X1R = 4, LF_UPUB = 5, LF_U = 6, LF_X2 = 7, LF_EPI = 8, LF_APUB = 9, LF_LNST = 10, LF_LNPUB2 = 11, LF_UARR = 24, LF_W0 = 25, LF_SSM = 12   };
constexpr int GF_EPOCH = 0, GF_H = 64, GF_H2 = 64 + 256, GF_U = 64 + 512, GF_A = 64 + 512 + 8 * 32;

NOPK __global__ __launch_bounds__(512) void k_fused(const u16* __restrict__ u, const u16* __restrict__ gw,
                                                    const u16* __restrict__ Are, const u16* __restrict__ Aim,
                                                    const u16* __restrict__ Bre, const u16* __restrict__ Bim,
                                                    const u16* __restrict__ Cre, const u16* __restrict__ Cim,
                                                    const u16* __restrict__ logdt, const u16* __restrict__ Dskip,
                                                    const u16* __restrict__ w1, const u16* __restrict__ b1,
                                                    const u16* __restrict__ w2, const u16* __restrict__ b2,
                                                    char* __restrict__ ws, u16* __restrict__ out) {
    __shared__ __attribute__((aligned(16))) char smem[LDS_BYTES];
    const int tid = TIDX, lane = tid & 63;
    const int w = __builtin_amdgcn_readfirstlane(tid >> 6);
    const int b = BIDX;
    const int xcd = b & 7, li = b >> 3;
    unsigned* lf = (unsigned*)(smem + LFLAGS);
    u16* hF = (u16*)ws;
    u16* uF = (u16*)(ws + (2 << 20));
    u16* aT = (u16*)(ws + (4 << 20));
    u16* UF = (u16*)(ws + (6 << 20));
    float* g = (float*)(ws + (8 << 20));
    u32* gf = (u32*)(ws + (9 << 20));
    const int stl = li & 7, dtl2 = li >> 3;
    const int st = (xcd >> 1) * 8 + stl, dt2 = (xcd & 1) * 4 + dtl2;
    STAMP_DECL;
    STAMP(0, 0);
    u32 cdummy = 0;
    if (w == 7) {
        const unsigned long long pc = __builtin_amdgcn_s_getpc() & ~127ull;
        int idx = li * 8 + lane;
        if (lane < 8 && idx * 128 < CODE_BYTES) touch(cdummy, (unsigned)(idx * 128), (const void*)pc);
    }
    if (tid < 32) lf[tid] = 0;
    wait_lgkm0();
    __builtin_amdgcn_s_barrier();

    if (w < 4) {
        __builtin_amdgcn_s_setprio(3);
        const int s0 = b * 8;
        u32x4 xv[2];
#pragma unroll
        for (int i = 0; i < 2; i++) xv[i] = *(const u32x4*)(u + (size_t)(s0 + 2 * w + i) * D + lane * 8);
        const u32x4 gv = *(const u32x4*)(gw + lane * 8);
        const u32 tag = ld_sc1_4(gf + GF_EPOCH) + 1u;
        STAMP(0, 8);
        lds_arrive(&lf[LF_UARR], lane);
        float gval[2];
#pragma unroll
        for (int i = 0; i < 2; i++) {
            const int r = 2 * w + i;
            float x[8];
#pragma unroll
            for (int j = 0; j < 4; j++) { x[2 * j] = lo_f(xv[i][j]); x[2 * j + 1] = hi_f(xv[i][j]); }
            float sum = 0.f, gs = 0.f;
#pragma unroll
            for (int j = 0; j < 4; j++) {
                sum += x[2 * j] + x[2 * j + 1];
                u32 p = pk2bf(x[2 * j] * lo_f(gv[j]), x[2 * j + 1] * hi_f(gv[j]));
                gs += lo_f(p) + hi_f(p);
            }
            sum = wave_sum(sum);
            gs = wave_sum(gs);
            const float mean = sum * (1.f / D);
            float sq = 0.f;
#pragma unroll
            for (int j = 0; j < 8; j++) { float dl = x[j] - mean; sq = __builtin_fmaf(dl, dl, sq); }
            sq = wave_sum(sq);
            const float var = rbf(sq * (1.f / D));
            const float m = rbf(mean);
            const float sd = rbf(__builtin_sqrtf(rbf(var + 1e-5f)));
            const float rs = __builtin_amdgcn_rcpf(sd);
            u32x4 hv;
#pragma unroll
            for (int j = 0; j < 4; j++) {
                u32 c = pk2bf(x[2 * j] - m, x[2 * j + 1] - m);
                hv[j] = pk2bf(div_nr(lo_f(c), sd, rs), div_nr(hi_f(c), sd, rs));
            }
            const int c16 = lane ^ ((r & 3) << 2);
            *(u32x4*)(smem + STG + r * 1024 + c16 * 16) = hv;
            *(u32x4*)(smem + STG + 8192 + r * 1024 + c16 * 16) = xv[i];
            gval[i] = rbf(__builtin_amdgcn_rcpf(1.f + fast_exp(-rbf(gs))));
        }
        STAMP(0, 10);
        lds_arrive(&lf[LF_LN], lane);
        lds_wait(&lf[LF_LN], 4);
        STAMP(0, 11);
        u32x4 ukeep[2];
        size_t uoff[2];
        {
            const int nt = lane >> 4, q = (lane & 15) >> 2, p = lane & 3;
            const int ks = b >> 2, kq = b & 3;
#pragma unroll
            for (int i = 0; i < 2; i++) {
                const int dt = w * 2 + i;
                const int cb = dt * 4 + nt;
                const unsigned o0 = q * 1024 + ((cb ^ (q << 1)) << 5) + p * 8;
                const unsigned o1 = o0 + 4096;
                u32x2 h0 = tr_read(smem + STG, o0), h1 = tr_read(smem + STG, o1);
                u32x2 u0 = tr_read(smem + STG + 8192, o0), u1 = tr_read(smem + STG + 8192, o1);
                const size_t off = ((size_t)((dt * 64 + ks) * 4 + nt) * 64 + kq * 16 + (lane & 15)) * 8;
                st_sc1_16(hF + off, u32x4{h0[0], h0[1], h1[0], h1[1]});
                ukeep[i] = u32x4{u0[0], u0[1], u1[0], u1[1]};
                uoff[i] = off;
            }
        }
        STAMP(0, 12);
        wait_vm0<0>();
        STAMP(0, 13);
        lds_arrive(&lf[LF_LNPUB], lane);
        if (w == 0) {
            lds_wait(&lf[LF_LNPUB], 4);
            if (lane == 0) st_sc1_4(gf + GF_H + b, tag);
            lds_arrive(&lf[LF_LNPUB], lane);
        } else {
            lds_wait(&lf[LF_LNPUB], 5);
        }
        st_sc1_16(uF + uoff[0], ukeep[0]);
        st_sc1_16(uF + uoff[1], ukeep[1]);
        if (lane < 2) st_sc1_4(g + s0 + 2 * w + lane, __builtin_bit_cast(u32, lane ? gval[1] : gval[0]));
        lds_arrive(&lf[LF_LNST], lane);
        __builtin_amdgcn_s_setprio(0);
        STAMP(0, 1);
        const bool has1 = li < 28;
        const int ftl = li % 7, dtl = li / 7;
        const int ft = (xcd >> 1) * 7 + ftl, dt = (xcd & 1) * 4 + dtl;
        u32x2 bb[4];
        GemmCtx<16, F * 2, true> c1;
        wait_vm0<0>();
        lds_arrive(&lf[LF_LNPUB2], lane);
        if (w == 1) {
            lds_wait(&lf[LF_LNPUB2], 4);
            if (lane == 0) st_sc1_4(gf + GF_H2 + b, tag);
            lds_arrive(&lf[LF_LNPUB2], lane);
        } else {
            lds_wait(&lf[LF_LNPUB2], 5);
        }
        if (has1) {
#pragma unroll
            for (int mt = 0; mt < 4; mt++) gload_early8(bb[mt], b1 + ft * 64 + mt * 16 + (lane >> 4) * 4);
            gemm_begin(c1, smem, (const char*)w1 + ((size_t)ft * 64) * 2, (size_t)512 * F * 2,
                       (const char*)hF + ((size_t)(dt * 64 + w * 16)) * 4096, RING, w, ROT1, lane);
        }
        if (w == 0) {
            poll_flags256(gf + GF_H, tag, lane);
            if (lane == 0) __hip_atomic_store(&lf[LF_H], 1u, __ATOMIC_RELAXED, __HIP_MEMORY_SCOPE_WORKGROUP);
        } else {
            lds_wait(&lf[LF_H], 1);
        }
        STAMP(0, 2);
        if (has1) {
            if (w == 1) { wait_vm0<16>(); if (lane == 0) __hip_atomic_store(&lf[LF_W0], 1u, __ATOMIC_RELAXED, __HIP_MEMORY_SCOPE_WORKGROUP); }
            gemm_run(c1, &lf[LF_W0], 1u, w);
            STAMP(0, 3);
#pragma unroll
            for (int mt = 0; mt < 4; mt++) tie(bb[mt]);
            f32x4 own[4];
            {
                char* mine = smem + RING + w * 16384;
#define XCH(k) if (w == k) { \
                _Pragma("unroll") for (int nt = 0; nt < 4; nt++) _Pragma("unroll") for (int mt = 0; mt < 4; mt++) \
                    if (nt != k) *(f32x4*)(mine + (nt * 4 + mt) * 1024 + lane * 16) = c1.acc[mt][nt]; \
                _Pragma("unroll") for (int mt = 0; mt < 4; mt++) { own[mt] = c1.acc[mt][k]; asm volatile("" : "+v"(own[mt])); } }
                XCH(0) XCH(1) XCH(2) XCH(3)
#undef XCH
            }
            lds_arrive(&lf[LF_X1], lane);
            lds_wait(&lf[LF_X1], 4);
            STAMP(0, 9);
#pragma unroll
            for (int o = 1; o < 4; o++) {
                const int ow = (w + o) & 3;
#pragma unroll
                for (int mt = 0; mt < 4; mt++)
                    own[mt] += *(const f32x4*)(smem + RING + ow * 16384 + (w * 4 + mt) * 1024 + lane * 16);
            }
            lds_arrive(&lf[LF_X1R], lane);
            const int mq = lane >> 4, n = lane & 15;
#pragma unroll
            for (int mt = 0; mt < 4; mt++) {
                float v[4];
                {
                    u32 p0 = pk2bf(own[mt][0], own[mt][1]), p1 = pk2bf(own[mt][2], own[mt][3]);
                    u32 s0_ = pk2bf(lo_f(p0) + lo_f(bb[mt][0]), hi_f(p0) + hi_f(bb[mt][0]));
                    u32 s1_ = pk2bf(lo_f(p1) + lo_f(bb[mt][1]), hi_f(p1) + hi_f(bb[mt][1]));
                    v[0] = lo_f(s0_); v[1] = hi_f(s0_); v[2] = lo_f(s1_); v[3] = hi_f(s1_);
                }
#pragma unroll
                for (int i = 0; i < 4; i++) {
                    const float x = v[i];
                    const float p = __builtin_fmaf(x * x, -2.f * 0.7978845608028654f * 0.044715f * 1.4426950408889634f,
                                                   -2.f * 0.7978845608028654f * 1.4426950408889634f);
                    const float e = __builtin_amdgcn_exp2f(x * p);
                    v[i] = x * __builtin_amdgcn_rcpf(1.f + e);
                }
                u32x2 o = {pk2bf(v[0], v[1]), pk2bf(v[2], v[3])};
                const int ks = ft * 2 + (mt >> 1), kq = 2 * (mt & 1) + (mq >> 1);
                char* dst = (char*)UF + ((size_t)((dt * 56 + ks) * 4 + w) * 64 + kq * 16 + n) * 16 + (mq & 1) * 8;
                st_sc1_8(dst, o);
            }
            STAMP(0, 14);
            wait_vm0<0>();
            STAMP(0, 15);
            lds_arrive(&lf[LF_UPUB], lane);
            if (w == 0) {
                lds_wait(&lf[LF_UPUB], 4);
                if (lane == 0) st_sc1_4(gf + GF_U + dt * 32 + ft, tag);
                lds_arrive(&lf[LF_UPUB], lane);
            } else {
                lds_wait(&lf[LF_UPUB], 5);
            }
            lds_wait(&lf[LF_X1R], 4);
        }
        STAMP(0, 4);
        GemmCtx<14, L * 2, false> c2;
        gemm_begin(c2, smem, (const char*)w2 + ((size_t)st * 64) * 2, (size_t)448 * L * 2,
                   (const char*)UF + ((size_t)(dt2 * 56 + w * 14)) * 4096, RING, w, ROT2, lane);
        if (w == 0) {
            poll_flags(gf + GF_U + dt2 * 32, 28, tag, lane);
            if (lane == 0) {
                __hip_atomic_store(&lf[LF_U], 1u, __ATOMIC_RELAXED, __HIP_MEMORY_SCOPE_WORKGROUP);
                if (b == 0) st_sc1_4(gf + GF_EPOCH, tag);
            }
        } else {
            lds_wait(&lf[LF_U], 1);
        }
        STAMP(0, 5);
        if (w == 1) { wait_vm0<16>(); if (lane == 0) __hip_atomic_store(&lf[LF_W0], 2u, __ATOMIC_RELAXED, __HIP_MEMORY_SCOPE_WORKGROUP); }
        gemm_run(c2, &lf[LF_W0], 2u, w);
        STAMP(0, 6);
        f32x4 own[4];
        {
            char* mine = smem + RING + w * 16384;
#define XCH(k) if (w == k) { \
            _Pragma("unroll") for (int nt = 0; nt < 4; nt++) _Pragma("unroll") for (int mt = 0; mt < 4; mt++) \
                if (nt != k) *(f32x4*)(mine + (nt * 4 + mt) * 1024 + lane * 16) = c2.acc[mt][nt]; \
            _Pragma("unroll") for (int mt = 0; mt < 4; mt++) { own[mt] = c2.acc[mt][k]; asm volatile("" : "+v"(own[mt])); } }
            XCH(0) XCH(1) XCH(2) XCH(3)
#undef XCH
        }
        lds_arrive(&lf[LF_X2], lane);
        lds_wait(&lf[LF_X2], 4);
#pragma unroll
        for (int o = 1; o < 4; o++) {
            const int ow = (w + o) & 3;
#pragma unroll
            for (int mt = 0; mt < 4; mt++)
                own[mt] += *(const f32x4*)(smem + RING + ow * 16384 + (w * 4 + mt) * 1024 + lane * 16);
        }
        lds_wait(&lf[LF_EPI], 4);
        {
            const int i = lane & 15, mq = lane >> 4, q = i >> 2, p = lane & 3;
            const float gg = *(const float*)(smem + STG + 8192 + (w * 16 + i) * 4);
            const float bias = *(const float*)(smem + STG + 8192 + 256 + (w * 16 + i) * 4);
            const float og = rbf(1.f - gg);
            u16* dst = out + (size_t)(st * 64 + w * 16 + i) * D + dt2 * 64 + mq * 4;
#pragma unroll
            for (int mt = 0; mt < 4; mt++) {
                const int r0 = 16 * mt + 4 * mq + q;
                const int f0 = ((r0 >> 1) & 1) | (((r0 >> 3) & 1) << 1);
                const u32x2 av = tr_read(smem, STG + r0 * 128 + ((w ^ f0) << 5) + p * 8);
                u32x2 o;
#pragma unroll
                for (int e = 0; e < 2; e++) {
                    u32 y = pk2bf(own[mt][2 * e], own[mt][2 * e + 1]);
                    y = pk2bf(lo_f(y) + bias, hi_f(y) + bias);
                    u32 t1 = pk2bf(gg * lo_f(av[e]), gg * hi_f(av[e]));
                    u32 t2 = pk2bf(og * lo_f(y), og * hi_f(y));
                    o[e] = pk2bf(lo_f(t1) + lo_f(t2), hi_f(t1) + hi_f(t2));
                }
                *(u32x2*)(dst + mt * 16) = o;
            }
        }
        STAMP(0, 7);
        STAMP_DUMP;
        return;
    }

    const int wv = w - 4, ch = wv >> 1, hf = wv & 1;
    const int d = xcd * 64 + li * 2 + ch;
    const int n = hf * 64 + lane;
    u32 r_are, r_aim, r_bre, r_bim, r_cre, r_cim, r_ldt, r_dsk, r_ep;
    {
        const unsigned po = (unsigned)(d * NS + n) * 2u;
        asm volatile("global_load_ushort %0, %9, %10\n\tglobal_load_ushort %1, %9, %11\n\t"
                     "global_load_ushort %2, %9, %12\n\tglobal_load_ushort %3, %9, %13\n\t"
                     "global_load_ushort %4, %9, %14\n\tglobal_load_ushort %5, %9, %15\n\t"
                     "global_load_ushort %6, %16, off\n\tglobal_load_ushort %7, %17, off\n\t"
                     "global_load_dword %8, %18, off sc1"
                     : "=&v"(r_are), "=&v"(r_aim), "=&v"(r_bre), "=&v"(r_bim), "=&v"(r_cre), "=&v"(r_cim), "=&v"(r_ldt), "=&v"(r_dsk), "=&v"(r_ep)
                     : "v"(po), "s"(Are), "s"(Aim), "s"(Bre), "s"(Bim), "s"(Cre), "s"(Cim), "v"(logdt + d), "v"(Dskip + d), "v"(gf + GF_EPOCH)
                     : "memory");
    }
    u32 dummy = 0;
    {
        const char* base = (const char*)w1 + ((size_t)(li * 64) * F + (xcd >> 1) * 448) * 2;
#pragma unroll
        for (int i = 0; i < T1; i++) {
            int idx = (i * 4 + wv) * 64 + lane;
            idx = idx < 448 ? idx : 447;
            const int row = idx / 7, sec = idx - row * 7;
            touch(dummy, (unsigned)(row * (F * 2) + sec * 128), base);
        }
    }
    asm volatile("s_waitcnt vmcnt(" STR(T1) ")" : "+v"(r_are), "+v"(r_aim), "+v"(r_bre), "+v"(r_bim), "+v"(r_cre), "+v"(r_cim), "+v"(r_ldt), "+v"(r_dsk), "+v"(r_ep) :: "memory");
    const u32 tag = r_ep + 1u;
    const unsigned sb_off = SSM0 + ch * SSM_SIZE;
    char* sb = smem + sb_off;
    unsigned* fl = lf + LF_SSM + ch * 4;
    const float dt = rbf(fast_exp(lo_f(r_ldt)));
    const float zr = dt * lo_f(r_are), zi = dt * lo_f(r_aim);
    const float dsk = lo_f(r_dsk);
    f32x2 W;
    {
        const f32x2 B = {dt * lo_f(r_bre), dt * lo_f(r_bim)};
        const f32x2 C = {lo_f(r_cre), lo_f(r_cim)};
        W = cmul(C, B);
    }
    u32x4 kix[2][2];
#pragma unroll
    for (int i = 0; i < 2; i++) {
        const u32x4* pt = (const u32x4*)(KIDX.v + (hf * 128 + i * 64 + lane) * 16);
        kix[i][0] = pt[0]; kix[i][1] = pt[1];
    }
    if (T1 < 2 && TMODE == 1) {
        lds_wait(&lf[LF_UARR], 4);
        const char* base = (const char*)w1 + ((size_t)(li * 64) * F + (xcd >> 1) * 448) * 2;
#pragma unroll
        for (int i = T1; i < 2; i++) {
            int idx = (i * 4 + wv) * 64 + lane;
            idx = idx < 448 ? idx : 447;
            const int row = idx / 7, sec = idx - row * 7;
            touch(dummy, (unsigned)(row * (F * 2) + sec * 128), base);
        }
    }
    STAMP(0, 1);
    char* tbl = sb + SSM_COPY + hf * TBL_SIZE;
    char* tw = tbl + lane * 8;
    const char* trd = tbl + (lane & 15) * TBL_PITCH + (lane >> 4) * 16;
    u32x4 Bh[8], Bl[8];
    f32x4 kacc[3];
    {
        f32x2 E8 = cexp_scaled(zr, zi, 8.f);
        E8.y = -E8.y;
        f32x2 pw[16];
        pw[0] = f32x2{1.f, 0.f};
        pw[1] = E8;
#pragma unroll
        for (int q = 2; q < 16; q++) pw[q] = (q & 1) ? cmul(pw[q - 1], E8) : cmul(pw[q / 2], pw[q / 2]);
#pragma unroll
        for (int q = 0; q < 16; q++) *(u32x2*)(tw + q * TBL_PITCH) = split_bf(pw[q]);
        wait_lgkm0();
#pragma unroll
        for (int T = 0; T < 8; T++) {
            u32x4 v = *(const u32x4*)(trd + T * 64);
            Bh[T] = u32x4{v[0], v[0], v[2], v[2]};
            Bl[T] = u32x4{v[1], v[1], v[3], v[3]};
        }
        wait_lgkm0();
    }
    if (T1 < 2 && TMODE != 1) {
        if (TMODE == 0) lds_wait(&lf[LF_LNST], 4);
        else lds_wait(&lf[LF_H], 1);
        const char* base = (const char*)w1 + ((size_t)(li * 64) * F + (xcd >> 1) * 448) * 2;
#pragma unroll
        for (int i = T1; i < 2; i++) {
            int idx = (i * 4 + wv) * 64 + lane;
            idx = idx < 448 ? idx : 447;
            const int row = idx / 7, sec = idx - row * 7;
            touch(dummy, (unsigned)(row * (F * 2) + sec * 128), base);
        }
    }
    {
        const f32x2 E1 = cexp_scaled(zr, zi, 1.f);
        const f32x2 E128 = cexp_scaled(zr, zi, 128.f);
        f32x2 WY[8];
        {
            f32x2 Y[8];
            Y[0] = f32x2{1.f, 0.f}; Y[1] = E1;
#pragma unroll
            for (int c = 2; c < 8; c++) Y[c] = (c & 1) ? cmul(Y[c - 1], E1) : cmul(Y[c / 2], Y[c / 2]);
            WY[0] = W;
#pragma unroll
            for (int c = 1; c < 8; c++) WY[c] = cmul(W, Y[c]);
        }
        f32x2 Xp[17];
        Xp[0] = f32x2{1.f, 0.f}; Xp[1] = E128;
#pragma unroll
        for (int a = 2; a < 17; a++) Xp[a] = (a & 1) ? cmul(Xp[a - 1], E128) : cmul(Xp[a / 2], Xp[a / 2]);
#pragma unroll
        for (int tile = 0; tile < 3; tile++) {
#pragma unroll
            for (int r = 0; r < 16; r++) {
                f32x2 v;
                bool valid = true;
                if (tile == 0) { v = (r < 8) ? WY[r] : cmul(WY[r - 8], Xp[1]); }
                else if (tile == 1) {
                    if (r < 8) v = cmul(WY[2 * (r & 3)], Xp[2 + (r >> 2)]);
                    else v = cmul(WY[4 * (r & 1)], Xp[4 + ((r - 8) >> 1)]);
                } else {
                    if (r < 8) v = cmul(W, Xp[8 + r]);
                    else if (r == 8) v = cmul(W, Xp[16]);
                    else valid = false;
                }
                if (valid) *(u32x2*)(tw + r * TBL_PITCH) = split_bf(v);
            }
            wait_lgkm0();
            f32x4 a4 = {0.f, 0.f, 0.f, 0.f};
#pragma unroll
            for (int T = 0; T < 8; T++) {
                u32x4 av = *(const u32x4*)(trd + T * 64);
                a4 = mfma16(av, Bh[T], a4);
                a4 = mfma16(av, Bl[T], a4);
            }
            kacc[tile] = a4;
            wait_lgkm0();
        }
    }
    STAMP(0, 2);
    {
        float* P = (float*)(sb + SSM_P + hf * 3456);
        const int mq = lane >> 4, col = lane & 15;
#pragma unroll
        for (int tile = 0; tile < 3; tile++)
#pragma unroll
            for (int r = 0; r < 4; r++)
                if (!(tile == 2 && r == 1 && 0)) P[(tile * 16 + mq * 4 + r) * 18 + col] = kacc[tile][r];
        wait_lgkm0();
        if (lane == 0) P[KC_ZERO * 18] = 0.f;
    }
    if (hf == 0 && lane < 32) *(u32x4*)(sb + SSM_U + lane * 16) = u32x4{0, 0, 0, 0};
    lds_arrive(&fl[0], lane);
    touch_done(dummy);
    tie(cdummy);
    if (wv == 0) {
        poll_flags256(gf + GF_H2, tag, lane);
        if (lane == 0) __hip_atomic_store(&lf[LF_SSM + 9], 1u, __ATOMIC_RELAXED, __HIP_MEMORY_SCOPE_WORKGROUP);
    } else {
        lds_wait(&lf[LF_SSM + 9], 1);
    }
    u32x4 ug0, ug1;
    const int up0 = hf * 128 + lane, up1 = up0 + 64;
    {
        const int dt_ = d >> 6, nt = (d >> 4) & 3, m = d & 15;
        const char* src = (const char*)uF + ((size_t)(dt_ * 64 * 4 + nt) * 64 + m) * 16;
        const char* pa = src + (up0 >> 2) * 4096 + (up0 & 3) * 256;
        const char* pb = src + (up1 >> 2) * 4096 + (up1 & 3) * 256;
        asm volatile("global_load_dwordx4 %0, %2, off sc1\n\tglobal_load_dwordx4 %1, %3, off sc1"
                     : "=&v"(ug0), "=&v"(ug1) : "v"(pa), "v"(pb) : "memory");
    }
    lds_wait(&fl[0], 2);
    STAMP(0, 3);
    {
        const char* P0 = sb + SSM_P;
        const char* P1 = sb + SSM_P + 3456;
#pragma unroll
        for (int i = 0; i < 2; i++) {
            const int ub = hf * 128 + i * 64 + lane;
            float v[11];
#pragma unroll
            for (int j = 0; j < 11; j++) {
                const u32 word = kix[i][j >> 3][(j >> 1) & 3];
                const unsigned off = (j & 1) ? (word >> 16) : (word & 0xffffu);
                v[j] = *(const float*)(P0 + off) + *(const float*)(P1 + off);
            }
            u32 e[5], o[5];
#pragma unroll
            for (int k = 0; k < 5; k++) { e[k] = pk2bf(v[2 * k], v[2 * k + 1]); o[k] = pk2bf(v[2 * k + 1], v[2 * k + 2]); }
            char* dst = sb + SSM_COPY + ub * 16;
            *(u32x4*)(dst) = u32x4{e[0], e[1], e[2], e[3]};
            *(u32x4*)(dst + CP_STRIDE) = u32x4{o[0], o[1], o[2], o[3]};
            *(u32x4*)(dst + 2 * CP_STRIDE) = u32x4{e[1], e[2], e[3], e[4]};
            *(u32x4*)(dst + 3 * CP_STRIDE) = u32x4{o[1], o[2], o[3], o[4]};
        }
        if (hf == 0 && lane < 8) {
            *(u32x4*)(sb + SSM_COPY + (lane >> 1) * CP_STRIDE + (256 + (lane & 1)) * 16) = u32x4{0, 0, 0, 0};
        }
    }
    STAMP(0, 4);
    asm volatile("s_waitcnt vmcnt(0)" : "+v"(ug0), "+v"(ug1) :: "memory");
    *(u32x4*)(sb + SSM_U + 512 + up0 * 16) = ug0;
    *(u32x4*)(sb + SSM_U + 512 + up1 * 16) = ug1;
    lds_arrive(&fl[1], lane);
    lds_wait(&fl[1], 2);
    STAMP(0, 5);
    f32x4 cacc[8];
#pragma unroll
    for (int i = 0; i < 8; i++) cacc[i] = f32x4{0.f, 0.f, 0.f, 0.f};
    {
        const int m = lane & 15, q = lane >> 4;
        const int gq = m >> 2, e = m & 3, c = 3 - e;
        const char* abase = sb + SSM_COPY + c * CP_STRIDE + 4104 - 8 * gq - 16 * q - 64 * 63 + 64 * (7 - hf * 4 - 3);
        const char* bbase = sb + SSM_U + 528 + 32 * m - 16 * q - 64 * 7 + 64 * (7 - hf * 4 - 3);
#pragma unroll
        for (int rr = 0; rr < 4; rr++) {
            const char* ab = abase + 64 * (3 - rr);
            const char* bb = bbase + 64 * (3 - rr);
            u32x4 A[8], B[8];
#pragma unroll
            for (int pp = 0; pp < 8; pp++) {
                const u32x2 a0 = *(const u32x2*)(ab + 64 * (56 - 8 * pp));
                const u32x2 a1 = *(const u32x2*)(ab + 64 * (56 - 8 * pp) + 8);
                A[pp] = u32x4{a0[0], a0[1], a1[0], a1[1]};
                B[pp] = *(const u32x4*)(bb + 512 * pp);
            }
#pragma unroll
            for (int i = 0; i < 8; i++)
#pragma unroll
                for (int pp = 0; pp <= i; pp++) cacc[i] = mfma16(A[pp], B[i - pp], cacc[i]);
        }
    }
    STAMP(0, 6);
    f32x4 keep[4];
    {
        char* xo = sb + SSM_X + hf * 4096;
        if (hf) {
#pragma unroll
            for (int i = 0; i < 4; i++) { *(f32x4*)(xo + i * 1024 + lane * 16) = cacc[i]; keep[i] = cacc[4 + i]; asm volatile("" : "+v"(keep[i])); }
        } else {
#pragma unroll
            for (int i = 0; i < 4; i++) { *(f32x4*)(xo + i * 1024 + lane * 16) = cacc[4 + i]; keep[i] = cacc[i]; asm volatile("" : "+v"(keep[i])); }
        }
    }
    lds_arrive(&fl[2], lane);
    lds_wait(&fl[2], 2);
    {
        const char* xi = sb + SSM_X + (hf ^ 1) * 4096;
        const int nn = lane & 15, mq = lane >> 4;
#pragma unroll
        for (int i = 0; i < 4; i++) {
            f32x4 v = keep[i];
            v += *(const f32x4*)(xi + i * 1024 + lane * 16);
            const int t = 256 * (hf * 4 + i) + 16 * nn + 4 * mq;
            const u32x2 uu = *(const u32x2*)(sb + SSM_U + 512 + t * 2);
            u32 y0 = pk2bf(v[0], v[1]), y1 = pk2bf(v[2], v[3]);
            u32 s0 = pk2bf(lo_f(uu[0]) * dsk, hi_f(uu[0]) * dsk), s1 = pk2bf(lo_f(uu[1]) * dsk, hi_f(uu[1]) * dsk);
            u32x2 o = {pk2bf(lo_f(y0) + lo_f(s0), hi_f(y0) + hi_f(s0)), pk2bf(lo_f(y1) + lo_f(s1), hi_f(y1) + hi_f(s1))};
            st_sc1_8(aT + (size_t)d * L + t, o);
        }
    }
    if (wv != 0) {
        const char* base = (const char*)w2 + ((size_t)(li * 56) * L + (xcd >> 1) * 512) * 2;
#pragma unroll
        for (int i = 0; i < 3; i++) {
            int idx = ((wv - 1) * 3 + i) * 64 + lane;
            idx = idx < 448 ? idx : 447;
            touch(dummy, (unsigned)((idx >> 3) * (L * 2) + (idx & 7) * 128), base);
        }
        wait_vm0<3>();
    } else {
        wait_vm0<0>();
    }
    lds_arrive(&lf[LF_APUB], lane);
    if (wv == 0) {
        lds_wait(&lf[LF_APUB], 4);
        if (lane == 0) st_sc1_4(gf + GF_A + xcd * 32 + li, tag);
    }
    STAMP(0, 7);
    if (wv == 0) {
        poll_flags(gf + GF_A + dt2 * 32, 32, tag, lane);
        if (lane == 0) __hip_atomic_store(&lf[LF_SSM + 8], 1u, __ATOMIC_RELAXED, __HIP_MEMORY_SCOPE_WORKGROUP);
    } else {
        lds_wait(&lf[LF_SSM + 8], 1);
    }
    {
        const int r = 16 * wv + (lane >> 2);
        const int j0 = (lane & 3) * 2, j1 = j0 + 1;
        const char* src = (const char*)aT + ((size_t)(dt2 * 64 + r) * L + st * 64) * 2;
        u32x4 a, c;
        ld2_sc1_16(a, c, src + j0 * 16, src + j1 * 16);
        const int f = ((r >> 1) & 1) | (((r >> 3) & 1) << 1);
        *(u32x4*)(smem + STG + r * 128 + ((j0 ^ (f << 1)) << 4)) = a;
        *(u32x4*)(smem + STG + r * 128 + ((j1 ^ (f << 1)) << 4)) = c;
        if (lane < 16) {
            const int s = st * 64 + wv * 16 + lane;
            const u32 gv = ld_sc1_4(g + s);
            *(u32*)(smem + STG + 8192 + (wv * 16 + lane) * 4) = gv;
            *(float*)(smem + STG + 8192 + 256 + (wv * 16 + lane) * 4) = bf2f(b2[s]);
        }
    }
    lds_arrive(&lf[LF_EPI], lane);
    touch_done(dummy);
    STAMP(0, 8);
    STAMP_DUMP;
}

extern "C" void kernel_launch(void* const* d_in, const int* in_sizes, int n_in,
                              void* d_out, int out_size, void* d_ws, size_t ws_size,
                              hipStream_t stream) {
    const u16* u = (const u16*)d_in[0];
    const u16* Are = (const u16*)d_in[1];
    const u16* Aim = (const u16*)d_in[2];
    const u16* Bre = (const u16*)d_in[3];
    const u16* Bim = (const u16*)d_in[4];
    const u16* Cre = (const u16*)d_in[5];
    const u16* Cim = (const u16*)d_in[6];
    const u16* logdt = (const u16*)d_in[7];
    const u16* Dskip = (const u16*)d_in[8];
    const u16* w1 = (const u16*)d_in[10];
    const u16* b1 = (const u16*)d_in[11];
    const u16* w2 = (const u16*)d_in[12];
    const u16* b2 = (const u16*)d_in[13];
    const u16* gw = (const u16*)d_in[14];
    k_fused<<<256, 512, 0, stream>>>(u, gw, Are, Aim, Bre, Bim, Cre, Cim, logdt, Dskip, w1, b1, w2, b2, (char*)d_ws, (u16*)d_out);
}
```

```cpp
#include <hip/hip_runtime.h>
#include <stdint.h>

typedef unsigned short u16;
typedef unsigned int u32;
using bf16x8 = __attribute__((ext_vector_type(8))) __bf16;
using bf16x2 = __attribute__((ext_vector_type(2))) __bf16;
using s16x4 = __attribute__((ext_vector_type(4))) short;
using f32x4 = __attribute__((ext_vector_type(4))) float;
using f32x2 = __attribute__((ext_vector_type(2))) float;
using u32x4 = __attribute__((ext_vector_type(4))) unsigned;
using u32x2 = __attribute__((ext_vector_type(2))) unsigned;
typedef __attribute__((address_space(3))) s16x4* lds_s16x4_ptr;

#if defined(__HIP_DEVICE_COMPILE__)
#define NOPK __attribute__((target("no-packed-fp32-ops")))
#else
#define NOPK
#endif
#define DEV NOPK __device__ __forceinline__
#define TIDX ((int)__builtin_amdgcn_workitem_id_x())
#define BIDX ((int)__builtin_amdgcn_workgroup_id_x())

#define STAMP_DECL do {} while (0)
#define STAMP(kern, idx) do {} while (0)
#define STAMP_DUMP do {} while (0)

constexpr int L = 2048, D = 512, NS = 128, F = 1792;
#define T1 1
#define TMODE 0
#define TLBWARM 1
#define CODE_BYTES 30000
#define RK 1
#define ROT1 ((16 * 4 - RK * (ftl + dtl)) & 15)
#define ROT2 ((14 * 4 - RK * (stl + dtl2)) % 14)
#define STR_(x) #x
#define STR(x) STR_(x)

DEV float bf2f(u16 v) { return __builtin_bit_cast(float, ((unsigned)v) << 16); }
DEV u32 pk2bf(float a, float b) {
    f32x2 v = {a, b};
    return __builtin_bit_cast(u32, __builtin_convertvector(v, bf16x2));
}
DEV float lo_f(u32 p) { return __builtin_bit_cast(float, p << 16); }
DEV float hi_f(u32 p) { return __builtin_bit_cast(float, p & 0xffff0000u); }
DEV float rbf(float f) { return lo_f(pk2bf(f, 0.f)); }

DEV u32x2 tr_read(const char* lds_generic_base, unsigned off) {
    s16x4 v = __builtin_amdgcn_ds_read_tr16_b64_v4i16((lds_s16x4_ptr)(lds_generic_base + off));
    return __builtin_bit_cast(u32x2, v);
}
DEV f32x4 mfma16(u32x4 a, u32x4 b, f32x4 c) {
    return __builtin_amdgcn_mfma_f32_16x16x32_bf16(__builtin_bit_cast(bf16x8, a), __builtin_bit_cast(bf16x8, b), c, 0, 0, 0);
}

DEV void gload_early8(u32x2& dst, const void* p) { asm volatile("global_load_dwordx2 %0, %1, off" : "=v"(dst) : "v"(p) : "memory"); }
template <typename T> DEV void tie(T& x) { asm volatile("" : "+v"(x) :: "memory"); }
DEV void touch(u32& dummy, unsigned voff, const void* sbase) {
    asm volatile("global_load_dword %0, %1, %2" : "+v"(dummy) : "v"(voff), "s"(sbase) : "memory");
}
DEV void touch_done(u32& dummy) { asm volatile("s_waitcnt vmcnt(0)" : "+v"(dummy) :: "memory"); }
template <int N>
DEV void wait_vm(u32x4& a, u32x4& b, u32x4& c, u32x4& d) {
    asm volatile("s_waitcnt vmcnt(%4)" : "+v"(a), "+v"(b), "+v"(c), "+v"(d) : "i"(N) : "memory");
}
template <int N>
DEV void wait_vm0() { asm volatile("s_waitcnt vmcnt(%0)" ::"i"(N) : "memory"); }
DEV void wait_lgkm0() { asm volatile("s_waitcnt lgkmcnt(0)" ::: "memory"); }
DEV u32 ld_sc1_4(const void* p) {
    u32 v; asm volatile("global_load_dword %0, %1, off sc1\n\ts_waitcnt vmcnt(0)" : "=&v"(v) : "v"(p) : "memory"); return v;
}
DEV u32x4 ld_sc1_16(const void* p) {
    u32x4 v; asm volatile("global_load_dwordx4 %0, %1, off sc1\n\ts_waitcnt vmcnt(0)" : "=&v"(v) : "v"(p) : "memory"); return v;
}
DEV void ld2_sc1_16(u32x4& a, u32x4& b, const void* pa, const void* pb) {
    asm volatile("global_load_dwordx4 %0, %2, off sc1\n\tglobal_load_dwordx4 %1, %3, off sc1\n\ts_waitcnt vmcnt(0)"
                 : "=&v"(a), "=&v"(b) : "v"(pa), "v"(pb) : "memory");
}
DEV void st_sc1_16(void* p, u32x4 v) { asm volatile("global_store_dwordx4 %0, %1, off sc1\n\ts_nop 1" :: "v"(p), "v"(v) : "memory"); }
DEV void st_sc1_8(void* p, u32x2 v) { asm volatile("global_store_dwordx2 %0, %1, off sc1\n\ts_nop 1" :: "v"(p), "v"(v) : "memory"); }
DEV void st_sc1_4(void* p, u32 v) { asm volatile("global_store_dword %0, %1, off sc1" :: "v"(p), "v"(v) : "memory"); }

template <int CTRL>
DEV float dpp_add(float v) {
    int t = __builtin_amdgcn_update_dpp(0, __builtin_bit_cast(int, v), CTRL, 0xf, 0xf, true);
    return v + __builtin_bit_cast(float, t);
}
DEV float rdlane(float v, int l) { return __builtin_bit_cast(float, __builtin_amdgcn_readlane(__builtin_bit_cast(int, v), l)); }
DEV float wave_sum(float v) {
    v = dpp_add<0xB1>(v);
    v = dpp_add<0x4E>(v);
    v = dpp_add<0x141>(v);
    v = dpp_add<0x140>(v);
    return (rdlane(v, 0) + rdlane(v, 16)) + (rdlane(v, 32) + rdlane(v, 48));
}
DEV float fast_exp(float x) { return __builtin_amdgcn_exp2f(x * 1.4426950408889634f); }
DEV void lds_arrive(unsigned* cnt, int lane) {
    wait_lgkm0();
    if (lane == 0) __hip_atomic_fetch_add(cnt, 1u, __ATOMIC_RELAXED, __HIP_MEMORY_SCOPE_WORKGROUP);
}
DEV void lds_wait(unsigned* cnt, unsigned target) {
    while (__hip_atomic_load(cnt, __ATOMIC_RELAXED, __HIP_MEMORY_SCOPE_WORKGROUP) < target) __builtin_amdgcn_s_sleep(1);
    asm volatile("" ::: "memory");
}
constexpr int SPIN_CAP = 20000;
DEV void poll_flags256(const u32* fl, u32 tag, int lane) {
#pragma unroll 1
    for (int spin = 0; spin < SPIN_CAP; spin++) {
        u32x4 v = ld_sc1_16(fl + lane * 4);
        bool ok = v[0] == tag && v[1] == tag && v[2] == tag && v[3] == tag;
        if (__builtin_amdgcn_ballot_w64(ok) == ~0ull) break;
        __builtin_amdgcn_s_sleep(2);
    }
}
DEV void poll_flags(const u32* fl, int n, u32 tag, int lane) {
    const u32* p = fl + (lane < n ? lane : 0);
#pragma unroll 1
    for (int spin = 0; spin < SPIN_CAP; spin++) {
        u32 v = ld_sc1_4(p);
        if (__builtin_amdgcn_ballot_w64(v == tag) == ~0ull) break;
        __builtin_amdgcn_s_sleep(2);
    }
}
DEV float div_nr(float a, float b, float r) {
    float q = a * r;
    float e = __builtin_fmaf(-q, b, a);
    return __builtin_fmaf(e, r, q);
}

struct KIdxTable { u16 v[256 * 16]; };
constexpr int round_bf16_int(int x) {
    if (x < 256) return x;
    int sh = 0;
    while ((x >> sh) >= 256) sh++;
    int q = x >> sh, r = x & ((1 << sh) - 1), half = 1 << (sh - 1);
    if (r > half || (r == half && (q & 1))) q++;
    return q << sh;
}
constexpr int KC_ZERO = 41;
constexpr int compact_pos(int tap) {
    int row = KC_ZERO, col = 0;
    if (tap >= 0) {
        int lr = round_bf16_int(tap);
        if (lr < 256) { row = (lr & 7) + 8 * (lr >> 7); col = (lr >> 3) & 15; }
        else if (lr < 512) { int x = lr - 256; row = 16 + ((x & 7) >> 1) + 4 * (x >> 7); col = (x >> 3) & 15; }
        else if (lr < 1024) { int x = lr - 512; row = 24 + ((x & 7) >> 2) + 2 * (x >> 7); col = (x >> 3) & 15; }
        else if (lr < 2048) { int x = lr - 1024; row = 32 + (x >> 7); col = (x >> 3) & 15; }
        else { row = 40; col = 0; }
    }
    return row * 18 + col;
}
constexpr KIdxTable make_kidx() {
    KIdxTable t{};
    for (int ub = 0; ub < 256; ub++)
        for (int i = 0; i < 16; i++) t.v[ub * 16 + i] = (u16)(4 * compact_pos(2047 - (8 * ub + i)));
    return t;
}
__device__ const KIdxTable KIDX = make_kidx();

DEV f32x2 cmul(f32x2 a, f32x2 b) {
    f32x2 r;
    r.x = __builtin_fmaf(-a.y, b.y, a.x * b.x);
    r.y = __builtin_fmaf(a.y, b.x, a.x * b.y);
    return r;
}
DEV f32x2 cexp_scaled(float zr, float zi, float l) {
    const float xr = zr * l, xi = zi * l;
    const float e = __builtin_amdgcn_exp2f(xr * 1.4426950408889634f);
    const float c_hi = 0.15915494309189535f;
    const float c_lo = 6.4206382e-09f;
    float rh = xi * c_hi;
    float rl = __builtin_fmaf(xi, c_lo, __builtin_fmaf(xi, c_hi, -rh));
    float fr = (rh - __builtin_rintf(rh)) + rl;
    return f32x2{e * __builtin_amdgcn_cosf(fr), e * __builtin_amdgcn_sinf(fr)};
}
DEV u32x2 split_bf(f32x2 v) {
    u32 h = pk2bf(v.x, v.y);
    u32 l = pk2bf(v.x - lo_f(h), v.y - hi_f(h));
    return u32x2{h, l};
}

DEV void dma_slot(unsigned lds_, const unsigned (&voff)[4], const void* sbase) {
    const unsigned lds = (unsigned)__builtin_amdgcn_readfirstlane((int)lds_);
    asm volatile("s_mov_b32 m0, %0\n\ts_nop 0\n\t"
                 "global_load_lds_dwordx4 %1, %5\n\t"
                 "global_load_lds_dwordx4 %2, %5 offset:1024\n\t"
                 "global_load_lds_dwordx4 %3, %5 offset:2048\n\t"
                 "global_load_lds_dwordx4 %4, %5 offset:3072"
                 :: "s"(lds), "v"(voff[0]), "v"(voff[1]), "v"(voff[2]), "v"(voff[3]), "s"(sbase) : "memory", "m0");
}
DEV void xload(u32x4 (&x)[4], unsigned voff, const void* sbase) {
    asm volatile("global_load_dwordx4 %0, %4, %5 sc1\n\t"
                 "global_load_dwordx4 %1, %4, %5 offset:1024 sc1\n\t"
                 "global_load_dwordx4 %2, %4, %5 offset:2048 sc1\n\t"
                 "global_load_dwordx4 %3, %4, %5 offset:3072 sc1"
                 : "+v"(x[0]), "+v"(x[1]), "+v"(x[2]), "+v"(x[3]) : "v"(voff), "s"(sbase) : "memory");
}
template <int S, int WPITCH, bool W_IS_A>
struct GemmCtx {
    const char* smem;
    const char* wbase;
    const char* xbase;
    unsigned ring;
    int nxt, nxtx;
    unsigned wvoff[4];
    unsigned xvoff;
    unsigned raddr[4];
    u32x4 X[4][4];
    f32x4 acc[4][4];
};
template <int J, int S, int WPITCH, bool W_IS_A>
DEV void g_issue_w(GemmCtx<S, WPITCH, W_IS_A>& c) {
    dma_slot(c.ring + J * 4096, c.wvoff, c.wbase + (size_t)((unsigned)c.nxt * (unsigned)(32 * WPITCH)));
    c.nxt = (c.nxt + 1 == S) ? 0 : c.nxt + 1;
}
template <int J, int S, int WPITCH, bool W_IS_A>
DEV void g_issue_x(GemmCtx<S, WPITCH, W_IS_A>& c) {
    xload(c.X[J], c.xvoff, c.xbase + (size_t)((unsigned)c.nxtx * 4096u));
    c.nxtx = (c.nxtx + 1 == S) ? 0 : c.nxtx + 1;
}
template <int J, int N, bool IX, bool IW, int S, int WPITCH, bool W_IS_A>
DEV void g_step(GemmCtx<S, WPITCH, W_IS_A>& c) {
    u32x4(&x)[4] = c.X[J];
    wait_vm<N>(x[0], x[1], x[2], x[3]);
    u32x4 Wf[4];
#pragma unroll
    for (int i = 0; i < 4; i++) {
        u32x2 a = tr_read(c.smem, c.raddr[i] + J * 4096);
        u32x2 b = tr_read(c.smem, c.raddr[i] + J * 4096 + 512);
        Wf[i] = u32x4{a[0], a[1], b[0], b[1]};
    }
    if constexpr (IX) g_issue_x<(J + 3) & 3>(c);
    if constexpr (IW) {
        asm volatile("s_waitcnt lgkmcnt(0)" : "+v"(Wf[0]), "+v"(Wf[1]), "+v"(Wf[2]), "+v"(Wf[3]) :: "memory");
        g_issue_w<J>(c);
    }
#pragma unroll
    for (int i = 0; i < 4; i++)
#pragma unroll
        for (int j = 0; j < 4; j++) {
            if constexpr (W_IS_A) c.acc[i][j] = mfma16(Wf[i], x[j], c.acc[i][j]);
            else c.acc[i][j] = mfma16(x[i], Wf[j], c.acc[i][j]);
        }
    __builtin_amdgcn_sched_barrier(0);
}
template <int S, int WPITCH, bool W_IS_A>
DEV void gemm_begin(GemmCtx<S, WPITCH, W_IS_A>& c, const char* smem, const char* wbase0, size_t wstride, const char* xbase, unsigned ring0, int w, int rot, int lane) {
    c.smem = smem; c.wbase = wbase0 + w * wstride; c.xbase = xbase; c.ring = ring0 + w * 16384; c.nxt = rot; c.nxtx = rot;
#pragma unroll
    for (int i = 0; i < 4; i++) {
        const int sw = (((lane >> 4) & 1) << 1) | ((i & 1) << 2);
        c.wvoff[i] = (unsigned)((8 * i + (lane >> 3)) * WPITCH + (((lane & 7) ^ sw) << 4) - 1024 * i);
    }
    c.xvoff = lane * 16;
    const int g = lane >> 4, q = (lane & 15) >> 2, p = lane & 3;
    const int fl = ((q >> 1) & 1) | ((g & 1) << 1);
#pragma unroll
    for (int i = 0; i < 4; i++) c.raddr[i] = c.ring + (8 * g + q) * 128 + ((i ^ fl) << 5) + p * 8;
    if (w == 0) {
        int k = rot + 4;
        c.nxt = k >= S ? k - S : k;
    } else {
        if (w == 1) {
            int k = rot;
#pragma unroll
            for (int j = 0; j < 4; j++) {
                dma_slot(ring0 + j * 4096, c.wvoff, wbase0 + (size_t)((unsigned)k * (unsigned)(32 * WPITCH)));
                k = (k + 1 == S) ? 0 : k + 1;
            }
        }
        g_issue_w<0>(c); g_issue_w<1>(c); g_issue_w<2>(c); g_issue_w<3>(c);
    }
}
template <int S, int WPITCH, bool W_IS_A>
DEV void gemm_run(GemmCtx<S, WPITCH, W_IS_A>& c, unsigned* w0flag, unsigned w0val, int w
) {
#pragma unroll
    for (int i = 0; i < 4; i++)
#pragma unroll
        for (int j = 0; j < 4; j++) { c.acc[i][j] = f32x4{0.f, 0.f, 0.f, 0.f}; c.X[i][j] = u32x4{0, 0, 0, 0}; }
    g_issue_x<0>(c); g_issue_x<1>(c); g_issue_x<2>(c);
    if (w == 0) lds_wait(w0flag, w0val);
    g_step<0, 8, true, true>(c);
    g_step<1, 12, true, true>(c);
    g_step<2, 16, true, true>(c);
    g_step<3, 20, true, true>(c);
    constexpr int STEADY = S - 8;
#pragma unroll 1
    for (int it = 0; it < STEADY / 4; it++) {
        g_step<0, 20, true, true>(c);
        g_step<1, 20, true, true>(c);
        g_step<2, 20, true, true>(c);
        g_step<3, 20, true, true>(c);
    }
    constexpr int R = STEADY % 4;
    if constexpr (R >= 1) g_step<0, 20, true, true>(c);
    if constexpr (R >= 2) g_step<1, 20, true, true>(c);
    if constexpr (R >= 3) g_step<2, 20, true, true>(c);
    g_step<(R + 0) & 3, 20, true, false>(c);
    g_step<(R + 1) & 3, 16, false, false>(c);
    g_step<(R + 2) & 3, 8, false, false>(c);
    g_step<(R + 3) & 3, 0, false, false>(c);
}

constexpr int RING = 0;
constexpr int STG = 65536;
constexpr int SSM0 = STG + 16384;
constexpr int TBL_PITCH = 528;
constexpr int TBL_SIZE = 16 * TBL_PITCH;
constexpr int CP_STRIDE = 4160;
constexpr int SSM_COPY = 0;
constexpr int SSM_U = 16896;
constexpr int SSM_P = SSM_U + 4608;
constexpr int SSM_X = SSM_P + 2 * 3456;
constexpr int SSM_SIZE = SSM_X + 8192;
constexpr int LFLAGS = SSM0 + 2 * SSM_SIZE;
constexpr int LDS_BYTES = LFLAGS + 128;
enum { LF_LN = 0, LF_LNPUB = 1, LF_H = 2, LF_X1 = 3, LF_X1R = 4, LF_UPUB = 5, LF_U = 6, LF_X2 = 7, LF_EPI = 8, LF_APUB = 9, LF_LNST = 10, LF_LNPUB2 = 11, LF_UARR = 24, LF_W0 = 25, LF_SSM = 12   };
constexpr int GF_EPOCH = 0, GF_H = 64, GF_H2 = 64 + 256, GF_U = 64 + 512, GF_A = 64 + 512 + 8 * 32;

NOPK __global__ __launch_bounds__(512) void k_fused(const u16* __restrict__ u, const u16* __restrict__ gw,
                                                    const u16* __restrict__ Are, const u16* __restrict__ Aim,
                                                    const u16* __restrict__ Bre, const u16* __restrict__ Bim,
                                                    const u16* __restrict__ Cre, const u16* __restrict__ Cim,
                                                    const u16* __restrict__ logdt, const u16* __restrict__ Dskip,
                                                    const u16* __restrict__ w1, const u16* __restrict__ b1,
                                                    const u16* __restrict__ w2, const u16* __restrict__ b2,
                                                    char* __restrict__ ws, u16* __restrict__ out) {
    __shared__ __attribute__((aligned(16))) char smem[LDS_BYTES];
    const int tid = TIDX, lane = tid & 63;
    const int w = __builtin_amdgcn_readfirstlane(tid >> 6);
    const int b = BIDX;
    const int xcd = b & 7, li = b >> 3;
    unsigned* lf = (unsigned*)(smem + LFLAGS);
    u16* hF = (u16*)ws;
    u16* uF = (u16*)(ws + (2 << 20));
    u16* aT = (u16*)(ws + (4 << 20));
    u16* UF = (u16*)(ws + (6 << 20));
    float* g = (float*)(ws + (8 << 20));
    u32* gf = (u32*)(ws + (9 << 20));
    const int stl = li & 7, dtl2 = li >> 3;
    const int st = (xcd >> 1) * 8 + stl, dt2 = (xcd & 1) * 4 + dtl2;
    STAMP_DECL;
    STAMP(0, 0);
    u32 cdummy = 0;
    if (w == 7) {
        const unsigned long long pc = __builtin_amdgcn_s_getpc() & ~127ull;
        int idx = li * 8 + lane;
        if (lane < 8 && idx * 128 < CODE_BYTES) touch(cdummy, (unsigned)(idx * 128), (const void*)pc);
    }
    if (w == 6 && li < 8) {
        const unsigned off = (unsigned)lane * 65536u;
        const char* base; unsigned lim;
        if (li < 3) { base = ws + (size_t)li * (4u << 20); lim = li < 2 ? (4u << 20) : (2u << 20); }
        else if (li < 5) { base = (const char*)w1 + (size_t)(li - 3) * (4u << 20); lim = li == 3 ? (4u << 20) : (unsigned)(L * F * 2 - (4 << 20)); }
        else if (li < 7) { base = (const char*)w2 + (size_t)(li - 5) * (4u << 20); lim = li == 5 ? (4u << 20) : (unsigned)(L * F * 2 - (4 << 20)); }
        else { base = (const char*)out; lim = 2u << 20; }
        if (off < lim) touch(cdummy, off, base);
    }
    if (tid < 32) lf[tid] = 0;
    wait_lgkm0();
    __builtin_amdgcn_s_barrier();

    if (w < 4) {
        __builtin_amdgcn_s_setprio(3);
        const int s0 = b * 8;
        u32x4 xv[2];
#pragma unroll
        for (int i = 0; i < 2; i++) xv[i] = *(const u32x4*)(u + (size_t)(s0 + 2 * w + i) * D + lane * 8);
        const u32x4 gv = *(const u32x4*)(gw + lane * 8);
        const u32 tag = ld_sc1_4(gf + GF_EPOCH) + 1u;
        STAMP(0, 8);
        lds_arrive(&lf[LF_UARR], lane);
        float gval[2];
#pragma unroll
        for (int i = 0; i < 2; i++) {
            const int r = 2 * w + i;
            float x[8];
#pragma unroll
            for (int j = 0; j < 4; j++) { x[2 * j] = lo_f(xv[i][j]); x[2 * j + 1] = hi_f(xv[i][j]); }
            float sum = 0.f, gs = 0.f;
#pragma unroll
            for (int j = 0; j < 4; j++) {
                sum += x[2 * j] + x[2 * j + 1];
                u32 p = pk2bf(x[2 * j] * lo_f(gv[j]), x[2 * j + 1] * hi_f(gv[j]));
                gs += lo_f(p) + hi_f(p);
            }
            sum = wave_sum(sum);
            gs = wave_sum(gs);
            const float mean = sum * (1.f / D);
            float sq = 0.f;
#pragma unroll
            for (int j = 0; j < 8; j++) { float dl = x[j] - mean; sq = __builtin_fmaf(dl, dl, sq); }
            sq = wave_sum(sq);
            const float var = rbf(sq * (1.f / D));
            const float m = rbf(mean);
            const float sd = rbf(__builtin_sqrtf(rbf(var + 1e-5f)));
            const float rs = __builtin_amdgcn_rcpf(sd);
            u32x4 hv;
#pragma unroll
            for (int j = 0; j < 4; j++) {
                u32 c = pk2bf(x[2 * j] - m, x[2 * j + 1] - m);
                hv[j] = pk2bf(div_nr(lo_f(c), sd, rs), div_nr(hi_f(c), sd, rs));
            }
            const int c16 = lane ^ ((r & 3) << 2);
            *(u32x4*)(smem + STG + r * 1024 + c16 * 16) = hv;
            *(u32x4*)(smem + STG + 8192 + r * 1024 + c16 * 16) = xv[i];
            gval[i] = rbf(__builtin_amdgcn_rcpf(1.f + fast_exp(-rbf(gs))));
        }
        STAMP(0, 10);
        lds_arrive(&lf[LF_LN], lane);
        lds_wait(&lf[LF_LN], 4);
        STAMP(0, 11);
        u32x4 ukeep[2];
        size_t uoff[2];
        {
            const int nt = lane >> 4, q = (lane & 15) >> 2, p = lane & 3;
            const int ks = b >> 2, kq = b & 3;
#pragma unroll
            for (int i = 0; i < 2; i++) {
                const int dt = w * 2 + i;
                const int cb = dt * 4 + nt;
                const unsigned o0 = q * 1024 + ((cb ^ (q << 1)) << 5) + p * 8;
                const unsigned o1 = o0 + 4096;
                u32x2 h0 = tr_read(smem + STG, o0), h1 = tr_read(smem + STG, o1);
                u32x2 u0 = tr_read(smem + STG + 8192, o0), u1 = tr_read(smem + STG + 8192, o1);
                const size_t off = ((size_t)((dt * 64 + ks) * 4 + nt) * 64 + kq * 16 + (lane & 15)) * 8;
                st_sc1_16(hF + off, u32x4{h0[0], h0[1], h1[0], h1[1]});
                ukeep[i] = u32x4{u0[0], u0[1], u1[0], u1[1]};
                uoff[i] = off;
            }
        }
        STAMP(0, 12);
        wait_vm0<0>();
        STAMP(0, 13);
        lds_arrive(&lf[LF_LNPUB], lane);
        if (w == 0) {
            lds_wait(&lf[LF_LNPUB], 4);
            if (lane == 0) st_sc1_4(gf + GF_H + b, tag);
            lds_arrive(&lf[LF_LNPUB], lane);
        } else {
            lds_wait(&lf[LF_LNPUB], 5);
        }
        st_sc1_16(uF + uoff[0], ukeep[0]);
        st_sc1_16(uF + uoff[1], ukeep[1]);
        if (lane < 2) st_sc1_4(g + s0 + 2 * w + lane, __builtin_bit_cast(u32, lane ? gval[1] : gval[0]));
        lds_arrive(&lf[LF_LNST], lane);
        __builtin_amdgcn_s_setprio(0);
        STAMP(0, 1);
        const bool has1 = li < 28;
        const int ftl = li % 7, dtl = li / 7;
        const int ft = (xcd >> 1) * 7 + ftl, dt = (xcd & 1) * 4 + dtl;
        u32x2 bb[4];
        GemmCtx<16, F * 2, true> c1;
        wait_vm0<0>();
        lds_arrive(&lf[LF_LNPUB2], lane);
        if (w == 1) {
            lds_wait(&lf[LF_LNPUB2], 4);
            if (lane == 0) st_sc1_4(gf + GF_H2 + b, tag);
            lds_arrive(&lf[LF_LNPUB2], lane);
        } else {
            lds_wait(&lf[LF_LNPUB2], 5);
        }
        if (has1) {
#pragma unroll
            for (int mt = 0; mt < 4; mt++) gload_early8(bb[mt], b1 + ft * 64 + mt * 16 + (lane >> 4) * 4);
            gemm_begin(c1, smem, (const char*)w1 + ((size_t)ft * 64) * 2, (size_t)512 * F * 2,
                       (const char*)hF + ((size_t)(dt * 64 + w * 16)) * 4096, RING, w, ROT1, lane);
        }
        if (w == 0) {
            poll_flags256(gf + GF_H, tag, lane);
            if (lane == 0) __hip_atomic_store(&lf[LF_H], 1u, __ATOMIC_RELAXED, __HIP_MEMORY_SCOPE_WORKGROUP);
        } else {
            lds_wait(&lf[LF_H], 1);
        }
        STAMP(0, 2);
        if (has1) {
            if (w == 1) { wait_vm0<16>(); if (lane == 0) __hip_atomic_store(&lf[LF_W0], 1u, __ATOMIC_RELAXED, __HIP_MEMORY_SCOPE_WORKGROUP); }
            gemm_run(c1, &lf[LF_W0], 1u, w);
            STAMP(0, 3);
#pragma unroll
            for (int mt = 0; mt < 4; mt++) tie(bb[mt]);
            f32x4 own[4];
            {
                char* mine = smem + RING + w * 16384;
#define XCH(k) if (w == k) { \
                _Pragma("unroll") for (int nt = 0; nt < 4; nt++) _Pragma("unroll") for (int mt = 0; mt < 4; mt++) \
                    if (nt != k) *(f32x4*)(mine + (nt * 4 + mt) * 1024 + lane * 16) = c1.acc[mt][nt]; \
                _Pragma("unroll") for (int mt = 0; mt < 4; mt++) { own[mt] = c1.acc[mt][k]; asm volatile("" : "+v"(own[mt])); } }
                XCH(0) XCH(1) XCH(2) XCH(3)
#undef XCH
            }
            lds_arrive(&lf[LF_X1], lane);
            lds_wait(&lf[LF_X1], 4);
            STAMP(0, 9);
#pragma unroll
            for (int o = 1; o < 4; o++) {
                const int ow = (w + o) & 3;
#pragma unroll
                for (int mt = 0; mt < 4; mt++)
                    own[mt] += *(const f32x4*)(smem + RING + ow * 16384 + (w * 4 + mt) * 1024 + lane * 16);
            }
            lds_arrive(&lf[LF_X1R], lane);
            const int mq = lane >> 4, n = lane & 15;
#pragma unroll
            for (int mt = 0; mt < 4; mt++) {
                float v[4];
                {
                    u32 p0 = pk2bf(own[mt][0], own[mt][1]), p1 = pk2bf(own[mt][2], own[mt][3]);
                    u32 s0_ = pk2bf(lo_f(p0) + lo_f(bb[mt][0]), hi_f(p0) + hi_f(bb[mt][0]));
                    u32 s1_ = pk2bf(lo_f(p1) + lo_f(bb[mt][1]), hi_f(p1) + hi_f(bb[mt][1]));
                    v[0] = lo_f(s0_); v[1] = hi_f(s0_); v[2] = lo_f(s1_); v[3] = hi_f(s1_);
                }
#pragma unroll
                for (int i = 0; i < 4; i++) {
                    const float x = v[i];
                    const float p = __builtin_fmaf(x * x, -2.f * 0.7978845608028654f * 0.044715f * 1.4426950408889634f,
                                                   -2.f * 0.7978845608028654f * 1.4426950408889634f);
                    const float e = __builtin_amdgcn_exp2f(x * p);
                    v[i] = x * __builtin_amdgcn_rcpf(1.f + e);
                }
                u32x2 o = {pk2bf(v[0], v[1]), pk2bf(v[2], v[3])};
                const int ks = ft * 2 + (mt >> 1), kq = 2 * (mt & 1) + (mq >> 1);
                char* dst = (char*)UF + ((size_t)((dt * 56 + ks) * 4 + w) * 64 + kq * 16 + n) * 16 + (mq & 1) * 8;
                st_sc1_8(dst, o);
            }
            STAMP(0, 14);
            wait_vm0<0>();
            STAMP(0, 15);
            lds_arrive(&lf[LF_UPUB], lane);
            if (w == 0) {
                lds_wait(&lf[LF_UPUB], 4);
                if (lane == 0) st_sc1_4(gf + GF_U + dt * 32 + ft, tag);
                lds_arrive(&lf[LF_UPUB], lane);
            } else {
                lds_wait(&lf[LF_UPUB], 5);
            }
            lds_wait(&lf[LF_X1R], 4);
        }
        STAMP(0, 4);
        GemmCtx<14, L * 2, false> c2;
        gemm_begin(c2, smem, (const char*)w2 + ((size_t)st * 64) * 2, (size_t)448 * L * 2,
                   (const char*)UF + ((size_t)(dt2 * 56 + w * 14)) * 4096, RING, w, ROT2, lane);
        if (w == 0) {
            poll_flags(gf + GF_U + dt2 * 32, 28, tag, lane);
            if (lane == 0) {
                __hip_atomic_store(&lf[LF_U], 1u, __ATOMIC_RELAXED, __HIP_MEMORY_SCOPE_WORKGROUP);
                if (b == 0) st_sc1_4(gf + GF_EPOCH, tag);
            }
        } else {
            lds_wait(&lf[LF_U], 1);
        }
        STAMP(0, 5);
        if (w == 1) { wait_vm0<16>(); if (lane == 0) __hip_atomic_store(&lf[LF_W0], 2u, __ATOMIC_RELAXED, __HIP_MEMORY_SCOPE_WORKGROUP); }
        gemm_run(c2, &lf[LF_W0], 2u, w);
        STAMP(0, 6);
        f32x4 own[4];
        {
            char* mine = smem + RING + w * 16384;
#define XCH(k) if (w == k) { \
            _Pragma("unroll") for (int nt = 0; nt < 4; nt++) _Pragma("unroll") for (int mt = 0; mt < 4; mt++) \
                if (nt != k) *(f32x4*)(mine + (nt * 4 + mt) * 1024 + lane * 16) = c2.acc[mt][nt]; \
            _Pragma("unroll") for (int mt = 0; mt < 4; mt++) { own[mt] = c2.acc[mt][k]; asm volatile("" : "+v"(own[mt])); } }
            XCH(0) XCH(1) XCH(2) XCH(3)
#undef XCH
        }
        lds_arrive(&lf[LF_X2], lane);
        lds_wait(&lf[LF_X2], 4);
#pragma unroll
        for (int o = 1; o < 4; o++) {
            const int ow = (w + o) & 3;
#pragma unroll
            for (int mt = 0; mt < 4; mt++)
                own[mt] += *(const f32x4*)(smem + RING + ow * 16384 + (w * 4 + mt) * 1024 + lane * 16);
        }
        lds_wait(&lf[LF_EPI], 4);
        {
            const int i = lane & 15, mq = lane >> 4, q = i >> 2, p = lane & 3;
            const float gg = *(const float*)(smem + STG + 8192 + (w * 16 + i) * 4);
            const float bias = *(const float*)(smem + STG + 8192 + 256 + (w * 16 + i) * 4);
            const float og = rbf(1.f - gg);
            u16* dst = out + (size_t)(st * 64 + w * 16 + i) * D + dt2 * 64 + mq * 4;
#pragma unroll
            for (int mt = 0; mt < 4; mt++) {
                const int r0 = 16 * mt + 4 * mq + q;
                const int f0 = ((r0 >> 1) & 1) | (((r0 >> 3) & 1) << 1);
                const u32x2 av = tr_read(smem, STG + r0 * 128 + ((w ^ f0) << 5) + p * 8);
                u32x2 o;
#pragma unroll
                for (int e = 0; e < 2; e++) {
                    u32 y = pk2bf(own[mt][2 * e], own[mt][2 * e + 1]);
                    y = pk2bf(lo_f(y) + bias, hi_f(y) + bias);
                    u32 t1 = pk2bf(gg * lo_f(av[e]), gg * hi_f(av[e]));
                    u32 t2 = pk2bf(og * lo_f(y), og * hi_f(y));
                    o[e] = pk2bf(lo_f(t1) + lo_f(t2), hi_f(t1) + hi_f(t2));
                }
                *(u32x2*)(dst + mt * 16) = o;
            }
        }
        STAMP(0, 7);
        STAMP_DUMP;
        return;
    }

    const int wv = w - 4, ch = wv >> 1, hf = wv & 1;
    const int d = xcd * 64 + li * 2 + ch;
    const int n = hf * 64 + lane;
    u32 r_are, r_aim, r_bre, r_bim, r_cre, r_cim, r_ldt, r_dsk, r_ep;
    {
        const unsigned po = (unsigned)(d * NS + n) * 2u;
        asm volatile("global_load_ushort %0, %9, %10\n\tglobal_load_ushort %1, %9, %11\n\t"
                     "global_load_ushort %2, %9, %12\n\tglobal_load_ushort %3, %9, %13\n\t"
                     "global_load_ushort %4, %9, %14\n\tglobal_load_ushort %5, %9, %15\n\t"
                     "global_load_ushort %6, %16, off\n\tglobal_load_ushort %7, %17, off\n\t"
                     "global_load_dword %8, %18, off sc1"
                     : "=&v"(r_are), "=&v"(r_aim), "=&v"(r_bre), "=&v"(r_bim), "=&v"(r_cre), "=&v"(r_cim), "=&v"(r_ldt), "=&v"(r_dsk), "=&v"(r_ep)
                     : "v"(po), "s"(Are), "s"(Aim), "s"(Bre), "s"(Bim), "s"(Cre), "s"(Cim), "v"(logdt + d), "v"(Dskip + d), "v"(gf + GF_EPOCH)
                     : "memory");
    }
    u32 dummy = 0;
    {
        const char* base = (const char*)w1 + ((size_t)(li * 64) * F + (xcd >> 1) * 448) * 2;
#pragma unroll
        for (int i = 0; i < T1; i++) {
            int idx = (i * 4 + wv) * 64 + lane;
            idx = idx < 448 ? idx : 447;
            const int row = idx / 7, sec = idx - row * 7;
            touch(dummy, (unsigned)(row * (F * 2) + sec * 128), base);
        }
    }
    asm volatile("s_waitcnt vmcnt(" STR(T1) ")" : "+v"(r_are), "+v"(r_aim), "+v"(r_bre), "+v"(r_bim), "+v"(r_cre), "+v"(r_cim), "+v"(r_ldt), "+v"(r_dsk), "+v"(r_ep) :: "memory");
    const u32 tag = r_ep + 1u;
    const unsigned sb_off = SSM0 + ch * SSM_SIZE;
    char* sb = smem + sb_off;
    unsigned* fl = lf + LF_SSM + ch * 4;
    const float dt = rbf(fast_exp(lo_f(r_ldt)));
    const float zr = dt * lo_f(r_are), zi = dt * lo_f(r_aim);
    const float dsk = lo_f(r_dsk);
    f32x2 W;
    {
        const f32x2 B = {dt * lo_f(r_bre), dt * lo_f(r_bim)};
        const f32x2 C = {lo_f(r_cre), lo_f(r_cim)};
        W = cmul(C, B);
    }
    u32x4 kix[2][2];
#pragma unroll
    for (int i = 0; i < 2; i++) {
        const u32x4* pt = (const u32x4*)(KIDX.v + (hf * 128 + i * 64 + lane) * 16);
        kix[i][0] = pt[0]; kix[i][1] = pt[1];
    }
    if (T1 < 2 && TMODE == 1) {
        lds_wait(&lf[LF_UARR], 4);
        const char* base = (const char*)w1 + ((size_t)(li * 64) * F + (xcd >> 1) * 448) * 2;
#pragma unroll
        for (int i = T1; i < 2; i++) {
            int idx = (i * 4 + wv) * 64 + lane;
            idx = idx < 448 ? idx : 447;
            const int row = idx / 7, sec = idx - row * 7;
            touch(dummy, (unsigned)(row * (F * 2) + sec * 128), base);
        }
    }
    STAMP(0, 1);
    char* tbl = sb + SSM_COPY + hf * TBL_SIZE;
    char* tw = tbl + lane * 8;
    const char* trd = tbl + (lane & 15) * TBL_PITCH + (lane >> 4) * 16;
    u32x4 Bh[8], Bl[8];
    f32x4 kacc[3];
    {
        f32x2 E8 = cexp_scaled(zr, zi, 8.f);
        E8.y = -E8.y;
        f32x2 pw[16];
        pw[0] = f32x2{1.f, 0.f};
        pw[1] = E8;
#pragma unroll
        for (int q = 2; q < 16; q++) pw[q] = (q & 1) ? cmul(pw[q - 1], E8) : cmul(pw[q / 2], pw[q / 2]);
#pragma unroll
        for (int q = 0; q < 16; q++) *(u32x2*)(tw + q * TBL_PITCH) = split_bf(pw[q]);
        wait_lgkm0();
#pragma unroll
        for (int T = 0; T < 8; T++) {
            u32x4 v = *(const u32x4*)(trd + T * 64);
            Bh[T] = u32x4{v[0], v[0], v[2], v[2]};
            Bl[T] = u32x4{v[1], v[1], v[3], v[3]};
        }
        wait_lgkm0();
    }
    if (T1 < 2 && TMODE != 1) {
        if (TMODE == 0) lds_wait(&lf[LF_LNST], 4);
        else lds_wait(&lf[LF_H], 1);
        const char* base = (const char*)w1 + ((size_t)(li * 64) * F + (xcd >> 1) * 448) * 2;
#pragma unroll
        for (int i = T1; i < 2; i++) {
            int idx = (i * 4 + wv) * 64 + lane;
            idx = idx < 448 ? idx : 447;
            const int row = idx / 7, sec = idx - row * 7;
            touch(dummy, (unsigned)(row * (F * 2) + sec * 128), base);
        }
    }
    {
        const f32x2 E1 = cexp_scaled(zr, zi, 1.f);
        const f32x2 E128 = cexp_scaled(zr, zi, 128.f);
        f32x2 WY[8];
        {
            f32x2 Y[8];
            Y[0] = f32x2{1.f, 0.f}; Y[1] = E1;
#pragma unroll
            for (int c = 2; c < 8; c++) Y[c] = (c & 1) ? cmul(Y[c - 1], E1) : cmul(Y[c / 2], Y[c / 2]);
            WY[0] = W;
#pragma unroll
            for (int c = 1; c < 8; c++) WY[c] = cmul(W, Y[c]);
        }
        f32x2 Xp[17];
        Xp[0] = f32x2{1.f, 0.f}; Xp[1] = E128;
#pragma unroll
        for (int a = 2; a < 17; a++) Xp[a] = (a & 1) ? cmul(Xp[a - 1], E128) : cmul(Xp[a / 2], Xp[a / 2]);
#pragma unroll
        for (int tile = 0; tile < 3; tile++) {
#pragma unroll
            for (int r = 0; r < 16; r++) {
                f32x2 v;
                bool valid = true;
                if (tile == 0) { v = (r < 8) ? WY[r] : cmul(WY[r - 8], Xp[1]); }
                else if (tile == 1) {
                    if (r < 8) v = cmul(WY[2 * (r & 3)], Xp[2 + (r >> 2)]);
                    else v = cmul(WY[4 * (r & 1)], Xp[4 + ((r - 8) >> 1)]);
                } else {
                    if (r < 8) v = cmul(W, Xp[8 + r]);
                    else if (r == 8) v = cmul(W, Xp[16]);
                    else valid = false;
                }
                if (valid) *(u32x2*)(tw + r * TBL_PITCH) = split_bf(v);
            }
            wait_lgkm0();
            f32x4 a4 = {0.f, 0.f, 0.f, 0.f};
#pragma unroll
            for (int T = 0; T < 8; T++) {
                u32x4 av = *(const u32x4*)(trd + T * 64);
                a4 = mfma16(av, Bh[T], a4);
                a4 = mfma16(av, Bl[T], a4);
            }
            kacc[tile] = a4;
            wait_lgkm0();
        }
    }
    STAMP(0, 2);
    {
        float* P = (float*)(sb + SSM_P + hf * 3456);
        const int mq = lane >> 4, col = lane & 15;
#pragma unroll
        for (int tile = 0; tile < 3; tile++)
#pragma unroll
            for (int r = 0; r < 4; r++)
                if (!(tile == 2 && r == 1 && 0)) P[(tile * 16 + mq * 4 + r) * 18 + col] = kacc[tile][r];
        wait_lgkm0();
        if (lane == 0) P[KC_ZERO * 18] = 0.f;
    }
    if (hf == 0 && lane < 32) *(u32x4*)(sb + SSM_U + lane * 16) = u32x4{0, 0, 0, 0};
    lds_arrive(&fl[0], lane);
    touch_done(dummy);
    tie(cdummy);
    if (wv == 0) {
        poll_flags256(gf + GF_H2, tag, lane);
        if (lane == 0) __hip_atomic_store(&lf[LF_SSM + 9], 1u, __ATOMIC_RELAXED, __HIP_MEMORY_SCOPE_WORKGROUP);
    } else {
        lds_wait(&lf[LF_SSM + 9], 1);
    }
    u32x4 ug0, ug1;
    const int up0 = hf * 128 + lane, up1 = up0 + 64;
    {
        const int dt_ = d >> 6, nt = (d >> 4) & 3, m = d & 15;
        const char* src = (const char*)uF + ((size_t)(dt_ * 64 * 4 + nt) * 64 + m) * 16;
        const char* pa = src + (up0 >> 2) * 4096 + (up0 & 3) * 256;
        const char* pb = src + (up1 >> 2) * 4096 + (up1 & 3) * 256;
        asm volatile("global_load_dwordx4 %0, %2, off sc1\n\tglobal_load_dwordx4 %1, %3, off sc1"
                     : "=&v"(ug0), "=&v"(ug1) : "v"(pa), "v"(pb) : "memory");
    }
    lds_wait(&fl[0], 2);
    STAMP(0, 3);
    {
        const char* P0 = sb + SSM_P;
        const char* P1 = sb + SSM_P + 3456;
#pragma unroll
        for (int i = 0; i < 2; i++) {
            const int ub = hf * 128 + i * 64 + lane;
            float v[11];
#pragma unroll
            for (int j = 0; j < 11; j++) {
                const u32 word = kix[i][j >> 3][(j >> 1) & 3];
                const unsigned off = (j & 1) ? (word >> 16) : (word & 0xffffu);
                v[j] = *(const float*)(P0 + off) + *(const float*)(P1 + off);
            }
            u32 e[5], o[5];
#pragma unroll
            for (int k = 0; k < 5; k++) { e[k] = pk2bf(v[2 * k], v[2 * k + 1]); o[k] = pk2bf(v[2 * k + 1], v[2 * k + 2]); }
            char* dst = sb + SSM_COPY + ub * 16;
            *(u32x4*)(dst) = u32x4{e[0], e[1], e[2], e[3]};
            *(u32x4*)(dst + CP_STRIDE) = u32x4{o[0], o[1], o[2], o[3]};
            *(u32x4*)(dst + 2 * CP_STRIDE) = u32x4{e[1], e[2], e[3], e[4]};
            *(u32x4*)(dst + 3 * CP_STRIDE) = u32x4{o[1], o[2], o[3], o[4]};
        }
        if (hf == 0 && lane < 8) {
            *(u32x4*)(sb + SSM_COPY + (lane >> 1) * CP_STRIDE + (256 + (lane & 1)) * 16) = u32x4{0, 0, 0, 0};
        }
    }
    STAMP(0, 4);
    asm volatile("s_waitcnt vmcnt(0)" : "+v"(ug0), "+v"(ug1) :: "memory");
    *(u32x4*)(sb + SSM_U + 512 + up0 * 16) = ug0;
    *(u32x4*)(sb + SSM_U + 512 + up1 * 16) = ug1;
    lds_arrive(&fl[1], lane);
    lds_wait(&fl[1], 2);
    STAMP(0, 5);
    f32x4 cacc[8];
#pragma unroll
    for (int i = 0; i < 8; i++) cacc[i] = f32x4{0.f, 0.f, 0.f, 0.f};
    {
        const int m = lane & 15, q = lane >> 4;
        const int gq = m >> 2, e = m & 3, c = 3 - e;
        const char* abase = sb + SSM_COPY + c * CP_STRIDE + 4104 - 8 * gq - 16 * q - 64 * 63 + 64 * (7 - hf * 4 - 3);
        const char* bbase = sb + SSM_U + 528 + 32 * m - 16 * q - 64 * 7 + 64 * (7 - hf * 4 - 3);
#pragma unroll
        for (int rr = 0; rr < 4; rr++) {
            const char* ab = abase + 64 * (3 - rr);
            const char* bb = bbase + 64 * (3 - rr);
            u32x4 A[8], B[8];
#pragma unroll
            for (int pp = 0; pp < 8; pp++) {
                const u32x2 a0 = *(const u32x2*)(ab + 64 * (56 - 8 * pp));
                const u32x2 a1 = *(const u32x2*)(ab + 64 * (56 - 8 * pp) + 8);
                A[pp] = u32x4{a0[0], a0[1], a1[0], a1[1]};
                B[pp] = *(const u32x4*)(bb + 512 * pp);
            }
#pragma unroll
            for (int i = 0; i < 8; i++)
#pragma unroll
                for (int pp = 0; pp <= i; pp++) cacc[i] = mfma16(A[pp], B[i - pp], cacc[i]);
        }
    }
    STAMP(0, 6);
    f32x4 keep[4];
    {
        char* xo = sb + SSM_X + hf * 4096;
        if (hf) {
#pragma unroll
            for (int i = 0; i < 4; i++) { *(f32x4*)(xo + i * 1024 + lane * 16) = cacc[i]; keep[i] = cacc[4 + i]; asm volatile("" : "+v"(keep[i])); }
        } else {
#pragma unroll
            for (int i = 0; i < 4; i++) { *(f32x4*)(xo + i * 1024 + lane * 16) = cacc[4 + i]; keep[i] = cacc[i]; asm volatile("" : "+v"(keep[i])); }
        }
    }
    lds_arrive(&fl[2], lane);
    lds_wait(&fl[2], 2);
    {
        const char* xi = sb + SSM_X + (hf ^ 1) * 4096;
        const int nn = lane & 15, mq = lane >> 4;
#pragma unroll
        for (int i = 0; i < 4; i++) {
            f32x4 v = keep[i];
            v += *(const f32x4*)(xi + i * 1024 + lane * 16);
            const int t = 256 * (hf * 4 + i) + 16 * nn + 4 * mq;
            const u32x2 uu = *(const u32x2*)(sb + SSM_U + 512 + t * 2);
            u32 y0 = pk2bf(v[0], v[1]), y1 = pk2bf(v[2], v[3]);
            u32 s0 = pk2bf(lo_f(uu[0]) * dsk, hi_f(uu[0]) * dsk), s1 = pk2bf(lo_f(uu[1]) * dsk, hi_f(uu[1]) * dsk);
            u32x2 o = {pk2bf(lo_f(y0) + lo_f(s0), hi_f(y0) + hi_f(s0)), pk2bf(lo_f(y1) + lo_f(s1), hi_f(y1) + hi_f(s1))};
            st_sc1_8(aT + (size_t)d * L + t, o);
        }
    }
    if (wv != 0) {
        const char* base = (const char*)w2 + ((size_t)(li * 56) * L + (xcd >> 1) * 512) * 2;
#pragma unroll
        for (int i = 0; i < 3; i++) {
            int idx = ((wv - 1) * 3 + i) * 64 + lane;
            idx = idx < 448 ? idx : 447;
            touch(dummy, (unsigned)((idx >> 3) * (L * 2) + (idx & 7) * 128), base);
        }
        wait_vm0<3>();
    } else {
        wait_vm0<0>();
    }
    lds_arrive(&lf[LF_APUB], lane);
    if (wv == 0) {
        lds_wait(&lf[LF_APUB], 4);
        if (lane == 0) st_sc1_4(gf + GF_A + xcd * 32 + li, tag);
    }
    STAMP(0, 7);
    if (wv == 0) {
        poll_flags(gf + GF_A + dt2 * 32, 32, tag, lane);
        if (lane == 0) __hip_atomic_store(&lf[LF_SSM + 8], 1u, __ATOMIC_RELAXED, __HIP_MEMORY_SCOPE_WORKGROUP);
    } else {
        lds_wait(&lf[LF_SSM + 8], 1);
    }
    {
        const int r = 16 * wv + (lane >> 2);
        const int j0 = (lane & 3) * 2, j1 = j0 + 1;
        const char* src = (const char*)aT + ((size_t)(dt2 * 64 + r) * L + st * 64) * 2;
        u32x4 a, c;
        ld2_sc1_16(a, c, src + j0 * 16, src + j1 * 16);
        const int f = ((r >> 1) & 1) | (((r >> 3) & 1) << 1);
        *(u32x4*)(smem + STG + r * 128 + ((j0 ^ (f << 1)) << 4)) = a;
        *(u32x4*)(smem + STG + r * 128 + ((j1 ^ (f << 1)) << 4)) = c;
        if (lane < 16) {
            const int s = st * 64 + wv * 16 + lane;
            const u32 gv = ld_sc1_4(g + s);
            *(u32*)(smem + STG + 8192 + (wv * 16 + lane) * 4) = gv;
            *(float*)(smem + STG + 8192 + 256 + (wv * 16 + lane) * 4) = bf2f(b2[s]);
        }
    }
    lds_arrive(&lf[LF_EPI], lane);
    touch_done(dummy);
    STAMP(0, 8);
    STAMP_DUMP;
}

extern "C" void kernel_launch(void* const* d_in, const int* in_sizes, int n_in,
                              void* d_out, int out_size, void* d_ws, size_t ws_size,
                              hipStream_t stream) {
    const u16* u = (const u16*)d_in[0];
    const u16* Are = (const u16*)d_in[1];
    const u16* Aim = (const u16*)d_in[2];
    const u16* Bre = (const u16*)d_in[3];
    const u16* Bim = (const u16*)d_in[4];
    const u16* Cre = (const u16*)d_in[5];
    const u16* Cim = (const u16*)d_in[6];
    const u16* logdt = (const u16*)d_in[7];
    const u16* Dskip = (const u16*)d_in[8];
    const u16* w1 = (const u16*)d_in[10];
    const u16* b1 = (const u16*)d_in[11];
    const u16* w2 = (const u16*)d_in[12];
    const u16* b2 = (const u16*)d_in[13];
    const u16* gw = (const u16*)d_in[14];
    k_fused<<<256, 512, 0, stream>>>(u, gw, Are, Aim, Bre, Bim, Cre, Cim, logdt, Dskip, w1, b1, w2, b2, (char*)d_ws, (u16*)d_out);
}
```

```cpp
#include <hip/hip_runtime.h>
#include <stdint.h>

typedef unsigned short u16;
typedef unsigned int u32;
using bf16x8 = __attribute__((ext_vector_type(8))) __bf16;
using bf16x2 = __attribute__((ext_vector_type(2))) __bf16;
using s16x4 = __attribute__((ext_vector_type(4))) short;
using f32x4 = __attribute__((ext_vector_type(4))) float;
using f32x2 = __attribute__((ext_vector_type(2))) float;
using u32x4 = __attribute__((ext_vector_type(4))) unsigned;
using u32x2 = __attribute__((ext_vector_type(2))) unsigned;
typedef __attribute__((address_space(3))) s16x4* lds_s16x4_ptr;

#if defined(__HIP_DEVICE_COMPILE__)
#define NOPK __attribute__((target("no-packed-fp32-ops")))
#else
#define NOPK
#endif
#define DEV NOPK __device__ __forceinline__
#define TIDX ((int)__builtin_amdgcn_workitem_id_x())
#define BIDX ((int)__builtin_amdgcn_workgroup_id_x())

#define STAMP_DECL do {} while (0)
#define STAMP(kern, idx) do {} while (0)
#define STAMP_DUMP do {} while (0)

constexpr int L = 2048, D = 512, NS = 128, F = 1792;
#define T1 1
#define TMODE 0
#define CONVDEFER 0
#define TLBWARM 1
#define CODE_BYTES 30000
#define RK 0
#define ROT1 ((16 * 4 - RK * (ftl + dtl)) & 15)
#define ROT2 ((14 * 4 - RK * (stl + dtl2)) % 14)
#define STR_(x) #x
#define STR(x) STR_(x)

DEV float bf2f(u16 v) { return __builtin_bit_cast(float, ((unsigned)v) << 16); }
DEV u32 pk2bf(float a, float b) {
    f32x2 v = {a, b};
    return __builtin_bit_cast(u32, __builtin_convertvector(v, bf16x2));
}
DEV float lo_f(u32 p) { return __builtin_bit_cast(float, p << 16); }
DEV float hi_f(u32 p) { return __builtin_bit_cast(float, p & 0xffff0000u); }
DEV float rbf(float f) { return lo_f(pk2bf(f, 0.f)); }

DEV u32x2 tr_read(const char* lds_generic_base, unsigned off) {
    s16x4 v = __builtin_amdgcn_ds_read_tr16_b64_v4i16((lds_s16x4_ptr)(lds_generic_base + off));
    return __builtin_bit_cast(u32x2, v);
}
DEV f32x4 mfma16(u32x4 a, u32x4 b, f32x4 c) {
    return __builtin_amdgcn_mfma_f32_16x16x32_bf16(__builtin_bit_cast(bf16x8, a), __builtin_bit_cast(bf16x8, b), c, 0, 0, 0);
}

DEV void gload_early8(u32x2& dst, const void* p) { asm volatile("global_load_dwordx2 %0, %1, off" : "=v"(dst) : "v"(p) : "memory"); }
template <typename T> DEV void tie(T& x) { asm volatile("" : "+v"(x) :: "memory"); }
DEV void touch(u32& dummy, unsigned voff, const void* sbase) {
    asm volatile("global_load_dword %0, %1, %2" : "+v"(dummy) : "v"(voff), "s"(sbase) : "memory");
}
DEV void touch_done(u32& dummy) { asm volatile("s_waitcnt vmcnt(0)" : "+v"(dummy) :: "memory"); }
template <int N>
DEV void wait_vm(u32x4& a, u32x4& b, u32x4& c, u32x4& d) {
    asm volatile("s_waitcnt vmcnt(%4)" : "+v"(a), "+v"(b), "+v"(c), "+v"(d) : "i"(N) : "memory");
}
template <int N>
DEV void wait_vm0() { asm volatile("s_waitcnt vmcnt(%0)" ::"i"(N) : "memory"); }
DEV void wait_lgkm0() { asm volatile("s_waitcnt lgkmcnt(0)" ::: "memory"); }
DEV u32 ld_sc1_4(const void* p) {
    u32 v; asm volatile("global_load_dword %0, %1, off sc1\n\ts_waitcnt vmcnt(0)" : "=&v"(v) : "v"(p) : "memory"); return v;
}
DEV u32x4 ld_sc1_16(const void* p) {
    u32x4 v; asm volatile("global_load_dwordx4 %0, %1, off sc1\n\ts_waitcnt vmcnt(0)" : "=&v"(v) : "v"(p) : "memory"); return v;
}
DEV void ld2_sc1_16(u32x4& a, u32x4& b, const void* pa, const void* pb) {
    asm volatile("global_load_dwordx4 %0, %2, off sc1\n\tglobal_load_dwordx4 %1, %3, off sc1\n\ts_waitcnt vmcnt(0)"
                 : "=&v"(a), "=&v"(b) : "v"(pa), "v"(pb) : "memory");
}
DEV void st_sc1_16(void* p, u32x4 v) { asm volatile("global_store_dwordx4 %0, %1, off sc1\n\ts_nop 1" :: "v"(p), "v"(v) : "memory"); }
DEV void st_sc1_8(void* p, u32x2 v) { asm volatile("global_store_dwordx2 %0, %1, off sc1\n\ts_nop 1" :: "v"(p), "v"(v) : "memory"); }
DEV void st_sc1_4(void* p, u32 v) { asm volatile("global_store_dword %0, %1, off sc1" :: "v"(p), "v"(v) : "memory"); }

template <int CTRL>
DEV float dpp_add(float v) {
    int t = __builtin_amdgcn_update_dpp(0, __builtin_bit_cast(int, v), CTRL, 0xf, 0xf, true);
    return v + __builtin_bit_cast(float, t);
}
DEV float rdlane(float v, int l) { return __builtin_bit_cast(float, __builtin_amdgcn_readlane(__builtin_bit_cast(int, v), l)); }
DEV float wave_sum(float v) {
    v = dpp_add<0xB1>(v);
    v = dpp_add<0x4E>(v);
    v = dpp_add<0x141>(v);
    v = dpp_add<0x140>(v);
    return (rdlane(v, 0) + rdlane(v, 16)) + (rdlane(v, 32) + rdlane(v, 48));
}
DEV float fast_exp(float x) { return __builtin_amdgcn_exp2f(x * 1.4426950408889634f); }
DEV void lds_arrive(unsigned* cnt, int lane) {
    wait_lgkm0();
    if (lane == 0) __hip_atomic_fetch_add(cnt, 1u, __ATOMIC_RELAXED, __HIP_MEMORY_SCOPE_WORKGROUP);
}
DEV void lds_wait(unsigned* cnt, unsigned target) {
    while ((unsigned)__builtin_amdgcn_readfirstlane((int)__hip_atomic_load(cnt, __ATOMIC_RELAXED, __HIP_MEMORY_SCOPE_WORKGROUP)) < target)
        __builtin_amdgcn_s_sleep(1);
    asm volatile("" ::: "memory");
}
constexpr int SPIN_CAP = 20000;
DEV void poll_flags256(const u32* fl, u32 tag, int lane) {
#pragma unroll 1
    for (int spin = 0; spin < SPIN_CAP; spin++) {
        u32x4 v = ld_sc1_16(fl + lane * 4);
        bool ok = v[0] == tag && v[1] == tag && v[2] == tag && v[3] == tag;
        if (__builtin_amdgcn_ballot_w64(ok) == ~0ull) break;
        __builtin_amdgcn_s_sleep(2);
    }
}
DEV void poll_flags(const u32* fl, int n, u32 tag, int lane) {
    const u32* p = fl + (lane < n ? lane : 0);
#pragma unroll 1
    for (int spin = 0; spin < SPIN_CAP; spin++) {
        u32 v = ld_sc1_4(p);
        if (__builtin_amdgcn_ballot_w64(v == tag) == ~0ull) break;
        __builtin_amdgcn_s_sleep(2);
    }
}
DEV float div_nr(float a, float b, float r) {
    float q = a * r;
    float e = __builtin_fmaf(-q, b, a);
    return __builtin_fmaf(e, r, q);
}

struct KIdxTable { u16 v[256 * 16]; };
constexpr int round_bf16_int(int x) {
    if (x < 256) return x;
    int sh = 0;
    while ((x >> sh) >= 256) sh++;
    int q = x >> sh, r = x & ((1 << sh) - 1), half = 1 << (sh - 1);
    if (r > half || (r == half && (q & 1))) q++;
    return q << sh;
}
constexpr int KC_ZERO = 41;
constexpr int compact_pos(int tap) {
    int row = KC_ZERO, col = 0;
    if (tap >= 0) {
        int lr = round_bf16_int(tap);
        if (lr < 256) { row = (lr & 7) + 8 * (lr >> 7); col = (lr >> 3) & 15; }
        else if (lr < 512) { int x = lr - 256; row = 16 + ((x & 7) >> 1) + 4 * (x >> 7); col = (x >> 3) & 15; }
        else if (lr < 1024) { int x = lr - 512; row = 24 + ((x & 7) >> 2) + 2 * (x >> 7); col = (x >> 3) & 15; }
        else if (lr < 2048) { int x = lr - 1024; row = 32 + (x >> 7); col = (x >> 3) & 15; }
        else { row = 40; col = 0; }
    }
    return row * 18 + col;
}
constexpr KIdxTable make_kidx() {
    KIdxTable t{};
    for (int ub = 0; ub < 256; ub++)
        for (int i = 0; i < 16; i++) t.v[ub * 16 + i] = (u16)(4 * compact_pos(2047 - (8 * ub + i)));
    return t;
}
__device__ const KIdxTable KIDX = make_kidx();

DEV f32x2 cmul(f32x2 a, f32x2 b) {
    f32x2 r;
    r.x = __builtin_fmaf(-a.y, b.y, a.x * b.x);
    r.y = __builtin_fmaf(a.y, b.x, a.x * b.y);
    return r;
}
DEV f32x2 cexp_scaled(float zr, float zi, float l) {
    const float xr = zr * l, xi = zi * l;
    const float e = __builtin_amdgcn_exp2f(xr * 1.4426950408889634f);
    const float c_hi = 0.15915494309189535f;
    const float c_lo = 6.4206382e-09f;
    float rh = xi * c_hi;
    float rl = __builtin_fmaf(xi, c_lo, __builtin_fmaf(xi, c_hi, -rh));
    float fr = (rh - __builtin_rintf(rh)) + rl;
    return f32x2{e * __builtin_amdgcn_cosf(fr), e * __builtin_amdgcn_sinf(fr)};
}
DEV u32x2 split_bf(f32x2 v) {
    u32 h = pk2bf(v.x, v.y);
    u32 l = pk2bf(v.x - lo_f(h), v.y - hi_f(h));
    return u32x2{h, l};
}

DEV void dma_slot(unsigned lds_, const unsigned (&voff)[4], const void* sbase) {
    const unsigned lds = (unsigned)__builtin_amdgcn_readfirstlane((int)lds_);
    asm volatile("s_mov_b32 m0, %0\n\ts_nop 0\n\t"
                 "global_load_lds_dwordx4 %1, %5\n\t"
                 "global_load_lds_dwordx4 %2, %5 offset:1024\n\t"
                 "global_load_lds_dwordx4 %3, %5 offset:2048\n\t"
                 "global_load_lds_dwordx4 %4, %5 offset:3072"
                 :: "s"(lds), "v"(voff[0]), "v"(voff[1]), "v"(voff[2]), "v"(voff[3]), "s"(sbase) : "memory", "m0");
}
DEV void xload(u32x4 (&x)[4], unsigned voff, const void* sbase) {
    asm volatile("global_load_dwordx4 %0, %4, %5 sc1\n\t"
                 "global_load_dwordx4 %1, %4, %5 offset:1024 sc1\n\t"
                 "global_load_dwordx4 %2, %4, %5 offset:2048 sc1\n\t"
                 "global_load_dwordx4 %3, %4, %5 offset:3072 sc1"
                 : "+v"(x[0]), "+v"(x[1]), "+v"(x[2]), "+v"(x[3]) : "v"(voff), "s"(sbase) : "memory");
}
template <int S, int WPITCH, bool W_IS_A>
struct GemmCtx {
    const char* smem;
    const char* wbase;
    const char* xbase;
    unsigned ring;
    int nxt, nxtx;
    unsigned wvoff[4];
    unsigned xvoff;
    unsigned raddr[4];
    u32x4 X[4][4];
    f32x4 acc[4][4];
};
template <int J, int S, int WPITCH, bool W_IS_A>
DEV void g_issue_w(GemmCtx<S, WPITCH, W_IS_A>& c) {
    dma_slot(c.ring + J * 4096, c.wvoff, c.wbase + (size_t)((unsigned)c.nxt * (unsigned)(32 * WPITCH)));
    c.nxt = (c.nxt + 1 == S) ? 0 : c.nxt + 1;
}
template <int J, int S, int WPITCH, bool W_IS_A>
DEV void g_issue_x(GemmCtx<S, WPITCH, W_IS_A>& c) {
    xload(c.X[J], c.xvoff, c.xbase + (size_t)((unsigned)c.nxtx * 4096u));
    c.nxtx = (c.nxtx + 1 == S) ? 0 : c.nxtx + 1;
}
template <int J, int N, bool IX, bool IW, int S, int WPITCH, bool W_IS_A>
DEV void g_step(GemmCtx<S, WPITCH, W_IS_A>& c) {
    u32x4(&x)[4] = c.X[J];
    wait_vm<N>(x[0], x[1], x[2], x[3]);
    u32x4 Wf[4];
#pragma unroll
    for (int i = 0; i < 4; i++) {
        u32x2 a = tr_read(c.smem, c.raddr[i] + J * 4096);
        u32x2 b = tr_read(c.smem, c.raddr[i] + J * 4096 + 512);
        Wf[i] = u32x4{a[0], a[1], b[0], b[1]};
    }
    if constexpr (IX) g_issue_x<(J + 3) & 3>(c);
    if constexpr (IW) {
        asm volatile("s_waitcnt lgkmcnt(0)" : "+v"(Wf[0]), "+v"(Wf[1]), "+v"(Wf[2]), "+v"(Wf[3]) :: "memory");
        g_issue_w<J>(c);
    }
#pragma unroll
    for (int i = 0; i < 4; i++)
#pragma unroll
        for (int j = 0; j < 4; j++) {
            if constexpr (W_IS_A) c.acc[i][j] = mfma16(Wf[i], x[j], c.acc[i][j]);
            else c.acc[i][j] = mfma16(x[i], Wf[j], c.acc[i][j]);
        }
    __builtin_amdgcn_sched_barrier(0);
}
template <int S, int WPITCH, bool W_IS_A>
DEV void gemm_begin(GemmCtx<S, WPITCH, W_IS_A>& c, const char* smem, const char* wbase0, size_t wstride, const char* xbase, unsigned ring0, int w, int rot, int lane) {
    c.smem = smem; c.wbase = wbase0 + w * wstride; c.xbase = xbase; c.ring = ring0 + w * 16384; c.nxt = rot; c.nxtx = rot;
#pragma unroll
    for (int i = 0; i < 4; i++) {
        const int sw = (((lane >> 4) & 1) << 1) | ((i & 1) << 2);
        c.wvoff[i] = (unsigned)((8 * i + (lane >> 3)) * WPITCH + (((lane & 7) ^ sw) << 4) - 1024 * i);
    }
    c.xvoff = lane * 16;
    const int g = lane >> 4, q = (lane & 15) >> 2, p = lane & 3;
    const int fl = ((q >> 1) & 1) | ((g & 1) << 1);
#pragma unroll
    for (int i = 0; i < 4; i++) c.raddr[i] = c.ring + (8 * g + q) * 128 + ((i ^ fl) << 5) + p * 8;
    if (w == 0) {
        int k = rot + 4;
        c.nxt = k >= S ? k - S : k;
    } else {
        if (w == 1) {
            int k = rot;
#pragma unroll
            for (int j = 0; j < 4; j++) {
                dma_slot(ring0 + j * 4096, c.wvoff, wbase0 + (size_t)((unsigned)k * (unsigned)(32 * WPITCH)));
                k = (k + 1 == S) ? 0 : k + 1;
            }
        }
        g_issue_w<0>(c); g_issue_w<1>(c); g_issue_w<2>(c); g_issue_w<3>(c);
    }
}
template <int S, int WPITCH, bool W_IS_A>
DEV void gemm_run(GemmCtx<S, WPITCH, W_IS_A>& c, unsigned* w0flag, unsigned w0val, int w
) {
#pragma unroll
    for (int i = 0; i < 4; i++)
#pragma unroll
        for (int j = 0; j < 4; j++) { c.acc[i][j] = f32x4{0.f, 0.f, 0.f, 0.f}; c.X[i][j] = u32x4{0, 0, 0, 0}; }
    g_issue_x<0>(c); g_issue_x<1>(c); g_issue_x<2>(c);
    if (w == 0) lds_wait(w0flag, w0val);
    g_step<0, 8, true, true>(c);
    g_step<1, 12, true, true>(c);
    g_step<2, 16, true, true>(c);
    g_step<3, 20, true, true>(c);
    constexpr int STEADY = S - 8;
#pragma unroll 1
    for (int it = 0; it < STEADY / 4; it++) {
        g_step<0, 20, true, true>(c);
        g_step<1, 20, true, true>(c);
        g_step<2, 20, true, true>(c);
        g_step<3, 20, true, true>(c);
    }
    constexpr int R = STEADY % 4;
    if constexpr (R >= 1) g_step<0, 20, true, true>(c);
    if constexpr (R >= 2) g_step<1, 20, true, true>(c);
    if constexpr (R >= 3) g_step<2, 20, true, true>(c);
    g_step<(R + 0) & 3, 20, true, false>(c);
    g_step<(R + 1) & 3, 16, false, false>(c);
    g_step<(R + 2) & 3, 8, false, false>(c);
    g_step<(R + 3) & 3, 0, false, false>(c);
}

constexpr int RING = 0;
constexpr int STG = 65536;
constexpr int SSM0 = STG + 16384;
constexpr int TBL_PITCH = 528;
constexpr int TBL_SIZE = 16 * TBL_PITCH;
constexpr int CP_STRIDE = 4160;
constexpr int SSM_COPY = 0;
constexpr int SSM_U = 16896;
constexpr int SSM_P = SSM_U + 4608;
constexpr int SSM_X = SSM_P + 2 * 3456;
constexpr int SSM_SIZE = SSM_X + 8192;
constexpr int LFLAGS = SSM0 + 2 * SSM_SIZE;
constexpr int LDS_BYTES = LFLAGS + 128;
enum { LF_LN = 0, LF_LNPUB = 1, LF_H = 2, LF_X1 = 3, LF_X1R = 4, LF_UPUB = 5, LF_U = 6, LF_X2 = 7, LF_EPI = 8, LF_APUB = 9, LF_LNST = 10, LF_LNPUB2 = 11, LF_UARR = 24, LF_W0 = 25, LF_G1DONE = 26, LF_SSM = 12   };
constexpr int GF_EPOCH = 0, GF_H = 64, GF_H2 = 64 + 256, GF_U = 64 + 512, GF_A = 64 + 512 + 8 * 32;

NOPK __global__ __launch_bounds__(512) void k_fused(const u16* __restrict__ u, const u16* __restrict__ gw,
                                                    const u16* __restrict__ Are, const u16* __restrict__ Aim,
                                                    const u16* __restrict__ Bre, const u16* __restrict__ Bim,
                                                    const u16* __restrict__ Cre, const u16* __restrict__ Cim,
                                                    const u16* __restrict__ logdt, const u16* __restrict__ Dskip,
                                                    const u16* __restrict__ w1, const u16* __restrict__ b1,
                                                    const u16* __restrict__ w2, const u16* __restrict__ b2,
                                                    char* __restrict__ ws, u16* __restrict__ out) {
    __shared__ __attribute__((aligned(16))) char smem[LDS_BYTES];
    const int tid = TIDX, lane = tid & 63;
    const int w = __builtin_amdgcn_readfirstlane(tid >> 6);
    const int b = BIDX;
    const int xcd = b & 7, li = b >> 3;
    unsigned* lf = (unsigned*)(smem + LFLAGS);
    u16* hF = (u16*)ws;
    u16* uF = (u16*)(ws + (2 << 20));
    u16* aT = (u16*)(ws + (4 << 20));
    u16* UF = (u16*)(ws + (6 << 20));
    float* g = (float*)(ws + (8 << 20));
    u32* gf = (u32*)(ws + (9 << 20));
    const int stl = li & 7, dtl2 = li >> 3;
    const int st = (xcd >> 1) * 8 + stl, dt2 = (xcd & 1) * 4 + dtl2;
    STAMP_DECL;
    STAMP(0, 0);
    u32 cdummy = 0;
    if (w == 7) {
        const unsigned long long pc = __builtin_amdgcn_s_getpc() & ~127ull;
        int idx = li * 8 + lane;
        if (lane < 8 && idx * 128 < CODE_BYTES) touch(cdummy, (unsigned)(idx * 128), (const void*)pc);
    }
    if (w == 6 && li < 8) {
        const unsigned off = (unsigned)lane * 65536u;
        const char* base; unsigned lim;
        if (li < 3) { base = ws + (size_t)li * (4u << 20); lim = li < 2 ? (4u << 20) : (2u << 20); }
        else if (li < 5) { base = (const char*)w1 + (size_t)(li - 3) * (4u << 20); lim = li == 3 ? (4u << 20) : (unsigned)(L * F * 2 - (4 << 20)); }
        else if (li < 7) { base = (const char*)w2 + (size_t)(li - 5) * (4u << 20); lim = li == 5 ? (4u << 20) : (unsigned)(L * F * 2 - (4 << 20)); }
        else { base = (const char*)out; lim = 2u << 20; }
        if (off < lim) touch(cdummy, off, base);
    }
    if (tid < 32) lf[tid] = 0;
    wait_lgkm0();
    __builtin_amdgcn_s_barrier();

    if (w < 4) {
        __builtin_amdgcn_s_setprio(3);
        const int s0 = b * 8;
        u32x4 xv[2];
#pragma unroll
        for (int i = 0; i < 2; i++) xv[i] = *(const u32x4*)(u + (size_t)(s0 + 2 * w + i) * D + lane * 8);
        const u32x4 gv = *(const u32x4*)(gw + lane * 8);
        const u32 tag = ld_sc1_4(gf + GF_EPOCH) + 1u;
        STAMP(0, 8);
        lds_arrive(&lf[LF_UARR], lane);
        float gval[2];
#pragma unroll
        for (int i = 0; i < 2; i++) {
            const int r = 2 * w + i;
            float x[8];
#pragma unroll
            for (int j = 0; j < 4; j++) { x[2 * j] = lo_f(xv[i][j]); x[2 * j + 1] = hi_f(xv[i][j]); }
            float sum = 0.f, gs = 0.f;
#pragma unroll
            for (int j = 0; j < 4; j++) {
                sum += x[2 * j] + x[2 * j + 1];
                u32 p = pk2bf(x[2 * j] * lo_f(gv[j]), x[2 * j + 1] * hi_f(gv[j]));
                gs += lo_f(p) + hi_f(p);
            }
            sum = wave_sum(sum);
            gs = wave_sum(gs);
            const float mean = sum * (1.f / D);
            float sq = 0.f;
#pragma unroll
            for (int j = 0; j < 8; j++) { float dl = x[j] - mean; sq = __builtin_fmaf(dl, dl, sq); }
            sq = wave_sum(sq);
            const float var = rbf(sq * (1.f / D));
            const float m = rbf(mean);
            const float sd = rbf(__builtin_sqrtf(rbf(var + 1e-5f)));
            const float rs = __builtin_amdgcn_rcpf(sd);
            u32x4 hv;
#pragma unroll
            for (int j = 0; j < 4; j++) {
                u32 c = pk2bf(x[2 * j] - m, x[2 * j + 1] - m);
                hv[j] = pk2bf(div_nr(lo_f(c), sd, rs), div_nr(hi_f(c), sd, rs));
            }
            const int c16 = lane ^ ((r & 3) << 2);
            *(u32x4*)(smem + STG + r * 1024 + c16 * 16) = hv;
            *(u32x4*)(smem + STG + 8192 + r * 1024 + c16 * 16) = xv[i];
            gval[i] = rbf(__builtin_amdgcn_rcpf(1.f + fast_exp(-rbf(gs))));
        }
        STAMP(0, 10);
        lds_arrive(&lf[LF_LN], lane);
        lds_wait(&lf[LF_LN], 4);
        STAMP(0, 11);
        u32x4 ukeep[2];
        size_t uoff[2];
        {
            const int nt = lane >> 4, q = (lane & 15) >> 2, p = lane & 3;
            const int ks = b >> 2, kq = b & 3;
#pragma unroll
            for (int i = 0; i < 2; i++) {
                const int dt = w * 2 + i;
                const int cb = dt * 4 + nt;
                const unsigned o0 = q * 1024 + ((cb ^ (q << 1)) << 5) + p * 8;
                const unsigned o1 = o0 + 4096;
                u32x2 h0 = tr_read(smem + STG, o0), h1 = tr_read(smem + STG, o1);
                u32x2 u0 = tr_read(smem + STG + 8192, o0), u1 = tr_read(smem + STG + 8192, o1);
                const size_t off = ((size_t)((dt * 64 + ks) * 4 + nt) * 64 + kq * 16 + (lane & 15)) * 8;
                st_sc1_16(hF + off, u32x4{h0[0], h0[1], h1[0], h1[1]});
                ukeep[i] = u32x4{u0[0], u0[1], u1[0], u1[1]};
                uoff[i] = off;
            }
        }
        STAMP(0, 12);
        wait_vm0<0>();
        STAMP(0, 13);
        lds_arrive(&lf[LF_LNPUB], lane);
        if (w == 0) {
            lds_wait(&lf[LF_LNPUB], 4);
            if (lane == 0) st_sc1_4(gf + GF_H + b, tag);
            lds_arrive(&lf[LF_LNPUB], lane);
        } else {
            lds_wait(&lf[LF_LNPUB], 5);
        }
        st_sc1_16(uF + uoff[0], ukeep[0]);
        st_sc1_16(uF + uoff[1], ukeep[1]);
        if (lane < 2) st_sc1_4(g + s0 + 2 * w + lane, __builtin_bit_cast(u32, lane ? gval[1] : gval[0]));
        lds_arrive(&lf[LF_LNST], lane);
        __builtin_amdgcn_s_setprio(0);
        STAMP(0, 1);
        const bool has1 = li < 28;
        const int ftl = li % 7, dtl = li / 7;
        const int ft = (xcd >> 1) * 7 + ftl, dt = (xcd & 1) * 4 + dtl;
        u32x2 bb[4];
        GemmCtx<16, F * 2, true> c1;
        wait_vm0<0>();
        lds_arrive(&lf[LF_LNPUB2], lane);
        if (w == 1) {
            lds_wait(&lf[LF_LNPUB2], 4);
            if (lane == 0) st_sc1_4(gf + GF_H2 + b, tag);
            lds_arrive(&lf[LF_LNPUB2], lane);
        } else {
            lds_wait(&lf[LF_LNPUB2], 5);
        }
        if (has1) {
#pragma unroll
            for (int mt = 0; mt < 4; mt++) gload_early8(bb[mt], b1 + ft * 64 + mt * 16 + (lane >> 4) * 4);
            gemm_begin(c1, smem, (const char*)w1 + ((size_t)ft * 64) * 2, (size_t)512 * F * 2,
                       (const char*)hF + ((size_t)(dt * 64 + w * 16)) * 4096, RING, w, ROT1, lane);
        }
        if (w == 0) {
            poll_flags256(gf + GF_H, tag, lane);
            if (lane == 0) __hip_atomic_store(&lf[LF_H], 1u, __ATOMIC_RELAXED, __HIP_MEMORY_SCOPE_WORKGROUP);
        } else {
            lds_wait(&lf[LF_H], 1);
        }
        STAMP(0, 2);
        if (has1) {
            if (w == 1) { wait_vm0<16>(); if (lane == 0) __hip_atomic_store(&lf[LF_W0], 1u, __ATOMIC_RELAXED, __HIP_MEMORY_SCOPE_WORKGROUP); }
            gemm_run(c1, &lf[LF_W0], 1u, w);
            STAMP(0, 3);
            lds_arrive(&lf[LF_G1DONE], lane);
#pragma unroll
            for (int mt = 0; mt < 4; mt++) tie(bb[mt]);
            f32x4 own[4];
            {
                char* mine = smem + RING + w * 16384;
#define XCH(k) if (w == k) { \
                _Pragma("unroll") for (int nt = 0; nt < 4; nt++) _Pragma("unroll") for (int mt = 0; mt < 4; mt++) \
                    if (nt != k) *(f32x4*)(mine + (nt * 4 + mt) * 1024 + lane * 16) = c1.acc[mt][nt]; \
                _Pragma("unroll") for (int mt = 0; mt < 4; mt++) { own[mt] = c1.acc[mt][k]; asm volatile("" : "+v"(own[mt])); } }
                XCH(0) XCH(1) XCH(2) XCH(3)
#undef XCH
            }
            lds_arrive(&lf[LF_X1], lane);
            lds_wait(&lf[LF_X1], 4);
            STAMP(0, 9);
#pragma unroll
            for (int o = 1; o < 4; o++) {
                const int ow = (w + o) & 3;
#pragma unroll
                for (int mt = 0; mt < 4; mt++)
                    own[mt] += *(const f32x4*)(smem + RING + ow * 16384 + (w * 4 + mt) * 1024 + lane * 16);
            }
            lds_arrive(&lf[LF_X1R], lane);
            const int mq = lane >> 4, n = lane & 15;
#pragma unroll
            for (int mt = 0; mt < 4; mt++) {
                float v[4];
                {
                    u32 p0 = pk2bf(own[mt][0], own[mt][1]), p1 = pk2bf(own[mt][2], own[mt][3]);
                    u32 s0_ = pk2bf(lo_f(p0) + lo_f(bb[mt][0]), hi_f(p0) + hi_f(bb[mt][0]));
                    u32 s1_ = pk2bf(lo_f(p1) + lo_f(bb[mt][1]), hi_f(p1) + hi_f(bb[mt][1]));
                    v[0] = lo_f(s0_); v[1] = hi_f(s0_); v[2] = lo_f(s1_); v[3] = hi_f(s1_);
                }
#pragma unroll
                for (int i = 0; i < 4; i++) {
                    const float x = v[i];
                    const float p = __builtin_fmaf(x * x, -2.f * 0.7978845608028654f * 0.044715f * 1.4426950408889634f,
                                                   -2.f * 0.7978845608028654f * 1.4426950408889634f);
                    const float e = __builtin_amdgcn_exp2f(x * p);
                    v[i] = x * __builtin_amdgcn_rcpf(1.f + e);
                }
                u32x2 o = {pk2bf(v[0], v[1]), pk2bf(v[2], v[3])};
                const int ks = ft * 2 + (mt >> 1), kq = 2 * (mt & 1) + (mq >> 1);
                char* dst = (char*)UF + ((size_t)((dt * 56 + ks) * 4 + w) * 64 + kq * 16 + n) * 16 + (mq & 1) * 8;
                st_sc1_8(dst, o);
            }
            STAMP(0, 14);
            wait_vm0<0>();
            STAMP(0, 15);
            lds_arrive(&lf[LF_UPUB], lane);
            if (w == 0) {
                lds_wait(&lf[LF_UPUB], 4);
                if (lane == 0) st_sc1_4(gf + GF_U + dt * 32 + ft, tag);
                lds_arrive(&lf[LF_UPUB], lane);
            } else {
                lds_wait(&lf[LF_UPUB], 5);
            }
            lds_wait(&lf[LF_X1R], 4);
        }
        if (!has1) lds_arrive(&lf[LF_G1DONE], lane);
        STAMP(0, 4);
        GemmCtx<14, L * 2, false> c2;
        gemm_begin(c2, smem, (const char*)w2 + ((size_t)st * 64) * 2, (size_t)448 * L * 2,
                   (const char*)UF + ((size_t)(dt2 * 56 + w * 14)) * 4096, RING, w, ROT2, lane);
        if (w == 0) {
            poll_flags(gf + GF_U + dt2 * 32, 28, tag, lane);
            if (lane == 0) {
                __hip_atomic_store(&lf[LF_U], 1u, __ATOMIC_RELAXED, __HIP_MEMORY_SCOPE_WORKGROUP);
                if (b == 0) st_sc1_4(gf + GF_EPOCH, tag);
            }
        } else {
            lds_wait(&lf[LF_U], 1);
        }
        STAMP(0, 5);
        if (w == 1) { wait_vm0<16>(); if (lane == 0) __hip_atomic_store(&lf[LF_W0], 2u, __ATOMIC_RELAXED, __HIP_MEMORY_SCOPE_WORKGROUP); }
        gemm_run(c2, &lf[LF_W0], 2u, w);
        STAMP(0, 6);
        f32x4 own[4];
        {
            char* mine = smem + RING + w * 16384;
#define XCH(k) if (w == k) { \
            _Pragma("unroll") for (int nt = 0; nt < 4; nt++) _Pragma("unroll") for (int mt = 0; mt < 4; mt++) \
                if (nt != k) *(f32x4*)(mine + (nt * 4 + mt) * 1024 + lane * 16) = c2.acc[mt][nt]; \
            _Pragma("unroll") for (int mt = 0; mt < 4; mt++) { own[mt] = c2.acc[mt][k]; asm volatile("" : "+v"(own[mt])); } }
            XCH(0) XCH(1) XCH(2) XCH(3)
#undef XCH
        }
        lds_arrive(&lf[LF_X2], lane);
        lds_wait(&lf[LF_X2], 4);
#pragma unroll
        for (int o = 1; o < 4; o++) {
            const int ow = (w + o) & 3;
#pragma unroll
            for (int mt = 0; mt < 4; mt++)
                own[mt] += *(const f32x4*)(smem + RING + ow * 16384 + (w * 4 + mt) * 1024 + lane * 16);
        }
        lds_wait(&lf[LF_EPI], 4);
        {
            const int i = lane & 15, mq = lane >> 4, q = i >> 2, p = lane & 3;
            const float gg = *(const float*)(smem + STG + 8192 + (w * 16 + i) * 4);
            const float bias = *(const float*)(smem + STG + 8192 + 256 + (w * 16 + i) * 4);
            const float og = rbf(1.f - gg);
            u16* dst = out + (size_t)(st * 64 + w * 16 + i) * D + dt2 * 64 + mq * 4;
#pragma unroll
            for (int mt = 0; mt < 4; mt++) {
                const int r0 = 16 * mt + 4 * mq + q;
                const int f0 = ((r0 >> 1) & 1) | (((r0 >> 3) & 1) << 1);
                const u32x2 av = tr_read(smem, STG + r0 * 128 + ((w ^ f0) << 5) + p * 8);
                u32x2 o;
#pragma unroll
                for (int e = 0; e < 2; e++) {
                    u32 y = pk2bf(own[mt][2 * e], own[mt][2 * e + 1]);
                    y = pk2bf(lo_f(y) + bias, hi_f(y) + bias);
                    u32 t1 = pk2bf(gg * lo_f(av[e]), gg * hi_f(av[e]));
                    u32 t2 = pk2bf(og * lo_f(y), og * hi_f(y));
                    o[e] = pk2bf(lo_f(t1) + lo_f(t2), hi_f(t1) + hi_f(t2));
                }
                *(u32x2*)(dst + mt * 16) = o;
            }
        }
        STAMP(0, 7);
        STAMP_DUMP;
        return;
    }

    const int wv = w - 4, ch = wv >> 1, hf = wv & 1;
    const int d = xcd * 64 + li * 2 + ch;
    const int n = hf * 64 + lane;
    u32 r_are, r_aim, r_bre, r_bim, r_cre, r_cim, r_ldt, r_dsk, r_ep;
    {
        const unsigned po = (unsigned)(d * NS + n) * 2u;
        asm volatile("global_load_ushort %0, %9, %10\n\tglobal_load_ushort %1, %9, %11\n\t"
                     "global_load_ushort %2, %9, %12\n\tglobal_load_ushort %3, %9, %13\n\t"
                     "global_load_ushort %4, %9, %14\n\tglobal_load_ushort %5, %9, %15\n\t"
                     "global_load_ushort %6, %16, off\n\tglobal_load_ushort %7, %17, off\n\t"
                     "global_load_dword %8, %18, off sc1"
                     : "=&v"(r_are), "=&v"(r_aim), "=&v"(r_bre), "=&v"(r_bim), "=&v"(r_cre), "=&v"(r_cim), "=&v"(r_ldt), "=&v"(r_dsk), "=&v"(r_ep)
                     : "v"(po), "s"(Are), "s"(Aim), "s"(Bre), "s"(Bim), "s"(Cre), "s"(Cim), "v"(logdt + d), "v"(Dskip + d), "v"(gf + GF_EPOCH)
                     : "memory");
    }
    u32 dummy = 0;
    {
        const char* base = (const char*)w1 + ((size_t)(li * 64) * F + (xcd >> 1) * 448) * 2;
#pragma unroll
        for (int i = 0; i < T1; i++) {
            int idx = (i * 4 + wv) * 64 + lane;
            idx = idx < 448 ? idx : 447;
            const int row = idx / 7, sec = idx - row * 7;
            touch(dummy, (unsigned)(row * (F * 2) + sec * 128), base);
        }
    }
    asm volatile("s_waitcnt vmcnt(" STR(T1) ")" : "+v"(r_are), "+v"(r_aim), "+v"(r_bre), "+v"(r_bim), "+v"(r_cre), "+v"(r_cim), "+v"(r_ldt), "+v"(r_dsk), "+v"(r_ep) :: "memory");
    const u32 tag = r_ep + 1u;
    const unsigned sb_off = SSM0 + ch * SSM_SIZE;
    char* sb = smem + sb_off;
    unsigned* fl = lf + LF_SSM + ch * 4;
    const float dt = rbf(fast_exp(lo_f(r_ldt)));
    const float zr = dt * lo_f(r_are), zi = dt * lo_f(r_aim);
    const float dsk = lo_f(r_dsk);
    f32x2 W;
    {
        const f32x2 B = {dt * lo_f(r_bre), dt * lo_f(r_bim)};
        const f32x2 C = {lo_f(r_cre), lo_f(r_cim)};
        W = cmul(C, B);
    }
    u32x4 kix[2][2];
#pragma unroll
    for (int i = 0; i < 2; i++) {
        const u32x4* pt = (const u32x4*)(KIDX.v + (hf * 128 + i * 64 + lane) * 16);
        kix[i][0] = pt[0]; kix[i][1] = pt[1];
    }
    if (T1 < 2 && TMODE == 1) {
        lds_wait(&lf[LF_UARR], 4);
        const char* base = (const char*)w1 + ((size_t)(li * 64) * F + (xcd >> 1) * 448) * 2;
#pragma unroll
        for (int i = T1; i < 2; i++) {
            int idx = (i * 4 + wv) * 64 + lane;
            idx = idx < 448 ? idx : 447;
            const int row = idx / 7, sec = idx - row * 7;
            touch(dummy, (unsigned)(row * (F * 2) + sec * 128), base);
        }
    }
    STAMP(0, 1);
    char* tbl = sb + SSM_COPY + hf * TBL_SIZE;
    char* tw = tbl + lane * 8;
    const char* trd = tbl + (lane & 15) * TBL_PITCH + (lane >> 4) * 16;
    u32x4 Bh[8], Bl[8];
    f32x4 kacc[3];
    {
        f32x2 E8 = cexp_scaled(zr, zi, 8.f);
        E8.y = -E8.y;
        f32x2 pw[16];
        pw[0] = f32x2{1.f, 0.f};
        pw[1] = E8;
#pragma unroll
        for (int q = 2; q < 16; q++) pw[q] = (q & 1) ? cmul(pw[q - 1], E8) : cmul(pw[q / 2], pw[q / 2]);
#pragma unroll
        for (int q = 0; q < 16; q++) *(u32x2*)(tw + q * TBL_PITCH) = split_bf(pw[q]);
        wait_lgkm0();
#pragma unroll
        for (int T = 0; T < 8; T++) {
            u32x4 v = *(const u32x4*)(trd + T * 64);
            Bh[T] = u32x4{v[0], v[0], v[2], v[2]};
            Bl[T] = u32x4{v[1], v[1], v[3], v[3]};
        }
        wait_lgkm0();
    }
    if (T1 < 2 && TMODE != 1) {
        if (TMODE == 0) lds_wait(&lf[LF_LNST], 4);
        else lds_wait(&lf[LF_H], 1);
        const char* base = (const char*)w1 + ((size_t)(li * 64) * F + (xcd >> 1) * 448) * 2;
#pragma unroll
        for (int i = T1; i < 2; i++) {
            int idx = (i * 4 + wv) * 64 + lane;
            idx = idx < 448 ? idx : 447;
            const int row = idx / 7, sec = idx - row * 7;
            touch(dummy, (unsigned)(row * (F * 2) + sec * 128), base);
        }
    }
    {
        const f32x2 E1 = cexp_scaled(zr, zi, 1.f);
        const f32x2 E128 = cexp_scaled(zr, zi, 128.f);
        f32x2 WY[8];
        {
            f32x2 Y[8];
            Y[0] = f32x2{1.f, 0.f}; Y[1] = E1;
#pragma unroll
            for (int c = 2; c < 8; c++) Y[c] = (c & 1) ? cmul(Y[c - 1], E1) : cmul(Y[c / 2], Y[c / 2]);
            WY[0] = W;
#pragma unroll
            for (int c = 1; c < 8; c++) WY[c] = cmul(W, Y[c]);
        }
        f32x2 Xp[17];
        Xp[0] = f32x2{1.f, 0.f}; Xp[1] = E128;
#pragma unroll
        for (int a = 2; a < 17; a++) Xp[a] = (a & 1) ? cmul(Xp[a - 1], E128) : cmul(Xp[a / 2], Xp[a / 2]);
#pragma unroll
        for (int tile = 0; tile < 3; tile++) {
#pragma unroll
            for (int r = 0; r < 16; r++) {
                f32x2 v;
                bool valid = true;
                if (tile == 0) { v = (r < 8) ? WY[r] : cmul(WY[r - 8], Xp[1]); }
                else if (tile == 1) {
                    if (r < 8) v = cmul(WY[2 * (r & 3)], Xp[2 + (r >> 2)]);
                    else v = cmul(WY[4 * (r & 1)], Xp[4 + ((r - 8) >> 1)]);
                } else {
                    if (r < 8) v = cmul(W, Xp[8 + r]);
                    else if (r == 8) v = cmul(W, Xp[16]);
                    else valid = false;
                }
                if (valid) *(u32x2*)(tw + r * TBL_PITCH) = split_bf(v);
            }
            wait_lgkm0();
            f32x4 a4 = {0.f, 0.f, 0.f, 0.f};
#pragma unroll
            for (int T = 0; T < 8; T++) {
                u32x4 av = *(const u32x4*)(trd + T * 64);
                a4 = mfma16(av, Bh[T], a4);
                a4 = mfma16(av, Bl[T], a4);
            }
            kacc[tile] = a4;
            wait_lgkm0();
        }
    }
    STAMP(0, 2);
    {
        float* P = (float*)(sb + SSM_P + hf * 3456);
        const int mq = lane >> 4, col = lane & 15;
#pragma unroll
        for (int tile = 0; tile < 3; tile++)
#pragma unroll
            for (int r = 0; r < 4; r++)
                if (!(tile == 2 && r == 1 && 0)) P[(tile * 16 + mq * 4 + r) * 18 + col] = kacc[tile][r];
        wait_lgkm0();
        if (lane == 0) P[KC_ZERO * 18] = 0.f;
    }
    if (hf == 0 && lane < 32) *(u32x4*)(sb + SSM_U + lane * 16) = u32x4{0, 0, 0, 0};
    lds_arrive(&fl[0], lane);
    touch_done(dummy);
    tie(cdummy);
    if (wv == 0) {
        poll_flags256(gf + GF_H2, tag, lane);
        if (lane == 0) __hip_atomic_store(&lf[LF_SSM + 9], 1u, __ATOMIC_RELAXED, __HIP_MEMORY_SCOPE_WORKGROUP);
    } else {
        lds_wait(&lf[LF_SSM + 9], 1);
    }
    u32x4 ug0, ug1;
    const int up0 = hf * 128 + lane, up1 = up0 + 64;
    {
        const int dt_ = d >> 6, nt = (d >> 4) & 3, m = d & 15;
        const char* src = (const char*)uF + ((size_t)(dt_ * 64 * 4 + nt) * 64 + m) * 16;
        const char* pa = src + (up0 >> 2) * 4096 + (up0 & 3) * 256;
        const char* pb = src + (up1 >> 2) * 4096 + (up1 & 3) * 256;
        asm volatile("global_load_dwordx4 %0, %2, off sc1\n\tglobal_load_dwordx4 %1, %3, off sc1"
                     : "=&v"(ug0), "=&v"(ug1) : "v"(pa), "v"(pb) : "memory");
    }
    lds_wait(&fl[0], 2);
    STAMP(0, 3);
    {
        const char* P0 = sb + SSM_P;
        const char* P1 = sb + SSM_P + 3456;
#pragma unroll
        for (int i = 0; i < 2; i++) {
            const int ub = hf * 128 + i * 64 + lane;
            float v[11];
#pragma unroll
            for (int j = 0; j < 11; j++) {
                const u32 word = kix[i][j >> 3][(j >> 1) & 3];
                const unsigned off = (j & 1) ? (word >> 16) : (word & 0xffffu);
                v[j] = *(const float*)(P0 + off) + *(const float*)(P1 + off);
            }
            u32 e[5], o[5];
#pragma unroll
            for (int k = 0; k < 5; k++) { e[k] = pk2bf(v[2 * k], v[2 * k + 1]); o[k] = pk2bf(v[2 * k + 1], v[2 * k + 2]); }
            char* dst = sb + SSM_COPY + ub * 16;
            *(u32x4*)(dst) = u32x4{e[0], e[1], e[2], e[3]};
            *(u32x4*)(dst + CP_STRIDE) = u32x4{o[0], o[1], o[2], o[3]};
            *(u32x4*)(dst + 2 * CP_STRIDE) = u32x4{e[1], e[2], e[3], e[4]};
            *(u32x4*)(dst + 3 * CP_STRIDE) = u32x4{o[1], o[2], o[3], o[4]};
        }
        if (hf == 0 && lane < 8) {
            *(u32x4*)(sb + SSM_COPY + (lane >> 1) * CP_STRIDE + (256 + (lane & 1)) * 16) = u32x4{0, 0, 0, 0};
        }
    }
    STAMP(0, 4);
    asm volatile("s_waitcnt vmcnt(0)" : "+v"(ug0), "+v"(ug1) :: "memory");
    *(u32x4*)(sb + SSM_U + 512 + up0 * 16) = ug0;
    *(u32x4*)(sb + SSM_U + 512 + up1 * 16) = ug1;
    {
        const char* base = (const char*)w2 + ((size_t)(li * 56) * L + (xcd >> 1) * 512) * 2;
#pragma unroll
        for (int i = 0; i < 2; i++) {
            const int idx = (wv * 2 + i) * 64 + lane;
            if (idx < 448) touch(dummy, (unsigned)((idx >> 3) * (L * 2) + (idx & 7) * 128), base);
        }
    }
    lds_arrive(&fl[1], lane);
    lds_wait(&fl[1], 2);
    STAMP(0, 5);
    f32x4 cacc[8];
#pragma unroll
    for (int i = 0; i < 8; i++) cacc[i] = f32x4{0.f, 0.f, 0.f, 0.f};
    {
        const int m = lane & 15, q = lane >> 4;
        const int gq = m >> 2, e = m & 3, c = 3 - e;
        const char* abase = sb + SSM_COPY + c * CP_STRIDE + 4104 - 8 * gq - 16 * q - 64 * 63 + 64 * (7 - hf * 4 - 3);
        const char* bbase = sb + SSM_U + 528 + 32 * m - 16 * q - 64 * 7 + 64 * (7 - hf * 4 - 3);
#pragma unroll
        for (int rr = 0; rr < 4; rr++) {
            const char* ab = abase + 64 * (3 - rr);
            const char* bb = bbase + 64 * (3 - rr);
            u32x4 A[8], B[8];
#pragma unroll
            for (int pp = 0; pp < 8; pp++) {
                const u32x2 a0 = *(const u32x2*)(ab + 64 * (56 - 8 * pp));
                const u32x2 a1 = *(const u32x2*)(ab + 64 * (56 - 8 * pp) + 8);
                A[pp] = u32x4{a0[0], a0[1], a1[0], a1[1]};
                B[pp] = *(const u32x4*)(bb + 512 * pp);
            }
#pragma unroll
            for (int i = 0; i < 8; i++)
#pragma unroll
                for (int pp = 0; pp <= i; pp++) cacc[i] = mfma16(A[pp], B[i - pp], cacc[i]);
        }
    }
    STAMP(0, 6);
    f32x4 keep[4];
    {
        char* xo = sb + SSM_X + hf * 4096;
        if (hf) {
#pragma unroll
            for (int i = 0; i < 4; i++) { *(f32x4*)(xo + i * 1024 + lane * 16) = cacc[i]; keep[i] = cacc[4 + i]; asm volatile("" : "+v"(keep[i])); }
        } else {
#pragma unroll
            for (int i = 0; i < 4; i++) { *(f32x4*)(xo + i * 1024 + lane * 16) = cacc[4 + i]; keep[i] = cacc[i]; asm volatile("" : "+v"(keep[i])); }
        }
    }
    lds_arrive(&fl[2], lane);
    lds_wait(&fl[2], 2);
    {
        const char* xi = sb + SSM_X + (hf ^ 1) * 4096;
        const int nn = lane & 15, mq = lane >> 4;
#pragma unroll
        for (int i = 0; i < 4; i++) {
            f32x4 v = keep[i];
            v += *(const f32x4*)(xi + i * 1024 + lane * 16);
            const int t = 256 * (hf * 4 + i) + 16 * nn + 4 * mq;
            const u32x2 uu = *(const u32x2*)(sb + SSM_U + 512 + t * 2);
            u32 y0 = pk2bf(v[0], v[1]), y1 = pk2bf(v[2], v[3]);
            u32 s0 = pk2bf(lo_f(uu[0]) * dsk, hi_f(uu[0]) * dsk), s1 = pk2bf(lo_f(uu[1]) * dsk, hi_f(uu[1]) * dsk);
            u32x2 o = {pk2bf(lo_f(y0) + lo_f(s0), hi_f(y0) + hi_f(s0)), pk2bf(lo_f(y1) + lo_f(s1), hi_f(y1) + hi_f(s1))};
            st_sc1_8(aT + (size_t)d * L + t, o);
        }
    }
    wait_vm0<0>();
    lds_arrive(&lf[LF_APUB], lane);
    if (wv == 0) {
        lds_wait(&lf[LF_APUB], 4);
        if (lane == 0) st_sc1_4(gf + GF_A + xcd * 32 + li, tag);
    }
    STAMP(0, 7);
    if (wv == 0) {
        poll_flags(gf + GF_A + dt2 * 32, 32, tag, lane);
        if (lane == 0) __hip_atomic_store(&lf[LF_SSM + 8], 1u, __ATOMIC_RELAXED, __HIP_MEMORY_SCOPE_WORKGROUP);
    } else {
        lds_wait(&lf[LF_SSM + 8], 1);
    }
    {
        const int r = 16 * wv + (lane >> 2);
        const int j0 = (lane & 3) * 2, j1 = j0 + 1;
        const char* src = (const char*)aT + ((size_t)(dt2 * 64 + r) * L + st * 64) * 2;
        u32x4 a, c;
        ld2_sc1_16(a, c, src + j0 * 16, src + j1 * 16);
        const int f = ((r >> 1) & 1) | (((r >> 3) & 1) << 1);
        *(u32x4*)(smem + STG + r * 128 + ((j0 ^ (f << 1)) << 4)) = a;
        *(u32x4*)(smem + STG + r * 128 + ((j1 ^ (f << 1)) << 4)) = c;
        if (lane < 16) {
            const int s = st * 64 + wv * 16 + lane;
            const u32 gv = ld_sc1_4(g + s);
            *(u32*)(smem + STG + 8192 + (wv * 16 + lane) * 4) = gv;
            *(float*)(smem + STG + 8192 + 256 + (wv * 16 + lane) * 4) = bf2f(b2[s]);
        }
    }
    lds_arrive(&lf[LF_EPI], lane);
    touch_done(dummy);
    STAMP(0, 8);
    STAMP_DUMP;
}

extern "C" void kernel_launch(void* const* d_in, const int* in_sizes, int n_in,
                              void* d_out, int out_size, void* d_ws, size_t ws_size,
                              hipStream_t stream) {
    const u16* u = (const u16*)d_in[0];
    const u16* Are = (const u16*)d_in[1];
    const u16* Aim = (const u16*)d_in[2];
    const u16* Bre = (const u16*)d_in[3];
    const u16* Bim = (const u16*)d_in[4];
    const u16* Cre = (const u16*)d_in[5];
    const u16* Cim = (const u16*)d_in[6];
    const u16* logdt = (const u16*)d_in[7];
    const u16* Dskip = (const u16*)d_in[8];
    const u16* w1 = (const u16*)d_in[10];
    const u16* b1 = (const u16*)d_in[11];
    const u16* w2 = (const u16*)d_in[12];
    const u16* b2 = (const u16*)d_in[13];
    const u16* gw = (const u16*)d_in[14];
    k_fused<<<256, 512, 0, stream>>>(u, gw, Are, Aim, Bre, Bim, Cre, Cim, logdt, Dskip, w1, b1, w2, b2, (char*)d_ws, (u16*)d_out);
}
```

```cpp
#include <hip/hip_runtime.h>
#include <stdint.h>

typedef unsigned short u16;
typedef unsigned int u32;
using bf16x8 = __attribute__((ext_vector_type(8))) __bf16;
using bf16x2 = __attribute__((ext_vector_type(2))) __bf16;
using s16x4 = __attribute__((ext_vector_type(4))) short;
using f32x4 = __attribute__((ext_vector_type(4))) float;
using f32x2 = __attribute__((ext_vector_type(2))) float;
using u32x4 = __attribute__((ext_vector_type(4))) unsigned;
using u32x2 = __attribute__((ext_vector_type(2))) unsigned;
typedef __attribute__((address_space(3))) s16x4* lds_s16x4_ptr;

#if defined(__HIP_DEVICE_COMPILE__)
#define NOPK __attribute__((target("no-packed-fp32-ops")))
#else
#define NOPK
#endif
#define DEV NOPK __device__ __forceinline__
#define TIDX ((int)__builtin_amdgcn_workitem_id_x())
#define BIDX ((int)__builtin_amdgcn_workgroup_id_x())

#define STAMP_DECL do {} while (0)
#define STAMP(kern, idx) do {} while (0)
#define STAMP_DUMP do {} while (0)

constexpr int L = 2048, D = 512, NS = 128, F = 1792;
#define T1 1
#define TMODE 0
#define CONVDEFER 0
#define TLBWARM 1
#define CODE_BYTES 32000
#define RK 0
#define ROT1 ((16 * 4 - RK * (ftl + dtl)) & 15)
#define ROT2 ((14 * 4 - RK * (stl + dtl2)) % 14)
#define STR_(x) #x
#define STR(x) STR_(x)

DEV float bf2f(u16 v) { return __builtin_bit_cast(float, ((unsigned)v) << 16); }
DEV u32 pk2bf(float a, float b) {
    f32x2 v = {a, b};
    return __builtin_bit_cast(u32, __builtin_convertvector(v, bf16x2));
}
DEV float lo_f(u32 p) { return __builtin_bit_cast(float, p << 16); }
DEV float hi_f(u32 p) { return __builtin_bit_cast(float, p & 0xffff0000u); }
DEV float rbf(float f) { return lo_f(pk2bf(f, 0.f)); }

DEV u32x2 tr_read(const char* lds_generic_base, unsigned off) {
    s16x4 v = __builtin_amdgcn_ds_read_tr16_b64_v4i16((lds_s16x4_ptr)(lds_generic_base + off));
    return __builtin_bit_cast(u32x2, v);
}
DEV f32x4 mfma16(u32x4 a, u32x4 b, f32x4 c) {
    return __builtin_amdgcn_mfma_f32_16x16x32_bf16(__builtin_bit_cast(bf16x8, a), __builtin_bit_cast(bf16x8, b), c, 0, 0, 0);
}

DEV void gload_early8(u32x2& dst, const void* p) { asm volatile("global_load_dwordx2 %0, %1, off" : "=v"(dst) : "v"(p) : "memory"); }
template <typename T> DEV void tie(T& x) { asm volatile("" : "+v"(x) :: "memory"); }
DEV void touch(u32& dummy, unsigned voff, const void* sbase) {
    asm volatile("global_load_dword %0, %1, %2" : "+v"(dummy) : "v"(voff), "s"(sbase) : "memory");
}
DEV void touch_done(u32& dummy) { asm volatile("s_waitcnt vmcnt(0)" : "+v"(dummy) :: "memory"); }
template <int N>
DEV void wait_vm(u32x4& a, u32x4& b, u32x4& c, u32x4& d) {
    asm volatile("s_waitcnt vmcnt(%4)" : "+v"(a), "+v"(b), "+v"(c), "+v"(d) : "i"(N) : "memory");
}
template <int N>
DEV void wait_vm0() { asm volatile("s_waitcnt vmcnt(%0)" ::"i"(N) : "memory"); }
DEV void wait_lgkm0() { asm volatile("s_waitcnt lgkmcnt(0)" ::: "memory"); }
DEV u32 ld_sc1_4(const void* p) {
    u32 v; asm volatile("global_load_dword %0, %1, off sc1\n\ts_waitcnt vmcnt(0)" : "=&v"(v) : "v"(p) : "memory"); return v;
}
DEV u32x4 ld_sc1_16(const void* p) {
    u32x4 v; asm volatile("global_load_dwordx4 %0, %1, off sc1\n\ts_waitcnt vmcnt(0)" : "=&v"(v) : "v"(p) : "memory"); return v;
}
DEV void ld2_sc1_16(u32x4& a, u32x4& b, const void* pa, const void* pb) {
    asm volatile("global_load_dwordx4 %0, %2, off sc1\n\tglobal_load_dwordx4 %1, %3, off sc1\n\ts_waitcnt vmcnt(0)"
                 : "=&v"(a), "=&v"(b) : "v"(pa), "v"(pb) : "memory");
}
DEV void st_sc1_16(void* p, u32x4 v) { asm volatile("global_store_dwordx4 %0, %1, off sc1\n\ts_nop 1" :: "v"(p), "v"(v) : "memory"); }
DEV void st_sc1_8(void* p, u32x2 v) { asm volatile("global_store_dwordx2 %0, %1, off sc1\n\ts_nop 1" :: "v"(p), "v"(v) : "memory"); }
DEV void st_sc1_4(void* p, u32 v) { asm volatile("global_store_dword %0, %1, off sc1" :: "v"(p), "v"(v) : "memory"); }

template <int CTRL>
DEV float dpp_add(float v) {
    int t = __builtin_amdgcn_update_dpp(0, __builtin_bit_cast(int, v), CTRL, 0xf, 0xf, true);
    return v + __builtin_bit_cast(float, t);
}
DEV float rdlane(float v, int l) { return __builtin_bit_cast(float, __builtin_amdgcn_readlane(__builtin_bit_cast(int, v), l)); }
DEV float wave_sum(float v) {
    v = dpp_add<0xB1>(v);
    v = dpp_add<0x4E>(v);
    v = dpp_add<0x141>(v);
    v = dpp_add<0x140>(v);
    return (rdlane(v, 0) + rdlane(v, 16)) + (rdlane(v, 32) + rdlane(v, 48));
}
DEV float fast_exp(float x) { return __builtin_amdgcn_exp2f(x * 1.4426950408889634f); }
DEV void lds_arrive(unsigned* cnt, int lane) {
    wait_lgkm0();
    if (lane == 0) __hip_atomic_fetch_add(cnt, 1u, __ATOMIC_RELAXED, __HIP_MEMORY_SCOPE_WORKGROUP);
}
DEV void lds_wait(unsigned* cnt, unsigned target) {
    while ((unsigned)__builtin_amdgcn_readfirstlane((int)__hip_atomic_load(cnt, __ATOMIC_RELAXED, __HIP_MEMORY_SCOPE_WORKGROUP)) < target)
        __builtin_amdgcn_s_sleep(1);
    asm volatile("" ::: "memory");
}
constexpr int SPIN_CAP = 20000;
DEV void poll_flags256(const u32* fl, u32 tag, int lane) {
#pragma unroll 1
    for (int spin = 0; spin < SPIN_CAP; spin++) {
        u32x4 v = ld_sc1_16(fl + lane * 4);
        bool ok = v[0] == tag && v[1] == tag && v[2] == tag && v[3] == tag;
        if (__builtin_amdgcn_ballot_w64(ok) == ~0ull) break;
        __builtin_amdgcn_s_sleep(2);
    }
}
DEV void poll_flags(const u32* fl, int n, u32 tag, int lane) {
    const u32* p = fl + (lane < n ? lane : 0);
#pragma unroll 1
    for (int spin = 0; spin < SPIN_CAP; spin++) {
        u32 v = ld_sc1_4(p);
        if (__builtin_amdgcn_ballot_w64(v == tag) == ~0ull) break;
        __builtin_amdgcn_s_sleep(2);
    }
}
DEV float div_nr(float a, float b, float r) {
    float q = a * r;
    float e = __builtin_fmaf(-q, b, a);
    return __builtin_fmaf(e, r, q);
}

struct KIdxTable { u16 v[256 * 16]; };
constexpr int round_bf16_int(int x) {
    if (x < 256) return x;
    int sh = 0;
    while ((x >> sh) >= 256) sh++;
    int q = x >> sh, r = x & ((1 << sh) - 1), half = 1 << (sh - 1);
    if (r > half || (r == half && (q & 1))) q++;
    return q << sh;
}
constexpr int KC_ZERO = 41;
constexpr int compact_pos(int tap) {
    int row = KC_ZERO, col = 0;
    if (tap >= 0) {
        int lr = round_bf16_int(tap);
        if (lr < 256) { row = (lr & 7) + 8 * (lr >> 7); col = (lr >> 3) & 15; }
        else if (lr < 512) { int x = lr - 256; row = 16 + ((x & 7) >> 1) + 4 * (x >> 7); col = (x >> 3) & 15; }
        else if (lr < 1024) { int x = lr - 512; row = 24 + ((x & 7) >> 2) + 2 * (x >> 7); col = (x >> 3) & 15; }
        else if (lr < 2048) { int x = lr - 1024; row = 32 + (x >> 7); col = (x >> 3) & 15; }
        else { row = 40; col = 0; }
    }
    return row * 18 + col;
}
constexpr KIdxTable make_kidx() {
    KIdxTable t{};
    for (int ub = 0; ub < 256; ub++)
        for (int i = 0; i < 16; i++) t.v[ub * 16 + i] = (u16)(4 * compact_pos(2047 - (8 * ub + i)));
    return t;
}
__device__ const KIdxTable KIDX = make_kidx();

DEV f32x2 cmul(f32x2 a, f32x2 b) {
    f32x2 r;
    r.x = __builtin_fmaf(-a.y, b.y, a.x * b.x);
    r.y = __builtin_fmaf(a.y, b.x, a.x * b.y);
    return r;
}
DEV f32x2 cexp_scaled(float zr, float zi, float l) {
    const float xr = zr * l, xi = zi * l;
    const float e = __builtin_amdgcn_exp2f(xr * 1.4426950408889634f);
    const float c_hi = 0.15915494309189535f;
    const float c_lo = 6.4206382e-09f;
    float rh = xi * c_hi;
    float rl = __builtin_fmaf(xi, c_lo, __builtin_fmaf(xi, c_hi, -rh));
    float fr = (rh - __builtin_rintf(rh)) + rl;
    return f32x2{e * __builtin_amdgcn_cosf(fr), e * __builtin_amdgcn_sinf(fr)};
}
DEV u32x2 split_bf(f32x2 v) {
    u32 h = pk2bf(v.x, v.y);
    u32 l = pk2bf(v.x - lo_f(h), v.y - hi_f(h));
    return u32x2{h, l};
}

DEV void dma_slot(unsigned lds_, const unsigned (&voff)[4], const void* sbase) {
    const unsigned lds = (unsigned)__builtin_amdgcn_readfirstlane((int)lds_);
    asm volatile("s_mov_b32 m0, %0\n\ts_nop 0\n\t"
                 "global_load_lds_dwordx4 %1, %5\n\t"
                 "global_load_lds_dwordx4 %2, %5 offset:1024\n\t"
                 "global_load_lds_dwordx4 %3, %5 offset:2048\n\t"
                 "global_load_lds_dwordx4 %4, %5 offset:3072"
                 :: "s"(lds), "v"(voff[0]), "v"(voff[1]), "v"(voff[2]), "v"(voff[3]), "s"(sbase) : "memory", "m0");
}
DEV void xload(u32x4 (&x)[4], unsigned voff, const void* sbase) {
    asm volatile("global_load_dwordx4 %0, %4, %5 sc1\n\t"
                 "global_load_dwordx4 %1, %4, %5 offset:1024 sc1\n\t"
                 "global_load_dwordx4 %2, %4, %5 offset:2048 sc1\n\t"
                 "global_load_dwordx4 %3, %4, %5 offset:3072 sc1"
                 : "+v"(x[0]), "+v"(x[1]), "+v"(x[2]), "+v"(x[3]) : "v"(voff), "s"(sbase) : "memory");
}
template <int S, int WPITCH, bool W_IS_A>
struct GemmCtx {
    const char* smem;
    const char* wbase;
    const char* xbase;
    unsigned ring;
    int nxt, nxtx;
    unsigned wvoff[4];
    unsigned xvoff;
    unsigned raddr[4];
    u32x4 X[4][4];
    f32x4 acc[4][4];
};
template <int J, int S, int WPITCH, bool W_IS_A>
DEV void g_issue_w(GemmCtx<S, WPITCH, W_IS_A>& c) {
    dma_slot(c.ring + J * 4096, c.wvoff, c.wbase + (size_t)((unsigned)c.nxt * (unsigned)(32 * WPITCH)));
    c.nxt = (c.nxt + 1 == S) ? 0 : c.nxt + 1;
}
template <int J, int S, int WPITCH, bool W_IS_A>
DEV void g_issue_x(GemmCtx<S, WPITCH, W_IS_A>& c) {
    xload(c.X[J], c.xvoff, c.xbase + (size_t)((unsigned)c.nxtx * 4096u));
    c.nxtx = (c.nxtx + 1 == S) ? 0 : c.nxtx + 1;
}
template <int J, int N, bool IX, bool IW, int S, int WPITCH, bool W_IS_A>
DEV void g_step(GemmCtx<S, WPITCH, W_IS_A>& c) {
    u32x4(&x)[4] = c.X[J];
    wait_vm<N>(x[0], x[1], x[2], x[3]);
    u32x4 Wf[4];
#pragma unroll
    for (int i = 0; i < 4; i++) {
        u32x2 a = tr_read(c.smem, c.raddr[i] + J * 4096);
        u32x2 b = tr_read(c.smem, c.raddr[i] + J * 4096 + 512);
        Wf[i] = u32x4{a[0], a[1], b[0], b[1]};
    }
    if constexpr (IX) g_issue_x<(J + 3) & 3>(c);
    if constexpr (IW) {
        asm volatile("s_waitcnt lgkmcnt(0)" : "+v"(Wf[0]), "+v"(Wf[1]), "+v"(Wf[2]), "+v"(Wf[3]) :: "memory");
        g_issue_w<J>(c);
    }
#pragma unroll
    for (int i = 0; i < 4; i++)
#pragma unroll
        for (int j = 0; j < 4; j++) {
            if constexpr (W_IS_A) c.acc[i][j] = mfma16(Wf[i], x[j], c.acc[i][j]);
            else c.acc[i][j] = mfma16(x[i], Wf[j], c.acc[i][j]);
        }
    __builtin_amdgcn_sched_barrier(0);
}
template <int S, int WPITCH, bool W_IS_A>
DEV void gemm_begin(GemmCtx<S, WPITCH, W_IS_A>& c, const char* smem, const char* wbase0, size_t wstride, const char* xbase, unsigned ring0, int w, int rot, int lane) {
    c.smem = smem; c.wbase = wbase0 + w * wstride; c.xbase = xbase; c.ring = ring0 + w * 16384; c.nxt = rot; c.nxtx = rot;
#pragma unroll
    for (int i = 0; i < 4; i++) {
        const int sw = (((lane >> 4) & 1) << 1) | ((i & 1) << 2);
        c.wvoff[i] = (unsigned)((8 * i + (lane >> 3)) * WPITCH + (((lane & 7) ^ sw) << 4) - 1024 * i);
    }
    c.xvoff = lane * 16;
    const int g = lane >> 4, q = (lane & 15) >> 2, p = lane & 3;
    const int fl = ((q >> 1) & 1) | ((g & 1) << 1);
#pragma unroll
    for (int i = 0; i < 4; i++) c.raddr[i] = c.ring + (8 * g + q) * 128 + ((i ^ fl) << 5) + p * 8;
    if (w != 0) { g_issue_w<0>(c); g_issue_w<1>(c); g_issue_w<2>(c); g_issue_w<3>(c); }
}
template <int S, int WPITCH, bool W_IS_A>
DEV void gemm_run(GemmCtx<S, WPITCH, W_IS_A>& c, unsigned* w0flag, unsigned w0val, int w
) {
#pragma unroll
    for (int i = 0; i < 4; i++)
#pragma unroll
        for (int j = 0; j < 4; j++) { c.acc[i][j] = f32x4{0.f, 0.f, 0.f, 0.f}; c.X[i][j] = u32x4{0, 0, 0, 0}; }
    if (w == 0) { g_issue_w<0>(c); g_issue_w<1>(c); g_issue_w<2>(c); g_issue_w<3>(c); }
    g_issue_x<0>(c); g_issue_x<1>(c); g_issue_x<2>(c);
    g_step<0, 8, true, true>(c);
    g_step<1, 12, true, true>(c);
    g_step<2, 16, true, true>(c);
    g_step<3, 20, true, true>(c);
    constexpr int STEADY = S - 8;
#pragma unroll 1
    for (int it = 0; it < STEADY / 4; it++) {
        g_step<0, 20, true, true>(c);
        g_step<1, 20, true, true>(c);
        g_step<2, 20, true, true>(c);
        g_step<3, 20, true, true>(c);
    }
    constexpr int R = STEADY % 4;
    if constexpr (R >= 1) g_step<0, 20, true, true>(c);
    if constexpr (R >= 2) g_step<1, 20, true, true>(c);
    if constexpr (R >= 3) g_step<2, 20, true, true>(c);
    g_step<(R + 0) & 3, 20, true, false>(c);
    g_step<(R + 1) & 3, 16, false, false>(c);
    g_step<(R + 2) & 3, 8, false, false>(c);
    g_step<(R + 3) & 3, 0, false, false>(c);
}

constexpr int RING = 0;
constexpr int STG = 65536;
constexpr int SSM0 = STG + 16384;
constexpr int TBL_PITCH = 528;
constexpr int TBL_SIZE = 16 * TBL_PITCH;
constexpr int CP_STRIDE = 4160;
constexpr int SSM_COPY = 0;
constexpr int SSM_U = 16896;
constexpr int SSM_P = SSM_U + 4608;
constexpr int SSM_X = SSM_P + 2 * 3456;
constexpr int SSM_SIZE = SSM_X + 8192;
constexpr int LFLAGS = SSM0 + 2 * SSM_SIZE;
constexpr int LDS_BYTES = LFLAGS + 128;
enum { LF_LN = 0, LF_LNPUB = 1, LF_H = 2, LF_X1 = 3, LF_X1R = 4, LF_UPUB = 5, LF_U = 6, LF_X2 = 7, LF_EPI = 8, LF_APUB = 9, LF_LNST = 10, LF_LNPUB2 = 11, LF_UARR = 24, LF_W0 = 25, LF_G1DONE = 26, LF_SSM = 12   };
constexpr int GF_EPOCH = 0, GF_H = 64, GF_H2 = 64 + 256, GF_U = 64 + 512, GF_A = 64 + 512 + 8 * 32;

NOPK __global__ __launch_bounds__(512) void k_fused(const u16* __restrict__ u, const u16* __restrict__ gw,
                                                    const u16* __restrict__ Are, const u16* __restrict__ Aim,
                                                    const u16* __restrict__ Bre, const u16* __restrict__ Bim,
                                                    const u16* __restrict__ Cre, const u16* __restrict__ Cim,
                                                    const u16* __restrict__ logdt, const u16* __restrict__ Dskip,
                                                    const u16* __restrict__ w1, const u16* __restrict__ b1,
                                                    const u16* __restrict__ w2, const u16* __restrict__ b2,
                                                    char* __restrict__ ws, u16* __restrict__ out) {
    __shared__ __attribute__((aligned(16))) char smem[LDS_BYTES];
    const int tid = TIDX, lane = tid & 63;
    const int w = __builtin_amdgcn_readfirstlane(tid >> 6);
    const int b = BIDX;
    const int xcd = b & 7, li = b >> 3;
    unsigned* lf = (unsigned*)(smem + LFLAGS);
    u16* hF = (u16*)ws;
    u16* uF = (u16*)(ws + (2 << 20));
    u16* aT = (u16*)(ws + (4 << 20));
    u16* UF = (u16*)(ws + (6 << 20));
    float* g = (float*)(ws + (8 << 20));
    u32* gf = (u32*)(ws + (9 << 20));
    const int stl = li & 7, dtl2 = li >> 3;
    const int st = (xcd >> 1) * 8 + stl, dt2 = (xcd & 1) * 4 + dtl2;
    STAMP_DECL;
    STAMP(0, 0);
    u32x4 xv[2], gv;
    if (w < 4) {
#pragma unroll
        for (int i = 0; i < 2; i++) xv[i] = *(const u32x4*)(u + (size_t)(b * 8 + 2 * w + i) * D + lane * 8);
        gv = *(const u32x4*)(gw + lane * 8);
    }
    u32 cdummy = 0;
    if (w == 7) {
        const unsigned long long pc = __builtin_amdgcn_s_getpc() & ~127ull;
        int idx = li * 8 + lane;
        if (lane < 8 && idx * 128 < CODE_BYTES) touch(cdummy, (unsigned)(idx * 128), (const void*)pc);
    }
    if (w == 6 && li < 8) {
        const unsigned off = (unsigned)lane * 65536u;
        const char* base; unsigned lim;
        if (li < 3) { base = ws + (size_t)li * (4u << 20); lim = li < 2 ? (4u << 20) : (2u << 20); }
        else if (li < 5) { base = (const char*)w1 + (size_t)(li - 3) * (4u << 20); lim = li == 3 ? (4u << 20) : (unsigned)(L * F * 2 - (4 << 20)); }
        else if (li < 7) { base = (const char*)w2 + (size_t)(li - 5) * (4u << 20); lim = li == 5 ? (4u << 20) : (unsigned)(L * F * 2 - (4 << 20)); }
        else { base = (const char*)out; lim = 2u << 20; }
        if (off < lim) touch(cdummy, off, base);
    }
    if (tid < 32) lf[tid] = 0;
    wait_lgkm0();
    __builtin_amdgcn_s_barrier();

    if (w < 4) {
        __builtin_amdgcn_s_setprio(3);
        const int s0 = b * 8;
        const u32 tag = ld_sc1_4(gf + GF_EPOCH) + 1u;
        STAMP(0, 8);
        lds_arrive(&lf[LF_UARR], lane);
        float gval[2];
#pragma unroll
        for (int i = 0; i < 2; i++) {
            const int r = 2 * w + i;
            float x[8];
#pragma unroll
            for (int j = 0; j < 4; j++) { x[2 * j] = lo_f(xv[i][j]); x[2 * j + 1] = hi_f(xv[i][j]); }
            float sum = 0.f, gs = 0.f;
#pragma unroll
            for (int j = 0; j < 4; j++) {
                sum += x[2 * j] + x[2 * j + 1];
                u32 p = pk2bf(x[2 * j] * lo_f(gv[j]), x[2 * j + 1] * hi_f(gv[j]));
                gs += lo_f(p) + hi_f(p);
            }
            sum = wave_sum(sum);
            gs = wave_sum(gs);
            const float mean = sum * (1.f / D);
            float sq = 0.f;
#pragma unroll
            for (int j = 0; j < 8; j++) { float dl = x[j] - mean; sq = __builtin_fmaf(dl, dl, sq); }
            sq = wave_sum(sq);
            const float var = rbf(sq * (1.f / D));
            const float m = rbf(mean);
            const float sd = rbf(__builtin_sqrtf(rbf(var + 1e-5f)));
            const float rs = __builtin_amdgcn_rcpf(sd);
            u32x4 hv;
#pragma unroll
            for (int j = 0; j < 4; j++) {
                u32 c = pk2bf(x[2 * j] - m, x[2 * j + 1] - m);
                hv[j] = pk2bf(div_nr(lo_f(c), sd, rs), div_nr(hi_f(c), sd, rs));
            }
            const int c16 = lane ^ ((r & 3) << 2);
            *(u32x4*)(smem + STG + r * 1024 + c16 * 16) = hv;
            *(u32x4*)(smem + STG + 8192 + r * 1024 + c16 * 16) = xv[i];
            gval[i] = rbf(__builtin_amdgcn_rcpf(1.f + fast_exp(-rbf(gs))));
        }
        STAMP(0, 10);
        lds_arrive(&lf[LF_LN], lane);
        lds_wait(&lf[LF_LN], 4);
        STAMP(0, 11);
        u32x4 ukeep[2];
        size_t uoff[2];
        {
            const int nt = lane >> 4, q = (lane & 15) >> 2, p = lane & 3;
            const int ks = b >> 2, kq = b & 3;
#pragma unroll
            for (int i = 0; i < 2; i++) {
                const int dt = w * 2 + i;
                const int cb = dt * 4 + nt;
                const unsigned o0 = q * 1024 + ((cb ^ (q << 1)) << 5) + p * 8;
                const unsigned o1 = o0 + 4096;
                u32x2 h0 = tr_read(smem + STG, o0), h1 = tr_read(smem + STG, o1);
                u32x2 u0 = tr_read(smem + STG + 8192, o0), u1 = tr_read(smem + STG + 8192, o1);
                const size_t off = ((size_t)((dt * 64 + ks) * 4 + nt) * 64 + kq * 16 + (lane & 15)) * 8;
                st_sc1_16(hF + off, u32x4{h0[0], h0[1], h1[0], h1[1]});
                ukeep[i] = u32x4{u0[0], u0[1], u1[0], u1[1]};
                uoff[i] = off;
            }
        }
        STAMP(0, 12);
        wait_vm0<0>();
        STAMP(0, 13);
        lds_arrive(&lf[LF_LNPUB], lane);
        if (w == 0) {
            lds_wait(&lf[LF_LNPUB], 4);
            if (lane == 0) st_sc1_4(gf + GF_H + b, tag);
            lds_arrive(&lf[LF_LNPUB], lane);
        } else {
            lds_wait(&lf[LF_LNPUB], 5);
        }
        st_sc1_16(uF + uoff[0], ukeep[0]);
        st_sc1_16(uF + uoff[1], ukeep[1]);
        if (lane < 2) st_sc1_4(g + s0 + 2 * w + lane, __builtin_bit_cast(u32, lane ? gval[1] : gval[0]));
        lds_arrive(&lf[LF_LNST], lane);
        __builtin_amdgcn_s_setprio(0);
        STAMP(0, 1);
        const bool has1 = li < 28;
        const int ftl = li % 7, dtl = li / 7;
        const int ft = (xcd >> 1) * 7 + ftl, dt = (xcd & 1) * 4 + dtl;
        u32x2 bb[4];
        GemmCtx<16, F * 2, true> c1;
        wait_vm0<0>();
        lds_arrive(&lf[LF_LNPUB2], lane);
        if (w == 1) {
            lds_wait(&lf[LF_LNPUB2], 4);
            if (lane == 0) st_sc1_4(gf + GF_H2 + b, tag);
            lds_arrive(&lf[LF_LNPUB2], lane);
        } else {
            lds_wait(&lf[LF_LNPUB2], 5);
        }
        if (has1) {
#pragma unroll
            for (int mt = 0; mt < 4; mt++) gload_early8(bb[mt], b1 + ft * 64 + mt * 16 + (lane >> 4) * 4);
            gemm_begin(c1, smem, (const char*)w1 + ((size_t)ft * 64) * 2, (size_t)512 * F * 2,
                       (const char*)hF + ((size_t)(dt * 64 + w * 16)) * 4096, RING, w, ROT1, lane);
        }
        if (w == 0) {
            poll_flags256(gf + GF_H, tag, lane);
            if (lane == 0) __hip_atomic_store(&lf[LF_H], 1u, __ATOMIC_RELAXED, __HIP_MEMORY_SCOPE_WORKGROUP);
        } else {
            lds_wait(&lf[LF_H], 1);
        }
        STAMP(0, 2);
        if (has1) {
            gemm_run(c1, &lf[LF_W0], 1u, w);
            STAMP(0, 3);
            lds_arrive(&lf[LF_G1DONE], lane);
#pragma unroll
            for (int mt = 0; mt < 4; mt++) tie(bb[mt]);
            f32x4 own[4];
            {
                char* mine = smem + RING + w * 16384;
#define XCH(k) if (w == k) { \
                _Pragma("unroll") for (int nt = 0; nt < 4; nt++) _Pragma("unroll") for (int mt = 0; mt < 4; mt++) \
                    if (nt != k) *(f32x4*)(mine + (nt * 4 + mt) * 1024 + lane * 16) = c1.acc[mt][nt]; \
                _Pragma("unroll") for (int mt = 0; mt < 4; mt++) { own[mt] = c1.acc[mt][k]; asm volatile("" : "+v"(own[mt])); } }
                XCH(0) XCH(1) XCH(2) XCH(3)
#undef XCH
            }
            lds_arrive(&lf[LF_X1], lane);
            lds_wait(&lf[LF_X1], 4);
            STAMP(0, 9);
#pragma unroll
            for (int o = 1; o < 4; o++) {
                const int ow = (w + o) & 3;
#pragma unroll
                for (int mt = 0; mt < 4; mt++)
                    own[mt] += *(const f32x4*)(smem + RING + ow * 16384 + (w * 4 + mt) * 1024 + lane * 16);
            }
            lds_arrive(&lf[LF_X1R], lane);
            const int mq = lane >> 4, n = lane & 15;
#pragma unroll
            for (int mt = 0; mt < 4; mt++) {
                float v[4];
                {
                    u32 p0 = pk2bf(own[mt][0], own[mt][1]), p1 = pk2bf(own[mt][2], own[mt][3]);
                    u32 s0_ = pk2bf(lo_f(p0) + lo_f(bb[mt][0]), hi_f(p0) + hi_f(bb[mt][0]));
                    u32 s1_ = pk2bf(lo_f(p1) + lo_f(bb[mt][1]), hi_f(p1) + hi_f(bb[mt][1]));
                    v[0] = lo_f(s0_); v[1] = hi_f(s0_); v[2] = lo_f(s1_); v[3] = hi_f(s1_);
                }
#pragma unroll
                for (int i = 0; i < 4; i++) {
                    const float x = v[i];
                    const float p = __builtin_fmaf(x * x, -2.f * 0.7978845608028654f * 0.044715f * 1.4426950408889634f,
                                                   -2.f * 0.7978845608028654f * 1.4426950408889634f);
                    const float e = __builtin_amdgcn_exp2f(x * p);
                    v[i] = x * __builtin_amdgcn_rcpf(1.f + e);
                }
                u32x2 o = {pk2bf(v[0], v[1]), pk2bf(v[2], v[3])};
                const int ks = ft * 2 + (mt >> 1), kq = 2 * (mt & 1) + (mq >> 1);
                char* dst = (char*)UF + ((size_t)((dt * 56 + ks) * 4 + w) * 64 + kq * 16 + n) * 16 + (mq & 1) * 8;
                st_sc1_8(dst, o);
            }
            STAMP(0, 14);
            wait_vm0<0>();
            STAMP(0, 15);
            lds_arrive(&lf[LF_UPUB], lane);
            if (w == 0) {
                lds_wait(&lf[LF_UPUB], 4);
                if (lane == 0) st_sc1_4(gf + GF_U + dt * 32 + ft, tag);
                lds_arrive(&lf[LF_UPUB], lane);
            } else {
                lds_wait(&lf[LF_UPUB], 5);
            }
            lds_wait(&lf[LF_X1R], 4);
        }
        if (!has1) lds_arrive(&lf[LF_G1DONE], lane);
        STAMP(0, 4);
        GemmCtx<14, L * 2, false> c2;
        gemm_begin(c2, smem, (const char*)w2 + ((size_t)st * 64) * 2, (size_t)448 * L * 2,
                   (const char*)UF + ((size_t)(dt2 * 56 + w * 14)) * 4096, RING, w, ROT2, lane);
        if (w == 0) {
            poll_flags(gf + GF_U + dt2 * 32, 28, tag, lane);
            if (lane == 0) {
                __hip_atomic_store(&lf[LF_U], 1u, __ATOMIC_RELAXED, __HIP_MEMORY_SCOPE_WORKGROUP);
                if (b == 0) st_sc1_4(gf + GF_EPOCH, tag);
            }
        } else {
            lds_wait(&lf[LF_U], 1);
        }
        STAMP(0, 5);
        gemm_run(c2, &lf[LF_W0], 2u, w);
        STAMP(0, 6);
        f32x4 own[4];
        {
            char* mine = smem + RING + w * 16384;
#define XCH(k) if (w == k) { \
            _Pragma("unroll") for (int nt = 0; nt < 4; nt++) _Pragma("unroll") for (int mt = 0; mt < 4; mt++) \
                if (nt != k) *(f32x4*)(mine + (nt * 4 + mt) * 1024 + lane * 16) = c2.acc[mt][nt]; \
            _Pragma("unroll") for (int mt = 0; mt < 4; mt++) { own[mt] = c2.acc[mt][k]; asm volatile("" : "+v"(own[mt])); } }
            XCH(0) XCH(1) XCH(2) XCH(3)
#undef XCH
        }
        lds_arrive(&lf[LF_X2], lane);
        lds_wait(&lf[LF_X2], 4);
#pragma unroll
        for (int o = 1; o < 4; o++) {
            const int ow = (w + o) & 3;
#pragma unroll
            for (int mt = 0; mt < 4; mt++)
                own[mt] += *(const f32x4*)(smem + RING + ow * 16384 + (w * 4 + mt) * 1024 + lane * 16);
        }
        lds_wait(&lf[LF_EPI], 4);
        {
            const int i = lane & 15, mq = lane >> 4, q = i >> 2, p = lane & 3;
            const float gg = *(const float*)(smem + STG + 8192 + (w * 16 + i) * 4);
            const float bias = *(const float*)(smem + STG + 8192 + 256 + (w * 16 + i) * 4);
            const float og = rbf(1.f - gg);
            u16* dst = out + (size_t)(st * 64 + w * 16 + i) * D + dt2 * 64 + mq * 4;
#pragma unroll
            for (int mt = 0; mt < 4; mt++) {
                const int r0 = 16 * mt + 4 * mq + q;
                const int f0 = ((r0 >> 1) & 1) | (((r0 >> 3) & 1) << 1);
                const u32x2 av = tr_read(smem, STG + r0 * 128 + ((w ^ f0) << 5) + p * 8);
                u32x2 o;
#pragma unroll
                for (int e = 0; e < 2; e++) {
                    u32 y = pk2bf(own[mt][2 * e], own[mt][2 * e + 1]);
                    y = pk2bf(lo_f(y) + bias, hi_f(y) + bias);
                    u32 t1 = pk2bf(gg * lo_f(av[e]), gg * hi_f(av[e]));
                    u32 t2 = pk2bf(og * lo_f(y), og * hi_f(y));
                    o[e] = pk2bf(lo_f(t1) + lo_f(t2), hi_f(t1) + hi_f(t2));
                }
                *(u32x2*)(dst + mt * 16) = o;
            }
        }
        STAMP(0, 7);
        STAMP_DUMP;
        return;
    }

    const int wv = w - 4, ch = wv >> 1, hf = wv & 1;
    const int d = xcd * 64 + li * 2 + ch;
    const int n = hf * 64 + lane;
    u32 r_are, r_aim, r_bre, r_bim, r_cre, r_cim, r_ldt, r_dsk, r_ep;
    {
        const unsigned po = (unsigned)(d * NS + n) * 2u;
        asm volatile("global_load_ushort %0, %9, %10\n\tglobal_load_ushort %1, %9, %11\n\t"
                     "global_load_ushort %2, %9, %12\n\tglobal_load_ushort %3, %9, %13\n\t"
                     "global_load_ushort %4, %9, %14\n\tglobal_load_ushort %5, %9, %15\n\t"
                     "global_load_ushort %6, %16, off\n\tglobal_load_ushort %7, %17, off\n\t"
                     "global_load_dword %8, %18, off sc1"
                     : "=&v"(r_are), "=&v"(r_aim), "=&v"(r_bre), "=&v"(r_bim), "=&v"(r_cre), "=&v"(r_cim), "=&v"(r_ldt), "=&v"(r_dsk), "=&v"(r_ep)
                     : "v"(po), "s"(Are), "s"(Aim), "s"(Bre), "s"(Bim), "s"(Cre), "s"(Cim), "v"(logdt + d), "v"(Dskip + d), "v"(gf + GF_EPOCH)
                     : "memory");
    }
    u32 dummy = 0;
    {
        const char* base = (const char*)w1 + ((size_t)(li * 64) * F + (xcd >> 1) * 448) * 2;
#pragma unroll
        for (int i = 0; i < T1; i++) {
            int idx = (i * 4 + wv) * 64 + lane;
            idx = idx < 448 ? idx : 447;
            const int row = idx / 7, sec = idx - row * 7;
            touch(dummy, (unsigned)(row * (F * 2) + sec * 128), base);
        }
    }
    asm volatile("s_waitcnt vmcnt(" STR(T1) ")" : "+v"(r_are), "+v"(r_aim), "+v"(r_bre), "+v"(r_bim), "+v"(r_cre), "+v"(r_cim), "+v"(r_ldt), "+v"(r_dsk), "+v"(r_ep) :: "memory");
    const u32 tag = r_ep + 1u;
    const unsigned sb_off = SSM0 + ch * SSM_SIZE;
    char* sb = smem + sb_off;
    unsigned* fl = lf + LF_SSM + ch * 4;
    const float dt = rbf(fast_exp(lo_f(r_ldt)));
    const float zr = dt * lo_f(r_are), zi = dt * lo_f(r_aim);
    const float dsk = lo_f(r_dsk);
    f32x2 W;
    {
        const f32x2 B = {dt * lo_f(r_bre), dt * lo_f(r_bim)};
        const f32x2 C = {lo_f(r_cre), lo_f(r_cim)};
        W = cmul(C, B);
    }
    u32x4 kix[2][2];
#pragma unroll
    for (int i = 0; i < 2; i++) {
        const u32x4* pt = (const u32x4*)(KIDX.v + (hf * 128 + i * 64 + lane) * 16);
        kix[i][0] = pt[0]; kix[i][1] = pt[1];
    }
    if (T1 < 2 && TMODE == 1) {
        lds_wait(&lf[LF_UARR], 4);
        const char* base = (const char*)w1 + ((size_t)(li * 64) * F + (xcd >> 1) * 448) * 2;
#pragma unroll
        for (int i = T1; i < 2; i++) {
            int idx = (i * 4 + wv) * 64 + lane;
            idx = idx < 448 ? idx : 447;
            const int row = idx / 7, sec = idx - row * 7;
            touch(dummy, (unsigned)(row * (F * 2) + sec * 128), base);
        }
    }
    STAMP(0, 1);
    char* tbl = sb + SSM_COPY + hf * TBL_SIZE;
    char* tw = tbl + lane * 8;
    const char* trd = tbl + (lane & 15) * TBL_PITCH + (lane >> 4) * 16;
    u32x4 Bh[8], Bl[8];
    f32x4 kacc[3];
    {
        f32x2 E8 = cexp_scaled(zr, zi, 8.f);
        E8.y = -E8.y;
        f32x2 pw[16];
        pw[0] = f32x2{1.f, 0.f};
        pw[1] = E8;
#pragma unroll
        for (int q = 2; q < 16; q++) pw[q] = (q & 1) ? cmul(pw[q - 1], E8) : cmul(pw[q / 2], pw[q / 2]);
#pragma unroll
        for (int q = 0; q < 16; q++) *(u32x2*)(tw + q * TBL_PITCH) = split_bf(pw[q]);
        wait_lgkm0();
#pragma unroll
        for (int T = 0; T < 8; T++) {
            u32x4 v = *(const u32x4*)(trd + T * 64);
            Bh[T] = u32x4{v[0], v[0], v[2], v[2]};
            Bl[T] = u32x4{v[1], v[1], v[3], v[3]};
        }
        wait_lgkm0();
    }
    if (T1 < 2 && TMODE != 1) {
        if (TMODE == 0) lds_wait(&lf[LF_LNST], 4);
        else lds_wait(&lf[LF_H], 1);
        const char* base = (const char*)w1 + ((size_t)(li * 64) * F + (xcd >> 1) * 448) * 2;
#pragma unroll
        for (int i = T1; i < 2; i++) {
            int idx = (i * 4 + wv) * 64 + lane;
            idx = idx < 448 ? idx : 447;
            const int row = idx / 7, sec = idx - row * 7;
            touch(dummy, (unsigned)(row * (F * 2) + sec * 128), base);
        }
    }
    {
        const f32x2 E1 = cexp_scaled(zr, zi, 1.f);
        const f32x2 E128 = cexp_scaled(zr, zi, 128.f);
        f32x2 WY[8];
        {
            f32x2 Y[8];
            Y[0] = f32x2{1.f, 0.f}; Y[1] = E1;
#pragma unroll
            for (int c = 2; c < 8; c++) Y[c] = (c & 1) ? cmul(Y[c - 1], E1) : cmul(Y[c / 2], Y[c / 2]);
            WY[0] = W;
#pragma unroll
            for (int c = 1; c < 8; c++) WY[c] = cmul(W, Y[c]);
        }
        f32x2 Xp[17];
        Xp[0] = f32x2{1.f, 0.f}; Xp[1] = E128;
#pragma unroll
        for (int a = 2; a < 17; a++) Xp[a] = (a & 1) ? cmul(Xp[a - 1], E128) : cmul(Xp[a / 2], Xp[a / 2]);
#pragma unroll
        for (int tile = 0; tile < 3; tile++) {
#pragma unroll
            for (int r = 0; r < 16; r++) {
                f32x2 v;
                bool valid = true;
                if (tile == 0) { v = (r < 8) ? WY[r] : cmul(WY[r - 8], Xp[1]); }
                else if (tile == 1) {
                    if (r < 8) v = cmul(WY[2 * (r & 3)], Xp[2 + (r >> 2)]);
                    else v = cmul(WY[4 * (r & 1)], Xp[4 + ((r - 8) >> 1)]);
                } else {
                    if (r < 8) v = cmul(W, Xp[8 + r]);
                    else if (r == 8) v = cmul(W, Xp[16]);
                    else valid = false;
                }
                if (valid) *(u32x2*)(tw + r * TBL_PITCH) = split_bf(v);
            }
            wait_lgkm0();
            f32x4 a4 = {0.f, 0.f, 0.f, 0.f};
#pragma unroll
            for (int T = 0; T < 8; T++) {
                u32x4 av = *(const u32x4*)(trd + T * 64);
                a4 = mfma16(av, Bh[T], a4);
                a4 = mfma16(av, Bl[T], a4);
            }
            kacc[tile] = a4;
            wait_lgkm0();
        }
    }
    STAMP(0, 2);
    {
        float* P = (float*)(sb + SSM_P + hf * 3456);
        const int mq = lane >> 4, col = lane & 15;
#pragma unroll
        for (int tile = 0; tile < 3; tile++)
#pragma unroll
            for (int r = 0; r < 4; r++)
                if (!(tile == 2 && r == 1 && 0)) P[(tile * 16 + mq * 4 + r) * 18 + col] = kacc[tile][r];
        wait_lgkm0();
        if (lane == 0) P[KC_ZERO * 18] = 0.f;
    }
    if (hf == 0 && lane < 32) *(u32x4*)(sb + SSM_U + lane * 16) = u32x4{0, 0, 0, 0};
    lds_arrive(&fl[0], lane);
    touch_done(dummy);
    tie(cdummy);
    if (wv == 0) {
        poll_flags256(gf + GF_H2, tag, lane);
        if (lane == 0) __hip_atomic_store(&lf[LF_SSM + 9], 1u, __ATOMIC_RELAXED, __HIP_MEMORY_SCOPE_WORKGROUP);
    } else {
        lds_wait(&lf[LF_SSM + 9], 1);
    }
    u32x4 ug0, ug1;
    const int up0 = hf * 128 + lane, up1 = up0 + 64;
    {
        const int dt_ = d >> 6, nt = (d >> 4) & 3, m = d & 15;
        const char* src = (const char*)uF + ((size_t)(dt_ * 64 * 4 + nt) * 64 + m) * 16;
        const char* pa = src + (up0 >> 2) * 4096 + (up0 & 3) * 256;
        const char* pb = src + (up1 >> 2) * 4096 + (up1 & 3) * 256;
        asm volatile("global_load_dwordx4 %0, %2, off sc1\n\tglobal_load_dwordx4 %1, %3, off sc1"
                     : "=&v"(ug0), "=&v"(ug1) : "v"(pa), "v"(pb) : "memory");
    }
    lds_wait(&fl[0], 2);
    STAMP(0, 3);
    {
        const char* P0 = sb + SSM_P;
        const char* P1 = sb + SSM_P + 3456;
#pragma unroll
        for (int i = 0; i < 2; i++) {
            const int ub = hf * 128 + i * 64 + lane;
            float v[11];
#pragma unroll
            for (int j = 0; j < 11; j++) {
                const u32 word = kix[i][j >> 3][(j >> 1) & 3];
                const unsigned off = (j & 1) ? (word >> 16) : (word & 0xffffu);
                v[j] = *(const float*)(P0 + off) + *(const float*)(P1 + off);
            }
            u32 e[5], o[5];
#pragma unroll
            for (int k = 0; k < 5; k++) { e[k] = pk2bf(v[2 * k], v[2 * k + 1]); o[k] = pk2bf(v[2 * k + 1], v[2 * k + 2]); }
            char* dst = sb + SSM_COPY + ub * 16;
            *(u32x4*)(dst) = u32x4{e[0], e[1], e[2], e[3]};
            *(u32x4*)(dst + CP_STRIDE) = u32x4{o[0], o[1], o[2], o[3]};
            *(u32x4*)(dst + 2 * CP_STRIDE) = u32x4{e[1], e[2], e[3], e[4]};
            *(u32x4*)(dst + 3 * CP_STRIDE) = u32x4{o[1], o[2], o[3], o[4]};
        }
        if (hf == 0 && lane < 8) {
            *(u32x4*)(sb + SSM_COPY + (lane >> 1) * CP_STRIDE + (256 + (lane & 1)) * 16) = u32x4{0, 0, 0, 0};
        }
    }
    STAMP(0, 4);
    asm volatile("s_waitcnt vmcnt(0)" : "+v"(ug0), "+v"(ug1) :: "memory");
    *(u32x4*)(sb + SSM_U + 512 + up0 * 16) = ug0;
    *(u32x4*)(sb + SSM_U + 512 + up1 * 16) = ug1;
    {
        const char* base = (const char*)w2 + ((size_t)(li * 56) * L + (xcd >> 1) * 512) * 2;
#pragma unroll
        for (int i = 0; i < 2; i++) {
            const int idx = (wv * 2 + i) * 64 + lane;
            if (idx < 448) touch(dummy, (unsigned)((idx >> 3) * (L * 2) + (idx & 7) * 128), base);
        }
    }
    lds_arrive(&fl[1], lane);
    lds_wait(&fl[1], 2);
    STAMP(0, 5);
    f32x4 cacc[8];
#pragma unroll
    for (int i = 0; i < 8; i++) cacc[i] = f32x4{0.f, 0.f, 0.f, 0.f};
    {
        const int m = lane & 15, q = lane >> 4;
        const int gq = m >> 2, e = m & 3, c = 3 - e;
        const char* abase = sb + SSM_COPY + c * CP_STRIDE + 4104 - 8 * gq - 16 * q - 64 * 63 + 64 * (7 - hf * 4 - 3);
        const char* bbase = sb + SSM_U + 528 + 32 * m - 16 * q - 64 * 7 + 64 * (7 - hf * 4 - 3);
#pragma unroll
        for (int rr = 0; rr < 4; rr++) {
            const char* ab = abase + 64 * (3 - rr);
            const char* bb = bbase + 64 * (3 - rr);
            u32x4 A[8], B[8];
#pragma unroll
            for (int pp = 0; pp < 8; pp++) {
                const u32x2 a0 = *(const u32x2*)(ab + 64 * (56 - 8 * pp));
                const u32x2 a1 = *(const u32x2*)(ab + 64 * (56 - 8 * pp) + 8);
                A[pp] = u32x4{a0[0], a0[1], a1[0], a1[1]};
                B[pp] = *(const u32x4*)(bb + 512 * pp);
            }
#pragma unroll
            for (int i = 0; i < 8; i++)
#pragma unroll
                for (int pp = 0; pp <= i; pp++) cacc[i] = mfma16(A[pp], B[i - pp], cacc[i]);
        }
    }
    STAMP(0, 6);
    f32x4 keep[4];
    {
        char* xo = sb + SSM_X + hf * 4096;
        if (hf) {
#pragma unroll
            for (int i = 0; i < 4; i++) { *(f32x4*)(xo + i * 1024 + lane * 16) = cacc[i]; keep[i] = cacc[4 + i]; asm volatile("" : "+v"(keep[i])); }
        } else {
#pragma unroll
            for (int i = 0; i < 4; i++) { *(f32x4*)(xo + i * 1024 + lane * 16) = cacc[4 + i]; keep[i] = cacc[i]; asm volatile("" : "+v"(keep[i])); }
        }
    }
    lds_arrive(&fl[2], lane);
    lds_wait(&fl[2], 2);
    {
        const char* xi = sb + SSM_X + (hf ^ 1) * 4096;
        const int nn = lane & 15, mq = lane >> 4;
#pragma unroll
        for (int i = 0; i < 4; i++) {
            f32x4 v = keep[i];
            v += *(const f32x4*)(xi + i * 1024 + lane * 16);
            const int t = 256 * (hf * 4 + i) + 16 * nn + 4 * mq;
            const u32x2 uu = *(const u32x2*)(sb + SSM_U + 512 + t * 2);
            u32 y0 = pk2bf(v[0], v[1]), y1 = pk2bf(v[2], v[3]);
            u32 s0 = pk2bf(lo_f(uu[0]) * dsk, hi_f(uu[0]) * dsk), s1 = pk2bf(lo_f(uu[1]) * dsk, hi_f(uu[1]) * dsk);
            u32x2 o = {pk2bf(lo_f(y0) + lo_f(s0), hi_f(y0) + hi_f(s0)), pk2bf(lo_f(y1) + lo_f(s1), hi_f(y1) + hi_f(s1))};
            st_sc1_8(aT + (size_t)d * L + t, o);
        }
    }
    wait_vm0<0>();
    lds_arrive(&lf[LF_APUB], lane);
    if (wv == 0) {
        lds_wait(&lf[LF_APUB], 4);
        if (lane == 0) st_sc1_4(gf + GF_A + xcd * 32 + li, tag);
    }
    STAMP(0, 7);
    if (wv == 0) {
        poll_flags(gf + GF_A + dt2 * 32, 32, tag, lane);
        if (lane == 0) __hip_atomic_store(&lf[LF_SSM + 8], 1u, __ATOMIC_RELAXED, __HIP_MEMORY_SCOPE_WORKGROUP);
    } else {
        lds_wait(&lf[LF_SSM + 8], 1);
    }
    {
        const int r = 16 * wv + (lane >> 2);
        const int j0 = (lane & 3) * 2, j1 = j0 + 1;
        const char* src = (const char*)aT + ((size_t)(dt2 * 64 + r) * L + st * 64) * 2;
        u32x4 a, c;
        ld2_sc1_16(a, c, src + j0 * 16, src + j1 * 16);
        const int f = ((r >> 1) & 1) | (((r >> 3) & 1) << 1);
        *(u32x4*)(smem + STG + r * 128 + ((j0 ^ (f << 1)) << 4)) = a;
        *(u32x4*)(smem + STG + r * 128 + ((j1 ^ (f << 1)) << 4)) = c;
        if (lane < 16) {
            const int s = st * 64 + wv * 16 + lane;
            const u32 gv = ld_sc1_4(g + s);
            *(u32*)(smem + STG + 8192 + (wv * 16 + lane) * 4) = gv;
            *(float*)(smem + STG + 8192 + 256 + (wv * 16 + lane) * 4) = bf2f(b2[s]);
        }
    }
    lds_arrive(&lf[LF_EPI], lane);
    touch_done(dummy);
    STAMP(0, 8);
    STAMP_DUMP;
}

extern "C" void kernel_launch(void* const* d_in, const int* in_sizes, int n_in,
                              void* d_out, int out_size, void* d_ws, size_t ws_size,
                              hipStream_t stream) {
    const u16* u = (const u16*)d_in[0];
    const u16* Are = (const u16*)d_in[1];
    const u16* Aim = (const u16*)d_in[2];
    const u16* Bre = (const u16*)d_in[3];
    const u16* Bim = (const u16*)d_in[4];
    const u16* Cre = (const u16*)d_in[5];
    const u16* Cim = (const u16*)d_in[6];
    const u16* logdt = (const u16*)d_in[7];
    const u16* Dskip = (const u16*)d_in[8];
    const u16* w1 = (const u16*)d_in[10];
    const u16* b1 = (const u16*)d_in[11];
    const u16* w2 = (const u16*)d_in[12];
    const u16* b2 = (const u16*)d_in[13];
    const u16* gw = (const u16*)d_in[14];
    k_fused<<<256, 512, 0, stream>>>(u, gw, Are, Aim, Bre, Bim, Cre, Cim, logdt, Dskip, w1, b1, w2, b2, (char*)d_ws, (u16*)d_out);
}
```

```cpp
#include <hip/hip_runtime.h>
#include <stdint.h>

typedef unsigned short u16;
typedef unsigned int u32;
using bf16x8 = __attribute__((ext_vector_type(8))) __bf16;
using bf16x2 = __attribute__((ext_vector_type(2))) __bf16;
using s16x4 = __attribute__((ext_vector_type(4))) short;
using f32x4 = __attribute__((ext_vector_type(4))) float;
using f32x2 = __attribute__((ext_vector_type(2))) float;
using u32x4 = __attribute__((ext_vector_type(4))) unsigned;
using u32x2 = __attribute__((ext_vector_type(2))) unsigned;
typedef __attribute__((address_space(3))) s16x4* lds_s16x4_ptr;

#if defined(__HIP_DEVICE_COMPILE__)
#define NOPK __attribute__((target("no-packed-fp32-ops")))
#else
#define NOPK
#endif
#define DEV NOPK __device__ __forceinline__
#define TIDX ((int)__builtin_amdgcn_workitem_id_x())
#define BIDX ((int)__builtin_amdgcn_workgroup_id_x())

#define STAMP_DECL do {} while (0)
#define STAMP(kern, idx) do {} while (0)
#define STAMP_DUMP do {} while (0)

constexpr int L = 2048, D = 512, NS = 128, F = 1792;
#define T1 1
#define TMODE 0
#define CONVDEFER 0
#define TLBWARM 1
#define CODE_BYTES 32000
#define W1LIM 8
#define W1COND ((li & 7) < W1LIM)
#define W2POS 3
#define RK 0
#define ROT1 ((16 * 4 - RK * (ftl + dtl)) & 15)
#define ROT2 ((14 * 4 - RK * (stl + dtl2)) % 14)
#define STR_(x) #x
#define STR(x) STR_(x)

DEV float bf2f(u16 v) { return __builtin_bit_cast(float, ((unsigned)v) << 16); }
DEV u32 pk2bf(float a, float b) {
    f32x2 v = {a, b};
    return __builtin_bit_cast(u32, __builtin_convertvector(v, bf16x2));
}
DEV float lo_f(u32 p) { return __builtin_bit_cast(float, p << 16); }
DEV float hi_f(u32 p) { return __builtin_bit_cast(float, p & 0xffff0000u); }
DEV float rbf(float f) { return lo_f(pk2bf(f, 0.f)); }

DEV u32x2 tr_read(const char* lds_generic_base, unsigned off) {
    s16x4 v = __builtin_amdgcn_ds_read_tr16_b64_v4i16((lds_s16x4_ptr)(lds_generic_base + off));
    return __builtin_bit_cast(u32x2, v);
}
DEV f32x4 mfma16(u32x4 a, u32x4 b, f32x4 c) {
    return __builtin_amdgcn_mfma_f32_16x16x32_bf16(__builtin_bit_cast(bf16x8, a), __builtin_bit_cast(bf16x8, b), c, 0, 0, 0);
}

DEV void gload_early8(u32x2& dst, const void* p) { asm volatile("global_load_dwordx2 %0, %1, off" : "=v"(dst) : "v"(p) : "memory"); }
template <typename T> DEV void tie(T& x) { asm volatile("" : "+v"(x) :: "memory"); }
DEV void touch(u32& dummy, unsigned voff, const void* sbase) {
    asm volatile("global_load_dword %0, %1, %2" : "+v"(dummy) : "v"(voff), "s"(sbase) : "memory");
}
DEV void touch_done(u32& dummy) { asm volatile("s_waitcnt vmcnt(0)" : "+v"(dummy) :: "memory"); }
template <int N>
DEV void wait_vm(u32x4& a, u32x4& b, u32x4& c, u32x4& d) {
    asm volatile("s_waitcnt vmcnt(%4)" : "+v"(a), "+v"(b), "+v"(c), "+v"(d) : "i"(N) : "memory");
}
template <int N>
DEV void wait_vm0() { asm volatile("s_waitcnt vmcnt(%0)" ::"i"(N) : "memory"); }
DEV void wait_lgkm0() { asm volatile("s_waitcnt lgkmcnt(0)" ::: "memory"); }
DEV u32 ld_sc1_4(const void* p) {
    u32 v; asm volatile("global_load_dword %0, %1, off sc1\n\ts_waitcnt vmcnt(0)" : "=&v"(v) : "v"(p) : "memory"); return v;
}
DEV u32x4 ld_sc1_16(const void* p) {
    u32x4 v; asm volatile("global_load_dwordx4 %0, %1, off sc1\n\ts_waitcnt vmcnt(0)" : "=&v"(v) : "v"(p) : "memory"); return v;
}
DEV void ld2_sc1_16(u32x4& a, u32x4& b, const void* pa, const void* pb) {
    asm volatile("global_load_dwordx4 %0, %2, off sc1\n\tglobal_load_dwordx4 %1, %3, off sc1\n\ts_waitcnt vmcnt(0)"
                 : "=&v"(a), "=&v"(b) : "v"(pa), "v"(pb) : "memory");
}
DEV void st_sc1_16(void* p, u32x4 v) { asm volatile("global_store_dwordx4 %0, %1, off sc1\n\ts_nop 1" :: "v"(p), "v"(v) : "memory"); }
DEV void st_sc1_8(void* p, u32x2 v) { asm volatile("global_store_dwordx2 %0, %1, off sc1\n\ts_nop 1" :: "v"(p), "v"(v) : "memory"); }
DEV void st_sc1_4(void* p, u32 v) { asm volatile("global_store_dword %0, %1, off sc1" :: "v"(p), "v"(v) : "memory"); }

template <int CTRL>
DEV float dpp_add(float v) {
    int t = __builtin_amdgcn_update_dpp(0, __builtin_bit_cast(int, v), CTRL, 0xf, 0xf, true);
    return v + __builtin_bit_cast(float, t);
}
DEV float rdlane(float v, int l) { return __builtin_bit_cast(float, __builtin_amdgcn_readlane(__builtin_bit_cast(int, v), l)); }
DEV float wave_sum(float v) {
    v = dpp_add<0xB1>(v);
    v = dpp_add<0x4E>(v);
    v = dpp_add<0x141>(v);
    v = dpp_add<0x140>(v);
    return (rdlane(v, 0) + rdlane(v, 16)) + (rdlane(v, 32) + rdlane(v, 48));
}
DEV float fast_exp(float x) { return __builtin_amdgcn_exp2f(x * 1.4426950408889634f); }
DEV void lds_arrive(unsigned* cnt, int lane) {
    wait_lgkm0();
    if (lane == 0) __hip_atomic_fetch_add(cnt, 1u, __ATOMIC_RELAXED, __HIP_MEMORY_SCOPE_WORKGROUP);
}
DEV void lds_wait(unsigned* cnt, unsigned target) {
    while ((unsigned)__builtin_amdgcn_readfirstlane((int)__hip_atomic_load(cnt, __ATOMIC_RELAXED, __HIP_MEMORY_SCOPE_WORKGROUP)) < target)
        __builtin_amdgcn_s_sleep(1);
    asm volatile("" ::: "memory");
}
constexpr int SPIN_CAP = 20000;
DEV void poll_flags256(const u32* fl, u32 tag, int lane) {
#pragma unroll 1
    for (int spin = 0; spin < SPIN_CAP; spin++) {
        u32x4 v = ld_sc1_16(fl + lane * 4);
        bool ok = v[0] == tag && v[1] == tag && v[2] == tag && v[3] == tag;
        if (__builtin_amdgcn_ballot_w64(ok) == ~0ull) break;
        __builtin_amdgcn_s_sleep(2);
    }
}
DEV void poll_flags(const u32* fl, int n, u32 tag, int lane) {
    const u32* p = fl + (lane < n ? lane : 0);
#pragma unroll 1
    for (int spin = 0; spin < SPIN_CAP; spin++) {
        u32 v = ld_sc1_4(p);
        if (__builtin_amdgcn_ballot_w64(v == tag) == ~0ull) break;
        __builtin_amdgcn_s_sleep(2);
    }
}
DEV float div_nr(float a, float b, float r) {
    float q = a * r;
    float e = __builtin_fmaf(-q, b, a);
    return __builtin_fmaf(e, r, q);
}

struct KIdxTable { u16 v[256 * 16]; };
constexpr int round_bf16_int(int x) {
    if (x < 256) return x;
    int sh = 0;
    while ((x >> sh) >= 256) sh++;
    int q = x >> sh, r = x & ((1 << sh) - 1), half = 1 << (sh - 1);
    if (r > half || (r == half && (q & 1))) q++;
    return q << sh;
}
constexpr int KC_ZERO = 41;
constexpr int compact_pos(int tap) {
    int row = KC_ZERO, col = 0;
    if (tap >= 0) {
        int lr = round_bf16_int(tap);
        if (lr < 256) { row = (lr & 7) + 8 * (lr >> 7); col = (lr >> 3) & 15; }
        else if (lr < 512) { int x = lr - 256; row = 16 + ((x & 7) >> 1) + 4 * (x >> 7); col = (x >> 3) & 15; }
        else if (lr < 1024) { int x = lr - 512; row = 24 + ((x & 7) >> 2) + 2 * (x >> 7); col = (x >> 3) & 15; }
        else if (lr < 2048) { int x = lr - 1024; row = 32 + (x >> 7); col = (x >> 3) & 15; }
        else { row = 40; col = 0; }
    }
    return row * 18 + col;
}
constexpr KIdxTable make_kidx() {
    KIdxTable t{};
    for (int ub = 0; ub < 256; ub++)
        for (int i = 0; i < 16; i++) t.v[ub * 16 + i] = (u16)(4 * compact_pos(2047 - (8 * ub + i)));
    return t;
}
__device__ const KIdxTable KIDX = make_kidx();

DEV f32x2 cmul(f32x2 a, f32x2 b) {
    f32x2 r;
    r.x = __builtin_fmaf(-a.y, b.y, a.x * b.x);
    r.y = __builtin_fmaf(a.y, b.x, a.x * b.y);
    return r;
}
DEV f32x2 cexp_scaled(float zr, float zi, float l) {
    const float xr = zr * l, xi = zi * l;
    const float e = __builtin_amdgcn_exp2f(xr * 1.4426950408889634f);
    const float c_hi = 0.15915494309189535f;
    const float c_lo = 6.4206382e-09f;
    float rh = xi * c_hi;
    float rl = __builtin_fmaf(xi, c_lo, __builtin_fmaf(xi, c_hi, -rh));
    float fr = (rh - __builtin_rintf(rh)) + rl;
    return f32x2{e * __builtin_amdgcn_cosf(fr), e * __builtin_amdgcn_sinf(fr)};
}
DEV u32x2 split_bf(f32x2 v) {
    u32 h = pk2bf(v.x, v.y);
    u32 l = pk2bf(v.x - lo_f(h), v.y - hi_f(h));
    return u32x2{h, l};
}

DEV void dma_slot(unsigned lds_, const unsigned (&voff)[4], const void* sbase) {
    const unsigned lds = (unsigned)__builtin_amdgcn_readfirstlane((int)lds_);
    asm volatile("s_mov_b32 m0, %0\n\ts_nop 0\n\t"
                 "global_load_lds_dwordx4 %1, %5\n\t"
                 "global_load_lds_dwordx4 %2, %5 offset:1024\n\t"
                 "global_load_lds_dwordx4 %3, %5 offset:2048\n\t"
                 "global_load_lds_dwordx4 %4, %5 offset:3072"
                 :: "s"(lds), "v"(voff[0]), "v"(voff[1]), "v"(voff[2]), "v"(voff[3]), "s"(sbase) : "memory", "m0");
}
DEV void xload(u32x4 (&x)[4], unsigned voff, const void* sbase) {
    asm volatile("global_load_dwordx4 %0, %4, %5 sc1\n\t"
                 "global_load_dwordx4 %1, %4, %5 offset:1024 sc1\n\t"
                 "global_load_dwordx4 %2, %4, %5 offset:2048 sc1\n\t"
                 "global_load_dwordx4 %3, %4, %5 offset:3072 sc1"
                 : "+v"(x[0]), "+v"(x[1]), "+v"(x[2]), "+v"(x[3]) : "v"(voff), "s"(sbase) : "memory");
}
template <int S, int WPITCH, bool W_IS_A>
struct GemmCtx {
    const char* smem;
    const char* wbase;
    const char* xbase;
    unsigned ring;
    int nxt, nxtx;
    unsigned wvoff[4];
    unsigned xvoff;
    unsigned raddr[4];
    u32x4 X[4][4];
    f32x4 acc[4][4];
};
template <int J, int S, int WPITCH, bool W_IS_A>
DEV void g_issue_w(GemmCtx<S, WPITCH, W_IS_A>& c) {
    dma_slot(c.ring + J * 4096, c.wvoff, c.wbase + (size_t)((unsigned)c.nxt * (unsigned)(32 * WPITCH)));
    c.nxt = (c.nxt + 1 == S) ? 0 : c.nxt + 1;
}
template <int J, int S, int WPITCH, bool W_IS_A>
DEV void g_issue_x(GemmCtx<S, WPITCH, W_IS_A>& c) {
    xload(c.X[J], c.xvoff, c.xbase + (size_t)((unsigned)c.nxtx * 4096u));
    c.nxtx = (c.nxtx + 1 == S) ? 0 : c.nxtx + 1;
}
template <int J, int N, bool IX, bool IW, int S, int WPITCH, bool W_IS_A>
DEV void g_step(GemmCtx<S, WPITCH, W_IS_A>& c) {
    u32x4(&x)[4] = c.X[J];
    wait_vm<N>(x[0], x[1], x[2], x[3]);
    u32x4 Wf[4];
#pragma unroll
    for (int i = 0; i < 4; i++) {
        u32x2 a = tr_read(c.smem, c.raddr[i] + J * 4096);
        u32x2 b = tr_read(c.smem, c.raddr[i] + J * 4096 + 512);
        Wf[i] = u32x4{a[0], a[1], b[0], b[1]};
    }
    if constexpr (IX) g_issue_x<(J + 3) & 3>(c);
    if constexpr (IW) {
        asm volatile("s_waitcnt lgkmcnt(0)" : "+v"(Wf[0]), "+v"(Wf[1]), "+v"(Wf[2]), "+v"(Wf[3]) :: "memory");
        g_issue_w<J>(c);
    }
#pragma unroll
    for (int i = 0; i < 4; i++)
#pragma unroll
        for (int j = 0; j < 4; j++) {
            if constexpr (W_IS_A) c.acc[i][j] = mfma16(Wf[i], x[j], c.acc[i][j]);
            else c.acc[i][j] = mfma16(x[i], Wf[j], c.acc[i][j]);
        }
    __builtin_amdgcn_sched_barrier(0);
}
template <int S, int WPITCH, bool W_IS_A>
DEV void gemm_begin(GemmCtx<S, WPITCH, W_IS_A>& c, const char* smem, const char* wbase0, size_t wstride, const char* xbase, unsigned ring0, int w, int rot, int lane) {
    c.smem = smem; c.wbase = wbase0 + w * wstride; c.xbase = xbase; c.ring = ring0 + w * 16384; c.nxt = rot; c.nxtx = rot;
#pragma unroll
    for (int i = 0; i < 4; i++) {
        const int sw = (((lane >> 4) & 1) << 1) | ((i & 1) << 2);
        c.wvoff[i] = (unsigned)((8 * i + (lane >> 3)) * WPITCH + (((lane & 7) ^ sw) << 4) - 1024 * i);
    }
    c.xvoff = lane * 16;
    const int g = lane >> 4, q = (lane & 15) >> 2, p = lane & 3;
    const int fl = ((q >> 1) & 1) | ((g & 1) << 1);
#pragma unroll
    for (int i = 0; i < 4; i++) c.raddr[i] = c.ring + (8 * g + q) * 128 + ((i ^ fl) << 5) + p * 8;
    if (w != 0) { g_issue_w<0>(c); g_issue_w<1>(c); g_issue_w<2>(c); g_issue_w<3>(c); }
}
template <int S, int WPITCH, bool W_IS_A>
DEV void gemm_run(GemmCtx<S, WPITCH, W_IS_A>& c, unsigned* w0flag, unsigned w0val, int w
) {
#pragma unroll
    for (int i = 0; i < 4; i++)
#pragma unroll
        for (int j = 0; j < 4; j++) { c.acc[i][j] = f32x4{0.f, 0.f, 0.f, 0.f}; c.X[i][j] = u32x4{0, 0, 0, 0}; }
    if (w == 0) { g_issue_w<0>(c); g_issue_w<1>(c); g_issue_w<2>(c); g_issue_w<3>(c); }
    g_issue_x<0>(c); g_issue_x<1>(c); g_issue_x<2>(c);
    g_step<0, 8, true, true>(c);
    g_step<1, 12, true, true>(c);
    g_step<2, 16, true, true>(c);
    g_step<3, 20, true, true>(c);
    constexpr int STEADY = S - 8;
#pragma unroll 1
    for (int it = 0; it < STEADY / 4; it++) {
        g_step<0, 20, true, true>(c);
        g_step<1, 20, true, true>(c);
        g_step<2, 20, true, true>(c);
        g_step<3, 20, true, true>(c);
    }
    constexpr int R = STEADY % 4;
    if constexpr (R >= 1) g_step<0, 20, true, true>(c);
    if constexpr (R >= 2) g_step<1, 20, true, true>(c);
    if constexpr (R >= 3) g_step<2, 20, true, true>(c);
    g_step<(R + 0) & 3, 20, true, false>(c);
    g_step<(R + 1) & 3, 16, false, false>(c);
    g_step<(R + 2) & 3, 8, false, false>(c);
    g_step<(R + 3) & 3, 0, false, false>(c);
}

constexpr int RING = 0;
constexpr int STG = 65536;
constexpr int SSM0 = STG + 16384;
constexpr int TBL_PITCH = 528;
constexpr int TBL_SIZE = 16 * TBL_PITCH;
constexpr int CP_STRIDE = 4160;
constexpr int SSM_COPY = 0;
constexpr int SSM_U = 16896;
constexpr int SSM_P = SSM_U + 4608;
constexpr int SSM_X = SSM_P + 2 * 3456;
constexpr int SSM_SIZE = SSM_X + 8192;
constexpr int LFLAGS = SSM0 + 2 * SSM_SIZE;
constexpr int LDS_BYTES = LFLAGS + 128;
enum { LF_LN = 0, LF_LNPUB = 1, LF_H = 2, LF_X1 = 3, LF_X1R = 4, LF_UPUB = 5, LF_U = 6, LF_X2 = 7, LF_EPI = 8, LF_APUB = 9, LF_LNST = 10, LF_LNPUB2 = 11, LF_UARR = 24, LF_W0 = 25, LF_G1DONE = 26, LF_SSM = 12   };
constexpr int GF_EPOCH = 0, GF_H = 64, GF_H2 = 64 + 256, GF_U = 64 + 512, GF_A = 64 + 512 + 8 * 32;

NOPK __global__ __launch_bounds__(512) void k_fused(const u16* __restrict__ u, const u16* __restrict__ gw,
                                                    const u16* __restrict__ Are, const u16* __restrict__ Aim,
                                                    const u16* __restrict__ Bre, const u16* __restrict__ Bim,
                                                    const u16* __restrict__ Cre, const u16* __restrict__ Cim,
                                                    const u16* __restrict__ logdt, const u16* __restrict__ Dskip,
                                                    const u16* __restrict__ w1, const u16* __restrict__ b1,
                                                    const u16* __restrict__ w2, const u16* __restrict__ b2,
                                                    char* __restrict__ ws, u16* __restrict__ out) {
    __shared__ __attribute__((aligned(16))) char smem[LDS_BYTES];
    const int tid = TIDX, lane = tid & 63;
    const int w = __builtin_amdgcn_readfirstlane(tid >> 6);
    const int b = BIDX;
    const int xcd = b & 7, li = b >> 3;
    unsigned* lf = (unsigned*)(smem + LFLAGS);
    u16* hF = (u16*)ws;
    u16* uF = (u16*)(ws + (2 << 20));
    u16* aT = (u16*)(ws + (4 << 20));
    u16* UF = (u16*)(ws + (6 << 20));
    float* g = (float*)(ws + (8 << 20));
    u32* gf = (u32*)(ws + (9 << 20));
    const int stl = li & 7, dtl2 = li >> 3;
    const int st = (xcd >> 1) * 8 + stl, dt2 = (xcd & 1) * 4 + dtl2;
    STAMP_DECL;
    STAMP(0, 0);
    u32x4 xv[2], gv;
    if (w < 4) {
#pragma unroll
        for (int i = 0; i < 2; i++) xv[i] = *(const u32x4*)(u + (size_t)(b * 8 + 2 * w + i) * D + lane * 8);
        gv = *(const u32x4*)(gw + lane * 8);
    }
    u32 cdummy = 0;
    if (w == 7) {
        const unsigned long long pc = __builtin_amdgcn_s_getpc() & ~127ull;
        int idx = li * 8 + lane;
        if (lane < 8 && idx * 128 < CODE_BYTES) touch(cdummy, (unsigned)(idx * 128), (const void*)pc);
    }
    if (w == 6 && li < 8) {
        const unsigned off = (unsigned)lane * 65536u;
        const char* base; unsigned lim;
        if (li < 3) { base = ws + (size_t)li * (4u << 20); lim = li < 2 ? (4u << 20) : (2u << 20); }
        else if (li < 5) { base = (const char*)w1 + (size_t)(li - 3) * (4u << 20); lim = li == 3 ? (4u << 20) : (unsigned)(L * F * 2 - (4 << 20)); }
        else if (li < 7) { base = (const char*)w2 + (size_t)(li - 5) * (4u << 20); lim = li == 5 ? (4u << 20) : (unsigned)(L * F * 2 - (4 << 20)); }
        else { base = (const char*)out; lim = 2u << 20; }
        if (off < lim) touch(cdummy, off, base);
    }
    if (tid < 32) lf[tid] = 0;
    wait_lgkm0();
    __builtin_amdgcn_s_barrier();

    if (w < 4) {
        __builtin_amdgcn_s_setprio(3);
        const int s0 = b * 8;
        const u32 tag = ld_sc1_4(gf + GF_EPOCH) + 1u;
        STAMP(0, 8);
        lds_arrive(&lf[LF_UARR], lane);
        float gval[2];
#pragma unroll
        for (int i = 0; i < 2; i++) {
            const int r = 2 * w + i;
            float x[8];
#pragma unroll
            for (int j = 0; j < 4; j++) { x[2 * j] = lo_f(xv[i][j]); x[2 * j + 1] = hi_f(xv[i][j]); }
            float sum = 0.f, gs = 0.f;
#pragma unroll
            for (int j = 0; j < 4; j++) {
                sum += x[2 * j] + x[2 * j + 1];
                u32 p = pk2bf(x[2 * j] * lo_f(gv[j]), x[2 * j + 1] * hi_f(gv[j]));
                gs += lo_f(p) + hi_f(p);
            }
            sum = wave_sum(sum);
            gs = wave_sum(gs);
            const float mean = sum * (1.f / D);
            float sq = 0.f;
#pragma unroll
            for (int j = 0; j < 8; j++) { float dl = x[j] - mean; sq = __builtin_fmaf(dl, dl, sq); }
            sq = wave_sum(sq);
            const float var = rbf(sq * (1.f / D));
            const float m = rbf(mean);
            const float sd = rbf(__builtin_sqrtf(rbf(var + 1e-5f)));
            const float rs = __builtin_amdgcn_rcpf(sd);
            u32x4 hv;
#pragma unroll
            for (int j = 0; j < 4; j++) {
                u32 c = pk2bf(x[2 * j] - m, x[2 * j + 1] - m);
                hv[j] = pk2bf(div_nr(lo_f(c), sd, rs), div_nr(hi_f(c), sd, rs));
            }
            const int c16 = lane ^ ((r & 3) << 2);
            *(u32x4*)(smem + STG + r * 1024 + c16 * 16) = hv;
            *(u32x4*)(smem + STG + 8192 + r * 1024 + c16 * 16) = xv[i];
            gval[i] = rbf(__builtin_amdgcn_rcpf(1.f + fast_exp(-rbf(gs))));
        }
        STAMP(0, 10);
        lds_arrive(&lf[LF_LN], lane);
        lds_wait(&lf[LF_LN], 4);
        STAMP(0, 11);
        u32x4 ukeep[2];
        size_t uoff[2];
        {
            const int nt = lane >> 4, q = (lane & 15) >> 2, p = lane & 3;
            const int ks = b >> 2, kq = b & 3;
#pragma unroll
            for (int i = 0; i < 2; i++) {
                const int dt = w * 2 + i;
                const int cb = dt * 4 + nt;
                const unsigned o0 = q * 1024 + ((cb ^ (q << 1)) << 5) + p * 8;
                const unsigned o1 = o0 + 4096;
                u32x2 h0 = tr_read(smem + STG, o0), h1 = tr_read(smem + STG, o1);
                u32x2 u0 = tr_read(smem + STG + 8192, o0), u1 = tr_read(smem + STG + 8192, o1);
                const size_t off = ((size_t)((dt * 64 + ks) * 4 + nt) * 64 + kq * 16 + (lane & 15)) * 8;
                st_sc1_16(hF + off, u32x4{h0[0], h0[1], h1[0], h1[1]});
                ukeep[i] = u32x4{u0[0], u0[1], u1[0], u1[1]};
                uoff[i] = off;
            }
        }
        STAMP(0, 12);
        wait_vm0<0>();
        STAMP(0, 13);
        lds_arrive(&lf[LF_LNPUB], lane);
        if (w == 0) {
            lds_wait(&lf[LF_LNPUB], 4);
            if (lane == 0) st_sc1_4(gf + GF_H + b, tag);
            lds_arrive(&lf[LF_LNPUB], lane);
        } else {
            lds_wait(&lf[LF_LNPUB], 5);
        }
        st_sc1_16(uF + uoff[0], ukeep[0]);
        st_sc1_16(uF + uoff[1], ukeep[1]);
        if (lane < 2) st_sc1_4(g + s0 + 2 * w + lane, __builtin_bit_cast(u32, lane ? gval[1] : gval[0]));
        lds_arrive(&lf[LF_LNST], lane);
        __builtin_amdgcn_s_setprio(0);
        STAMP(0, 1);
        const bool has1 = li < 28;
        const int ftl = li % 7, dtl = li / 7;
        const int ft = (xcd >> 1) * 7 + ftl, dt = (xcd & 1) * 4 + dtl;
        u32x2 bb[4];
        GemmCtx<16, F * 2, true> c1;
        wait_vm0<0>();
        lds_arrive(&lf[LF_LNPUB2], lane);
        if (w == 1) {
            lds_wait(&lf[LF_LNPUB2], 4);
            if (lane == 0) st_sc1_4(gf + GF_H2 + b, tag);
            lds_arrive(&lf[LF_LNPUB2], lane);
        } else {
            lds_wait(&lf[LF_LNPUB2], 5);
        }
        if (has1) {
#pragma unroll
            for (int mt = 0; mt < 4; mt++) gload_early8(bb[mt], b1 + ft * 64 + mt * 16 + (lane >> 4) * 4);
            gemm_begin(c1, smem, (const char*)w1 + ((size_t)ft * 64) * 2, (size_t)512 * F * 2,
                       (const char*)hF + ((size_t)(dt * 64 + w * 16)) * 4096, RING, w, ROT1, lane);
        }
        if (w == 0) {
            poll_flags256(gf + GF_H, tag, lane);
            if (lane == 0) __hip_atomic_store(&lf[LF_H], 1u, __ATOMIC_RELAXED, __HIP_MEMORY_SCOPE_WORKGROUP);
        } else {
            lds_wait(&lf[LF_H], 1);
        }
        STAMP(0, 2);
        if (has1) {
            gemm_run(c1, &lf[LF_W0], 1u, w);
            STAMP(0, 3);
            lds_arrive(&lf[LF_G1DONE], lane);
#pragma unroll
            for (int mt = 0; mt < 4; mt++) tie(bb[mt]);
            f32x4 own[4];
            {
                char* mine = smem + RING + w * 16384;
#define XCH(k) if (w == k) { \
                _Pragma("unroll") for (int nt = 0; nt < 4; nt++) _Pragma("unroll") for (int mt = 0; mt < 4; mt++) \
                    if (nt != k) *(f32x4*)(mine + (nt * 4 + mt) * 1024 + lane * 16) = c1.acc[mt][nt]; \
                _Pragma("unroll") for (int mt = 0; mt < 4; mt++) { own[mt] = c1.acc[mt][k]; asm volatile("" : "+v"(own[mt])); } }
                XCH(0) XCH(1) XCH(2) XCH(3)
#undef XCH
            }
            lds_arrive(&lf[LF_X1], lane);
            lds_wait(&lf[LF_X1], 4);
            STAMP(0, 9);
#pragma unroll
            for (int o = 1; o < 4; o++) {
                const int ow = (w + o) & 3;
#pragma unroll
                for (int mt = 0; mt < 4; mt++)
                    own[mt] += *(const f32x4*)(smem + RING + ow * 16384 + (w * 4 + mt) * 1024 + lane * 16);
            }
            lds_arrive(&lf[LF_X1R], lane);
            const int mq = lane >> 4, n = lane & 15;
#pragma unroll
            for (int mt = 0; mt < 4; mt++) {
                float v[4];
                {
                    u32 p0 = pk2bf(own[mt][0], own[mt][1]), p1 = pk2bf(own[mt][2], own[mt][3]);
                    u32 s0_ = pk2bf(lo_f(p0) + lo_f(bb[mt][0]), hi_f(p0) + hi_f(bb[mt][0]));
                    u32 s1_ = pk2bf(lo_f(p1) + lo_f(bb[mt][1]), hi_f(p1) + hi_f(bb[mt][1]));
                    v[0] = lo_f(s0_); v[1] = hi_f(s0_); v[2] = lo_f(s1_); v[3] = hi_f(s1_);
                }
#pragma unroll
                for (int i = 0; i < 4; i++) {
                    const float x = v[i];
                    const float p = __builtin_fmaf(x * x, -2.f * 0.7978845608028654f * 0.044715f * 1.4426950408889634f,
                                                   -2.f * 0.7978845608028654f * 1.4426950408889634f);
                    const float e = __builtin_amdgcn_exp2f(x * p);
                    v[i] = x * __builtin_amdgcn_rcpf(1.f + e);
                }
                u32x2 o = {pk2bf(v[0], v[1]), pk2bf(v[2], v[3])};
                const int ks = ft * 2 + (mt >> 1), kq = 2 * (mt & 1) + (mq >> 1);
                char* dst = (char*)UF + ((size_t)((dt * 56 + ks) * 4 + w) * 64 + kq * 16 + n) * 16 + (mq & 1) * 8;
                st_sc1_8(dst, o);
            }
            STAMP(0, 14);
            wait_vm0<0>();
            STAMP(0, 15);
            lds_arrive(&lf[LF_UPUB], lane);
            if (w == 0) {
                lds_wait(&lf[LF_UPUB], 4);
                if (lane == 0) st_sc1_4(gf + GF_U + dt * 32 + ft, tag);
                lds_arrive(&lf[LF_UPUB], lane);
            } else {
                lds_wait(&lf[LF_UPUB], 5);
            }
            lds_wait(&lf[LF_X1R], 4);
        }
        if (!has1) lds_arrive(&lf[LF_G1DONE], lane);
        STAMP(0, 4);
        GemmCtx<14, L * 2, false> c2;
        gemm_begin(c2, smem, (const char*)w2 + ((size_t)st * 64) * 2, (size_t)448 * L * 2,
                   (const char*)UF + ((size_t)(dt2 * 56 + w * 14)) * 4096, RING, w, ROT2, lane);
        if (w == 0) {
            poll_flags(gf + GF_U + dt2 * 32, 28, tag, lane);
            if (lane == 0) {
                __hip_atomic_store(&lf[LF_U], 1u, __ATOMIC_RELAXED, __HIP_MEMORY_SCOPE_WORKGROUP);
                if (b == 0) st_sc1_4(gf + GF_EPOCH, tag);
            }
        } else {
            lds_wait(&lf[LF_U], 1);
        }
        STAMP(0, 5);
        gemm_run(c2, &lf[LF_W0], 2u, w);
        STAMP(0, 6);
        f32x4 own[4];
        {
            char* mine = smem + RING + w * 16384;
#define XCH(k) if (w == k) { \
            _Pragma("unroll") for (int nt = 0; nt < 4; nt++) _Pragma("unroll") for (int mt = 0; mt < 4; mt++) \
                if (nt != k) *(f32x4*)(mine + (nt * 4 + mt) * 1024 + lane * 16) = c2.acc[mt][nt]; \
            _Pragma("unroll") for (int mt = 0; mt < 4; mt++) { own[mt] = c2.acc[mt][k]; asm volatile("" : "+v"(own[mt])); } }
            XCH(0) XCH(1) XCH(2) XCH(3)
#undef XCH
        }
        lds_arrive(&lf[LF_X2], lane);
        lds_wait(&lf[LF_X2], 4);
#pragma unroll
        for (int o = 1; o < 4; o++) {
            const int ow = (w + o) & 3;
#pragma unroll
            for (int mt = 0; mt < 4; mt++)
                own[mt] += *(const f32x4*)(smem + RING + ow * 16384 + (w * 4 + mt) * 1024 + lane * 16);
        }
        lds_wait(&lf[LF_EPI], 4);
        {
            const int i = lane & 15, mq = lane >> 4, q = i >> 2, p = lane & 3;
            const float gg = *(const float*)(smem + STG + 8192 + (w * 16 + i) * 4);
            const float bias = *(const float*)(smem + STG + 8192 + 256 + (w * 16 + i) * 4);
            const float og = rbf(1.f - gg);
            u16* dst = out + (size_t)(st * 64 + w * 16 + i) * D + dt2 * 64 + mq * 4;
#pragma unroll
            for (int mt = 0; mt < 4; mt++) {
                const int r0 = 16 * mt + 4 * mq + q;
                const int f0 = ((r0 >> 1) & 1) | (((r0 >> 3) & 1) << 1);
                const u32x2 av = tr_read(smem, STG + r0 * 128 + ((w ^ f0) << 5) + p * 8);
                u32x2 o;
#pragma unroll
                for (int e = 0; e < 2; e++) {
                    u32 y = pk2bf(own[mt][2 * e], own[mt][2 * e + 1]);
                    y = pk2bf(lo_f(y) + bias, hi_f(y) + bias);
                    u32 t1 = pk2bf(gg * lo_f(av[e]), gg * hi_f(av[e]));
                    u32 t2 = pk2bf(og * lo_f(y), og * hi_f(y));
                    o[e] = pk2bf(lo_f(t1) + lo_f(t2), hi_f(t1) + hi_f(t2));
                }
                *(u32x2*)(dst + mt * 16) = o;
            }
        }
        STAMP(0, 7);
        STAMP_DUMP;
        return;
    }

    const int wv = w - 4, ch = wv >> 1, hf = wv & 1;
    const int d = xcd * 64 + li * 2 + ch;
    const int n = hf * 64 + lane;
    u32 r_are, r_aim, r_bre, r_bim, r_cre, r_cim, r_ldt, r_dsk, r_ep;
    {
        const unsigned po = (unsigned)(d * NS + n) * 2u;
        asm volatile("global_load_ushort %0, %9, %10\n\tglobal_load_ushort %1, %9, %11\n\t"
                     "global_load_ushort %2, %9, %12\n\tglobal_load_ushort %3, %9, %13\n\t"
                     "global_load_ushort %4, %9, %14\n\tglobal_load_ushort %5, %9, %15\n\t"
                     "global_load_ushort %6, %16, off\n\tglobal_load_ushort %7, %17, off\n\t"
                     "global_load_dword %8, %18, off sc1"
                     : "=&v"(r_are), "=&v"(r_aim), "=&v"(r_bre), "=&v"(r_bim), "=&v"(r_cre), "=&v"(r_cim), "=&v"(r_ldt), "=&v"(r_dsk), "=&v"(r_ep)
                     : "v"(po), "s"(Are), "s"(Aim), "s"(Bre), "s"(Bim), "s"(Cre), "s"(Cim), "v"(logdt + d), "v"(Dskip + d), "v"(gf + GF_EPOCH)
                     : "memory");
    }
    u32 dummy = 0;
    {
        const char* base = (const char*)w1 + ((size_t)(li * 64) * F + (xcd >> 1) * 448) * 2;
#pragma unroll
        for (int i = 0; i < T1; i++) {
            int idx = (i * 4 + wv) * 64 + lane;
            idx = idx < 448 ? idx : 447;
            const int row = idx / 7, sec = idx - row * 7;
            touch(dummy, (W1COND) ? (unsigned)(row * (F * 2) + sec * 128) : 0u, base);
        }
    }
    asm volatile("s_waitcnt vmcnt(" STR(T1) ")" : "+v"(r_are), "+v"(r_aim), "+v"(r_bre), "+v"(r_bim), "+v"(r_cre), "+v"(r_cim), "+v"(r_ldt), "+v"(r_dsk), "+v"(r_ep) :: "memory");
    const u32 tag = r_ep + 1u;
    const unsigned sb_off = SSM0 + ch * SSM_SIZE;
    char* sb = smem + sb_off;
    unsigned* fl = lf + LF_SSM + ch * 4;
    const float dt = rbf(fast_exp(lo_f(r_ldt)));
    const float zr = dt * lo_f(r_are), zi = dt * lo_f(r_aim);
    const float dsk = lo_f(r_dsk);
    f32x2 W;
    {
        const f32x2 B = {dt * lo_f(r_bre), dt * lo_f(r_bim)};
        const f32x2 C = {lo_f(r_cre), lo_f(r_cim)};
        W = cmul(C, B);
    }
    u32x4 kix[2][2];
#pragma unroll
    for (int i = 0; i < 2; i++) {
        const u32x4* pt = (const u32x4*)(KIDX.v + (hf * 128 + i * 64 + lane) * 16);
        kix[i][0] = pt[0]; kix[i][1] = pt[1];
    }
    if (T1 < 2 && TMODE == 1) {
        lds_wait(&lf[LF_UARR], 4);
        const char* base = (const char*)w1 + ((size_t)(li * 64) * F + (xcd >> 1) * 448) * 2;
#pragma unroll
        for (int i = T1; i < 2; i++) {
            int idx = (i * 4 + wv) * 64 + lane;
            idx = idx < 448 ? idx : 447;
            const int row = idx / 7, sec = idx - row * 7;
            touch(dummy, (W1COND) ? (unsigned)(row * (F * 2) + sec * 128) : 0u, base);
        }
    }
    STAMP(0, 1);
    char* tbl = sb + SSM_COPY + hf * TBL_SIZE;
    char* tw = tbl + lane * 8;
    const char* trd = tbl + (lane & 15) * TBL_PITCH + (lane >> 4) * 16;
    u32x4 Bh[8], Bl[8];
    f32x4 kacc[3];
    {
        f32x2 E8 = cexp_scaled(zr, zi, 8.f);
        E8.y = -E8.y;
        f32x2 pw[16];
        pw[0] = f32x2{1.f, 0.f};
        pw[1] = E8;
#pragma unroll
        for (int q = 2; q < 16; q++) pw[q] = (q & 1) ? cmul(pw[q - 1], E8) : cmul(pw[q / 2], pw[q / 2]);
#pragma unroll
        for (int q = 0; q < 16; q++) *(u32x2*)(tw + q * TBL_PITCH) = split_bf(pw[q]);
        wait_lgkm0();
#pragma unroll
        for (int T = 0; T < 8; T++) {
            u32x4 v = *(const u32x4*)(trd + T * 64);
            Bh[T] = u32x4{v[0], v[0], v[2], v[2]};
            Bl[T] = u32x4{v[1], v[1], v[3], v[3]};
        }
        wait_lgkm0();
    }
    if (T1 < 2 && TMODE != 1) {
        if (TMODE == 0) lds_wait(&lf[LF_LNST], 4);
        else lds_wait(&lf[LF_H], 1);
        const char* base = (const char*)w1 + ((size_t)(li * 64) * F + (xcd >> 1) * 448) * 2;
#pragma unroll
        for (int i = T1; i < 2; i++) {
            int idx = (i * 4 + wv) * 64 + lane;
            idx = idx < 448 ? idx : 447;
            const int row = idx / 7, sec = idx - row * 7;
            touch(dummy, (W1COND) ? (unsigned)(row * (F * 2) + sec * 128) : 0u, base);
        }
    }
    {
        const f32x2 E1 = cexp_scaled(zr, zi, 1.f);
        const f32x2 E128 = cexp_scaled(zr, zi, 128.f);
        f32x2 WY[8];
        {
            f32x2 Y[8];
            Y[0] = f32x2{1.f, 0.f}; Y[1] = E1;
#pragma unroll
            for (int c = 2; c < 8; c++) Y[c] = (c & 1) ? cmul(Y[c - 1], E1) : cmul(Y[c / 2], Y[c / 2]);
            WY[0] = W;
#pragma unroll
            for (int c = 1; c < 8; c++) WY[c] = cmul(W, Y[c]);
        }
        f32x2 Xp[17];
        Xp[0] = f32x2{1.f, 0.f}; Xp[1] = E128;
#pragma unroll
        for (int a = 2; a < 17; a++) Xp[a] = (a & 1) ? cmul(Xp[a - 1], E128) : cmul(Xp[a / 2], Xp[a / 2]);
#pragma unroll
        for (int tile = 0; tile < 3; tile++) {
#pragma unroll
            for (int r = 0; r < 16; r++) {
                f32x2 v;
                bool valid = true;
                if (tile == 0) { v = (r < 8) ? WY[r] : cmul(WY[r - 8], Xp[1]); }
                else if (tile == 1) {
                    if (r < 8) v = cmul(WY[2 * (r & 3)], Xp[2 + (r >> 2)]);
                    else v = cmul(WY[4 * (r & 1)], Xp[4 + ((r - 8) >> 1)]);
                } else {
                    if (r < 8) v = cmul(W, Xp[8 + r]);
                    else if (r == 8) v = cmul(W, Xp[16]);
                    else valid = false;
                }
                if (valid) *(u32x2*)(tw + r * TBL_PITCH) = split_bf(v);
            }
            wait_lgkm0();
            f32x4 a4 = {0.f, 0.f, 0.f, 0.f};
#pragma unroll
            for (int T = 0; T < 8; T++) {
                u32x4 av = *(const u32x4*)(trd + T * 64);
                a4 = mfma16(av, Bh[T], a4);
                a4 = mfma16(av, Bl[T], a4);
            }
            kacc[tile] = a4;
            wait_lgkm0();
        }
    }
    STAMP(0, 2);
    {
        float* P = (float*)(sb + SSM_P + hf * 3456);
        const int mq = lane >> 4, col = lane & 15;
#pragma unroll
        for (int tile = 0; tile < 3; tile++)
#pragma unroll
            for (int r = 0; r < 4; r++)
                if (!(tile == 2 && r == 1 && 0)) P[(tile * 16 + mq * 4 + r) * 18 + col] = kacc[tile][r];
        wait_lgkm0();
        if (lane == 0) P[KC_ZERO * 18] = 0.f;
    }
    if (hf == 0 && lane < 32) *(u32x4*)(sb + SSM_U + lane * 16) = u32x4{0, 0, 0, 0};
    lds_arrive(&fl[0], lane);
    touch_done(dummy);
    tie(cdummy);
    if (wv == 0) {
        poll_flags256(gf + GF_H2, tag, lane);
        if (lane == 0) __hip_atomic_store(&lf[LF_SSM + 9], 1u, __ATOMIC_RELAXED, __HIP_MEMORY_SCOPE_WORKGROUP);
    } else {
        lds_wait(&lf[LF_SSM + 9], 1);
    }
    u32x4 ug0, ug1;
    const int up0 = hf * 128 + lane, up1 = up0 + 64;
    {
        const int dt_ = d >> 6, nt = (d >> 4) & 3, m = d & 15;
        const char* src = (const char*)uF + ((size_t)(dt_ * 64 * 4 + nt) * 64 + m) * 16;
        const char* pa = src + (up0 >> 2) * 4096 + (up0 & 3) * 256;
        const char* pb = src + (up1 >> 2) * 4096 + (up1 & 3) * 256;
        asm volatile("global_load_dwordx4 %0, %2, off sc1\n\tglobal_load_dwordx4 %1, %3, off sc1"
                     : "=&v"(ug0), "=&v"(ug1) : "v"(pa), "v"(pb) : "memory");
    }
    lds_wait(&fl[0], 2);
    STAMP(0, 3);
    {
        const char* P0 = sb + SSM_P;
        const char* P1 = sb + SSM_P + 3456;
#pragma unroll
        for (int i = 0; i < 2; i++) {
            const int ub = hf * 128 + i * 64 + lane;
            float v[11];
#pragma unroll
            for (int j = 0; j < 11; j++) {
                const u32 word = kix[i][j >> 3][(j >> 1) & 3];
                const unsigned off = (j & 1) ? (word >> 16) : (word & 0xffffu);
                v[j] = *(const float*)(P0 + off) + *(const float*)(P1 + off);
            }
            u32 e[5], o[5];
#pragma unroll
            for (int k = 0; k < 5; k++) { e[k] = pk2bf(v[2 * k], v[2 * k + 1]); o[k] = pk2bf(v[2 * k + 1], v[2 * k + 2]); }
            char* dst = sb + SSM_COPY + ub * 16;
            *(u32x4*)(dst) = u32x4{e[0], e[1], e[2], e[3]};
            *(u32x4*)(dst + CP_STRIDE) = u32x4{o[0], o[1], o[2], o[3]};
            *(u32x4*)(dst + 2 * CP_STRIDE) = u32x4{e[1], e[2], e[3], e[4]};
            *(u32x4*)(dst + 3 * CP_STRIDE) = u32x4{o[1], o[2], o[3], o[4]};
        }
        if (hf == 0 && lane < 8) {
            *(u32x4*)(sb + SSM_COPY + (lane >> 1) * CP_STRIDE + (256 + (lane & 1)) * 16) = u32x4{0, 0, 0, 0};
        }
    }
    STAMP(0, 4);
    asm volatile("s_waitcnt vmcnt(0)" : "+v"(ug0), "+v"(ug1) :: "memory");
    *(u32x4*)(sb + SSM_U + 512 + up0 * 16) = ug0;
    *(u32x4*)(sb + SSM_U + 512 + up1 * 16) = ug1;
    lds_arrive(&fl[1], lane);
    lds_wait(&fl[1], 2);
    STAMP(0, 5);
    f32x4 cacc[8];
#pragma unroll
    for (int i = 0; i < 8; i++) cacc[i] = f32x4{0.f, 0.f, 0.f, 0.f};
    {
        const int m = lane & 15, q = lane >> 4;
        const int gq = m >> 2, e = m & 3, c = 3 - e;
        const char* abase = sb + SSM_COPY + c * CP_STRIDE + 4104 - 8 * gq - 16 * q - 64 * 63 + 64 * (7 - hf * 4 - 3);
        const char* bbase = sb + SSM_U + 528 + 32 * m - 16 * q - 64 * 7 + 64 * (7 - hf * 4 - 3);
#pragma unroll
        for (int rr = 0; rr < 4; rr++) {
            const char* ab = abase + 64 * (3 - rr);
            const char* bb = bbase + 64 * (3 - rr);
            u32x4 A[8], B[8];
#pragma unroll
            for (int pp = 0; pp < 8; pp++) {
                const u32x2 a0 = *(const u32x2*)(ab + 64 * (56 - 8 * pp));
                const u32x2 a1 = *(const u32x2*)(ab + 64 * (56 - 8 * pp) + 8);
                A[pp] = u32x4{a0[0], a0[1], a1[0], a1[1]};
                B[pp] = *(const u32x4*)(bb + 512 * pp);
            }
#pragma unroll
            for (int i = 0; i < 8; i++)
#pragma unroll
                for (int pp = 0; pp <= i; pp++) cacc[i] = mfma16(A[pp], B[i - pp], cacc[i]);
            if (rr == W2POS - 2)
    {
        const char* base = (const char*)w2 + ((size_t)(li * 56) * L + (xcd >> 1) * 512) * 2;
#pragma unroll
        for (int i = 0; i < 2; i++) {
            const int idx = (wv * 2 + i) * 64 + lane;
            if (idx < 448) touch(dummy, (unsigned)((idx >> 3) * (L * 2) + (idx & 7) * 128), base);
        }
    }
        }
    }
    STAMP(0, 6);
    f32x4 keep[4];
    {
        char* xo = sb + SSM_X + hf * 4096;
        if (hf) {
#pragma unroll
            for (int i = 0; i < 4; i++) { *(f32x4*)(xo + i * 1024 + lane * 16) = cacc[i]; keep[i] = cacc[4 + i]; asm volatile("" : "+v"(keep[i])); }
        } else {
#pragma unroll
            for (int i = 0; i < 4; i++) { *(f32x4*)(xo + i * 1024 + lane * 16) = cacc[4 + i]; keep[i] = cacc[i]; asm volatile("" : "+v"(keep[i])); }
        }
    }
    lds_arrive(&fl[2], lane);
    lds_wait(&fl[2], 2);
    {
        const char* xi = sb + SSM_X + (hf ^ 1) * 4096;
        const int nn = lane & 15, mq = lane >> 4;
#pragma unroll
        for (int i = 0; i < 4; i++) {
            f32x4 v = keep[i];
            v += *(const f32x4*)(xi + i * 1024 + lane * 16);
            const int t = 256 * (hf * 4 + i) + 16 * nn + 4 * mq;
            const u32x2 uu = *(const u32x2*)(sb + SSM_U + 512 + t * 2);
            u32 y0 = pk2bf(v[0], v[1]), y1 = pk2bf(v[2], v[3]);
            u32 s0 = pk2bf(lo_f(uu[0]) * dsk, hi_f(uu[0]) * dsk), s1 = pk2bf(lo_f(uu[1]) * dsk, hi_f(uu[1]) * dsk);
            u32x2 o = {pk2bf(lo_f(y0) + lo_f(s0), hi_f(y0) + hi_f(s0)), pk2bf(lo_f(y1) + lo_f(s1), hi_f(y1) + hi_f(s1))};
            st_sc1_8(aT + (size_t)d * L + t, o);
        }
    }
    wait_vm0<0>();
    lds_arrive(&lf[LF_APUB], lane);
    if (wv == 0) {
        lds_wait(&lf[LF_APUB], 4);
        if (lane == 0) st_sc1_4(gf + GF_A + xcd * 32 + li, tag);
    }
    STAMP(0, 7);
    if (wv == 0) {
        poll_flags(gf + GF_A + dt2 * 32, 32, tag, lane);
        if (lane == 0) __hip_atomic_store(&lf[LF_SSM + 8], 1u, __ATOMIC_RELAXED, __HIP_MEMORY_SCOPE_WORKGROUP);
    } else {
        lds_wait(&lf[LF_SSM + 8], 1);
    }
    {
        const int r = 16 * wv + (lane >> 2);
        const int j0 = (lane & 3) * 2, j1 = j0 + 1;
        const char* src = (const char*)aT + ((size_t)(dt2 * 64 + r) * L + st * 64) * 2;
        u32x4 a, c;
        ld2_sc1_16(a, c, src + j0 * 16, src + j1 * 16);
        const int f = ((r >> 1) & 1) | (((r >> 3) & 1) << 1);
        *(u32x4*)(smem + STG + r * 128 + ((j0 ^ (f << 1)) << 4)) = a;
        *(u32x4*)(smem + STG + r * 128 + ((j1 ^ (f << 1)) << 4)) = c;
        if (lane < 16) {
            const int s = st * 64 + wv * 16 + lane;
            const u32 gv = ld_sc1_4(g + s);
            *(u32*)(smem + STG + 8192 + (wv * 16 + lane) * 4) = gv;
            *(float*)(smem + STG + 8192 + 256 + (wv * 16 + lane) * 4) = bf2f(b2[s]);
        }
    }
    lds_arrive(&lf[LF_EPI], lane);
    touch_done(dummy);
    STAMP(0, 8);
    STAMP_DUMP;
}

extern "C" void kernel_launch(void* const* d_in, const int* in_sizes, int n_in,
                              void* d_out, int out_size, void* d_ws, size_t ws_size,
                              hipStream_t stream) {
    const u16* u = (const u16*)d_in[0];
    const u16* Are = (const u16*)d_in[1];
    const u16* Aim = (const u16*)d_in[2];
    const u16* Bre = (const u16*)d_in[3];
    const u16* Bim = (const u16*)d_in[4];
    const u16* Cre = (const u16*)d_in[5];
    const u16* Cim = (const u16*)d_in[6];
    const u16* logdt = (const u16*)d_in[7];
    const u16* Dskip = (const u16*)d_in[8];
    const u16* w1 = (const u16*)d_in[10];
    const u16* b1 = (const u16*)d_in[11];
    const u16* w2 = (const u16*)d_in[12];
    const u16* b2 = (const u16*)d_in[13];
    const u16* gw = (const u16*)d_in[14];
    k_fused<<<256, 512, 0, stream>>>(u, gw, Are, Aim, Bre, Bim, Cre, Cim, logdt, Dskip, w1, b1, w2, b2, (char*)d_ws, (u16*)d_out);
}
```

```cpp
#include <hip/hip_runtime.h>
#include <stdint.h>

typedef unsigned short u16;
typedef unsigned int u32;
using bf16x8 = __attribute__((ext_vector_type(8))) __bf16;
using bf16x2 = __attribute__((ext_vector_type(2))) __bf16;
using s16x4 = __attribute__((ext_vector_type(4))) short;
using f32x4 = __attribute__((ext_vector_type(4))) float;
using f32x2 = __attribute__((ext_vector_type(2))) float;
using u32x4 = __attribute__((ext_vector_type(4))) unsigned;
using u32x2 = __attribute__((ext_vector_type(2))) unsigned;
typedef __attribute__((address_space(3))) s16x4* lds_s16x4_ptr;

#if defined(__HIP_DEVICE_COMPILE__)
#define NOPK __attribute__((target("no-packed-fp32-ops")))
#else
#define NOPK
#endif
#define DEV NOPK __device__ __forceinline__
#define TIDX ((int)__builtin_amdgcn_workitem_id_x())
#define BIDX ((int)__builtin_amdgcn_workgroup_id_x())

constexpr int L = 2048, D = 512, NS = 128, F = 1792;
constexpr int ROT1 = 0, ROT2 = 0;
constexpr int CODE_BYTES = 32000;
constexpr int T1 = 1;
#define STR_(x) #x
#define STR(x) STR_(x)

DEV float bf2f(u16 v) { return __builtin_bit_cast(float, ((unsigned)v) << 16); }
DEV u32 pk2bf(float a, float b) {
    f32x2 v = {a, b};
    return __builtin_bit_cast(u32, __builtin_convertvector(v, bf16x2));
}
DEV float lo_f(u32 p) { return __builtin_bit_cast(float, p << 16); }
DEV float hi_f(u32 p) { return __builtin_bit_cast(float, p & 0xffff0000u); }
DEV float rbf(float f) { return lo_f(pk2bf(f, 0.f)); }

DEV u32x2 tr_read(const char* lds_generic_base, unsigned off) {
    s16x4 v = __builtin_amdgcn_ds_read_tr16_b64_v4i16((lds_s16x4_ptr)(lds_generic_base + off));
    return __builtin_bit_cast(u32x2, v);
}
DEV f32x4 mfma16(u32x4 a, u32x4 b, f32x4 c) {
    return __builtin_amdgcn_mfma_f32_16x16x32_bf16(__builtin_bit_cast(bf16x8, a), __builtin_bit_cast(bf16x8, b), c, 0, 0, 0);
}

DEV void gload_early8(u32x2& dst, const void* p) { asm volatile("global_load_dwordx2 %0, %1, off" : "=v"(dst) : "v"(p) : "memory"); }
template <typename T> DEV void tie(T& x) { asm volatile("" : "+v"(x) :: "memory"); }
DEV void touch(u32& dummy, unsigned voff, const void* sbase) {
    asm volatile("global_load_dword %0, %1, %2" : "+v"(dummy) : "v"(voff), "s"(sbase) : "memory");
}
DEV void touch_done(u32& dummy) { asm volatile("s_waitcnt vmcnt(0)" : "+v"(dummy) :: "memory"); }
template <int N>
DEV void wait_vm(u32x4& a, u32x4& b, u32x4& c, u32x4& d) {
    asm volatile("s_waitcnt vmcnt(%4)" : "+v"(a), "+v"(b), "+v"(c), "+v"(d) : "i"(N) : "memory");
}
template <int N>
DEV void wait_vm0() { asm volatile("s_waitcnt vmcnt(%0)" ::"i"(N) : "memory"); }
DEV void wait_lgkm0() { asm volatile("s_waitcnt lgkmcnt(0)" ::: "memory"); }
DEV u32 ld_sc1_4(const void* p) {
    u32 v; asm volatile("global_load_dword %0, %1, off sc1\n\ts_waitcnt vmcnt(0)" : "=&v"(v) : "v"(p) : "memory"); return v;
}
DEV u32x4 ld_sc1_16(const void* p) {
    u32x4 v; asm volatile("global_load_dwordx4 %0, %1, off sc1\n\ts_waitcnt vmcnt(0)" : "=&v"(v) : "v"(p) : "memory"); return v;
}
DEV void ld2_sc1_16(u32x4& a, u32x4& b, const void* pa, const void* pb) {
    asm volatile("global_load_dwordx4 %0, %2, off sc1\n\tglobal_load_dwordx4 %1, %3, off sc1\n\ts_waitcnt vmcnt(0)"
                 : "=&v"(a), "=&v"(b) : "v"(pa), "v"(pb) : "memory");
}
DEV void st_sc1_16(void* p, u32x4 v) { asm volatile("global_store_dwordx4 %0, %1, off sc1\n\ts_nop 1" :: "v"(p), "v"(v) : "memory"); }
DEV void st_sc1_8(void* p, u32x2 v) { asm volatile("global_store_dwordx2 %0, %1, off sc1\n\ts_nop 1" :: "v"(p), "v"(v) : "memory"); }
DEV void st_sc1_4(void* p, u32 v) { asm volatile("global_store_dword %0, %1, off sc1" :: "v"(p), "v"(v) : "memory"); }

template <int CTRL>
DEV float dpp_add(float v) {
    int t = __builtin_amdgcn_update_dpp(0, __builtin_bit_cast(int, v), CTRL, 0xf, 0xf, true);
    return v + __builtin_bit_cast(float, t);
}
DEV float rdlane(float v, int l) { return __builtin_bit_cast(float, __builtin_amdgcn_readlane(__builtin_bit_cast(int, v), l)); }
DEV float wave_sum(float v) {
    v = dpp_add<0xB1>(v);
    v = dpp_add<0x4E>(v);
    v = dpp_add<0x141>(v);
    v = dpp_add<0x140>(v);
    return (rdlane(v, 0) + rdlane(v, 16)) + (rdlane(v, 32) + rdlane(v, 48));
}
DEV float fast_exp(float x) { return __builtin_amdgcn_exp2f(x * 1.4426950408889634f); }
DEV void lds_arrive(unsigned* cnt, int lane) {
    wait_lgkm0();
    if (lane == 0) __hip_atomic_fetch_add(cnt, 1u, __ATOMIC_RELAXED, __HIP_MEMORY_SCOPE_WORKGROUP);
}
DEV void lds_wait(unsigned* cnt, unsigned target) {
    while ((unsigned)__builtin_amdgcn_readfirstlane((int)__hip_atomic_load(cnt, __ATOMIC_RELAXED, __HIP_MEMORY_SCOPE_WORKGROUP)) < target)
        ;
    asm volatile("" ::: "memory");
}
constexpr int SPIN_CAP = 20000;
DEV void poll_flags256(const u32* fl, u32 tag, int lane) {
#pragma unroll 1
    for (int spin = 0; spin < SPIN_CAP; spin++) {
        u32x4 v = ld_sc1_16(fl + lane * 4);
        bool ok = v[0] == tag && v[1] == tag && v[2] == tag && v[3] == tag;
        if (__builtin_amdgcn_ballot_w64(ok) == ~0ull) break;
        __builtin_amdgcn_s_sleep(1);
    }
}
DEV void poll_flags(const u32* fl, int n, u32 tag, int lane) {
    const u32* p = fl + (lane < n ? lane : 0);
#pragma unroll 1
    for (int spin = 0; spin < SPIN_CAP; spin++) {
        u32 v = ld_sc1_4(p);
        if (__builtin_amdgcn_ballot_w64(v == tag) == ~0ull) break;
        __builtin_amdgcn_s_sleep(1);
    }
}
DEV float div_nr(float a, float b, float r) {
    float q = a * r;
    float e = __builtin_fmaf(-q, b, a);
    return __builtin_fmaf(e, r, q);
}

struct KIdxTable { u16 v[256 * 16]; };
constexpr int round_bf16_int(int x) {
    if (x < 256) return x;
    int sh = 0;
    while ((x >> sh) >= 256) sh++;
    int q = x >> sh, r = x & ((1 << sh) - 1), half = 1 << (sh - 1);
    if (r > half || (r == half && (q & 1))) q++;
    return q << sh;
}
constexpr int KC_ZERO = 41;
constexpr int compact_pos(int tap) {
    int row = KC_ZERO, col = 0;
    if (tap >= 0) {
        int lr = round_bf16_int(tap);
        if (lr < 256) { row = (lr & 7) + 8 * (lr >> 7); col = (lr >> 3) & 15; }
        else if (lr < 512) { int x = lr - 256; row = 16 + ((x & 7) >> 1) + 4 * (x >> 7); col = (x >> 3) & 15; }
        else if (lr < 1024) { int x = lr - 512; row = 24 + ((x & 7) >> 2) + 2 * (x >> 7); col = (x >> 3) & 15; }
        else if (lr < 2048) { int x = lr - 1024; row = 32 + (x >> 7); col = (x >> 3) & 15; }
        else { row = 40; col = 0; }
    }
    return row * 18 + col;
}
constexpr KIdxTable make_kidx() {
    KIdxTable t{};
    for (int ub = 0; ub < 256; ub++)
        for (int i = 0; i < 16; i++) t.v[ub * 16 + i] = (u16)(4 * compact_pos(2047 - (8 * ub + i)));
    return t;
}
__device__ const KIdxTable KIDX = make_kidx();

DEV f32x2 cmul(f32x2 a, f32x2 b) {
    f32x2 r;
    r.x = __builtin_fmaf(-a.y, b.y, a.x * b.x);
    r.y = __builtin_fmaf(a.y, b.x, a.x * b.y);
    return r;
}
DEV f32x2 cexp_scaled(float zr, float zi, float l) {
    const float xr = zr * l, xi = zi * l;
    const float e = __builtin_amdgcn_exp2f(xr * 1.4426950408889634f);
    const float c_hi = 0.15915494309189535f;
    const float c_lo = 6.4206382e-09f;
    float rh = xi * c_hi;
    float rl = __builtin_fmaf(xi, c_lo, __builtin_fmaf(xi, c_hi, -rh));
    float fr = (rh - __builtin_rintf(rh)) + rl;
    return f32x2{e * __builtin_amdgcn_cosf(fr), e * __builtin_amdgcn_sinf(fr)};
}
DEV u32x2 split_bf(f32x2 v) {
    u32 h = pk2bf(v.x, v.y);
    u32 l = pk2bf(v.x - lo_f(h), v.y - hi_f(h));
    return u32x2{h, l};
}

DEV void dma_slot(unsigned lds_, const unsigned (&voff)[4], const void* sbase) {
    const unsigned lds = (unsigned)__builtin_amdgcn_readfirstlane((int)lds_);
    asm volatile("s_mov_b32 m0, %0\n\ts_nop 0\n\t"
                 "global_load_lds_dwordx4 %1, %5\n\t"
                 "global_load_lds_dwordx4 %2, %5 offset:1024\n\t"
                 "global_load_lds_dwordx4 %3, %5 offset:2048\n\t"
                 "global_load_lds_dwordx4 %4, %5 offset:3072"
                 :: "s"(lds), "v"(voff[0]), "v"(voff[1]), "v"(voff[2]), "v"(voff[3]), "s"(sbase) : "memory", "m0");
}
DEV void xload(u32x4 (&x)[4], unsigned voff, const void* sbase) {
    asm volatile("global_load_dwordx4 %0, %4, %5 sc1\n\t"
                 "global_load_dwordx4 %1, %4, %5 offset:1024 sc1\n\t"
                 "global_load_dwordx4 %2, %4, %5 offset:2048 sc1\n\t"
                 "global_load_dwordx4 %3, %4, %5 offset:3072 sc1"
                 : "+v"(x[0]), "+v"(x[1]), "+v"(x[2]), "+v"(x[3]) : "v"(voff), "s"(sbase) : "memory");
}
template <int S, int WPITCH, bool W_IS_A>
struct GemmCtx {
    const char* smem;
    const char* wbase;
    const char* xbase;
    unsigned ring;
    int nxt, nxtx;
    unsigned wvoff[4];
    unsigned xvoff;
    unsigned raddr[4];
    u32x4 X[4][4];
    f32x4 acc[4][4];
};
template <int J, int S, int WPITCH, bool W_IS_A>
DEV void g_issue_w(GemmCtx<S, WPITCH, W_IS_A>& c) {
    dma_slot(c.ring + J * 4096, c.wvoff, c.wbase + (size_t)((unsigned)c.nxt * (unsigned)(32 * WPITCH)));
    c.nxt = (c.nxt + 1 == S) ? 0 : c.nxt + 1;
}
template <int J, int S, int WPITCH, bool W_IS_A>
DEV void g_issue_x(GemmCtx<S, WPITCH, W_IS_A>& c) {
    xload(c.X[J], c.xvoff, c.xbase + (size_t)((unsigned)c.nxtx * 4096u));
    c.nxtx = (c.nxtx + 1 == S) ? 0 : c.nxtx + 1;
}
template <int J, int N, bool IX, bool IW, int S, int WPITCH, bool W_IS_A>
DEV void g_step(GemmCtx<S, WPITCH, W_IS_A>& c) {
    u32x4(&x)[4] = c.X[J];
    wait_vm<N>(x[0], x[1], x[2], x[3]);
    u32x4 Wf[4];
#pragma unroll
    for (int i = 0; i < 4; i++) {
        u32x2 a = tr_read(c.smem, c.raddr[i] + J * 4096);
        u32x2 b = tr_read(c.smem, c.raddr[i] + J * 4096 + 512);
        Wf[i] = u32x4{a[0], a[1], b[0], b[1]};
    }
    if constexpr (IX) g_issue_x<(J + 3) & 3>(c);
    if constexpr (IW) {
        asm volatile("s_waitcnt lgkmcnt(0)" : "+v"(Wf[0]), "+v"(Wf[1]), "+v"(Wf[2]), "+v"(Wf[3]) :: "memory");
        g_issue_w<J>(c);
    }
#pragma unroll
    for (int i = 0; i < 4; i++)
#pragma unroll
        for (int j = 0; j < 4; j++) {
            if constexpr (W_IS_A) c.acc[i][j] = mfma16(Wf[i], x[j], c.acc[i][j]);
            else c.acc[i][j] = mfma16(x[i], Wf[j], c.acc[i][j]);
        }
    __builtin_amdgcn_sched_barrier(0);
}
template <int S, int WPITCH, bool W_IS_A>
DEV void gemm_begin(GemmCtx<S, WPITCH, W_IS_A>& c, const char* smem, const char* wbase0, size_t wstride, const char* xbase, unsigned ring0, int w, int rot, int lane) {
    c.smem = smem; c.wbase = wbase0 + w * wstride; c.xbase = xbase; c.ring = ring0 + w * 16384; c.nxt = rot; c.nxtx = rot;
#pragma unroll
    for (int i = 0; i < 4; i++) {
        const int sw = (((lane >> 4) & 1) << 1) | ((i & 1) << 2);
        c.wvoff[i] = (unsigned)((8 * i + (lane >> 3)) * WPITCH + (((lane & 7) ^ sw) << 4) - 1024 * i);
    }
    c.xvoff = lane * 16;
    const int g = lane >> 4, q = (lane & 15) >> 2, p = lane & 3;
    const int fl = ((q >> 1) & 1) | ((g & 1) << 1);
#pragma unroll
    for (int i = 0; i < 4; i++) c.raddr[i] = c.ring + (8 * g + q) * 128 + ((i ^ fl) << 5) + p * 8;
    if (w != 0) { g_issue_w<0>(c); g_issue_w<1>(c); g_issue_w<2>(c); g_issue_w<3>(c); }
}
template <int S, int WPITCH, bool W_IS_A>
DEV void gemm_run(GemmCtx<S, WPITCH, W_IS_A>& c, unsigned* w0flag, unsigned w0val, int w
) {
#pragma unroll
    for (int i = 0; i < 4; i++)
#pragma unroll
        for (int j = 0; j < 4; j++) { c.acc[i][j] = f32x4{0.f, 0.f, 0.f, 0.f}; c.X[i][j] = u32x4{0, 0, 0, 0}; }
    if (w == 0) { g_issue_w<0>(c); g_issue_w<1>(c); g_issue_w<2>(c); g_issue_w<3>(c); }
    g_issue_x<0>(c); g_issue_x<1>(c); g_issue_x<2>(c);
    g_step<0, 8, true, true>(c);
    g_step<1, 12, true, true>(c);
    g_step<2, 16, true, true>(c);
    g_step<3, 20, true, true>(c);
    constexpr int STEADY = S - 8;
#pragma unroll 1
    for (int it = 0; it < STEADY / 4; it++) {
        g_step<0, 20, true, true>(c);
        g_step<1, 20, true, true>(c);
        g_step<2, 20, true, true>(c);
        g_step<3, 20, true, true>(c);
    }
    constexpr int R = STEADY % 4;
    if constexpr (R >= 1) g_step<0, 20, true, true>(c);
    if constexpr (R >= 2) g_step<1, 20, true, true>(c);
    if constexpr (R >= 3) g_step<2, 20, true, true>(c);
    g_step<(R + 0) & 3, 20, true, false>(c);
    g_step<(R + 1) & 3, 16, false, false>(c);
    g_step<(R + 2) & 3, 8, false, false>(c);
    g_step<(R + 3) & 3, 0, false, false>(c);
}

constexpr int RING = 0;
constexpr int STG = 65536;
constexpr int SSM0 = STG + 16384;
constexpr int TBL_PITCH = 528;
constexpr int TBL_SIZE = 16 * TBL_PITCH;
constexpr int CP_STRIDE = 4160;
constexpr int SSM_COPY = 0;
constexpr int SSM_U = 16896;
constexpr int SSM_P = SSM_U + 4608;
constexpr int SSM_X = SSM_P + 2 * 3456;
constexpr int SSM_SIZE = SSM_X + 8192;
constexpr int LFLAGS = SSM0 + 2 * SSM_SIZE;
constexpr int LDS_BYTES = LFLAGS + 128;
enum { LF_LN = 0, LF_LNPUB = 1, LF_H = 2, LF_X1 = 3, LF_X1R = 4, LF_UPUB = 5, LF_U = 6, LF_X2 = 7, LF_EPI = 8, LF_APUB = 9, LF_LNST = 10, LF_LNPUB2 = 11, LF_UARR = 24, LF_W0 = 25, LF_G1DONE = 26, LF_SSM = 12   };
constexpr int GF_EPOCH = 0, GF_H = 64, GF_H2 = 64 + 256, GF_U = 64 + 512, GF_A = 64 + 512 + 8 * 32;

NOPK __global__ __launch_bounds__(512) void k_fused(const u16* __restrict__ u, const u16* __restrict__ gw,
                                                    const u16* __restrict__ Are, const u16* __restrict__ Aim,
                                                    const u16* __restrict__ Bre, const u16* __restrict__ Bim,
                                                    const u16* __restrict__ Cre, const u16* __restrict__ Cim,
                                                    const u16* __restrict__ logdt, const u16* __restrict__ Dskip,
                                                    const u16* __restrict__ w1, const u16* __restrict__ b1,
                                                    const u16* __restrict__ w2, const u16* __restrict__ b2,
                                                    char* __restrict__ ws, u16* __restrict__ out) {
    __shared__ __attribute__((aligned(16))) char smem[LDS_BYTES];
    const int tid = TIDX, lane = tid & 63;
    const int w = __builtin_amdgcn_readfirstlane(tid >> 6);
    const int b = BIDX;
    const int xcd = b & 7, li = b >> 3;
    unsigned* lf = (unsigned*)(smem + LFLAGS);
    u16* hF = (u16*)ws;
    u16* uF = (u16*)(ws + (2 << 20));
    u16* aT = (u16*)(ws + (4 << 20));
    u16* UF = (u16*)(ws + (6 << 20));
    float* g = (float*)(ws + (8 << 20));
    u32* gf = (u32*)(ws + (9 << 20));
    const int stl = li & 7, dtl2 = li >> 3;
    const int st = (xcd >> 1) * 8 + stl, dt2 = (xcd & 1) * 4 + dtl2;
    u32x4 xv[2], gv;
    if (w < 4) {
#pragma unroll
        for (int i = 0; i < 2; i++) xv[i] = *(const u32x4*)(u + (size_t)(b * 8 + 2 * w + i) * D + lane * 8);
        gv = *(const u32x4*)(gw + lane * 8);
    }
    u32 cdummy = 0;
    if (w == 7) {
        const unsigned long long pc = __builtin_amdgcn_s_getpc() & ~127ull;
        int idx = li * 8 + lane;
        if (lane < 8 && idx * 128 < CODE_BYTES) touch(cdummy, (unsigned)(idx * 128), (const void*)pc);
    }
    if (w == 6 && li < 8) {
        const unsigned off = (unsigned)lane * 65536u;
        const char* base; unsigned lim;
        if (li < 3) { base = ws + (size_t)li * (4u << 20); lim = li < 2 ? (4u << 20) : (2u << 20); }
        else if (li < 5) { base = (const char*)w1 + (size_t)(li - 3) * (4u << 20); lim = li == 3 ? (4u << 20) : (unsigned)(L * F * 2 - (4 << 20)); }
        else if (li < 7) { base = (const char*)w2 + (size_t)(li - 5) * (4u << 20); lim = li == 5 ? (4u << 20) : (unsigned)(L * F * 2 - (4 << 20)); }
        else { base = (const char*)out; lim = 2u << 20; }
        if (off < lim) touch(cdummy, off, base);
    }
    if (tid < 32) lf[tid] = 0;
    wait_lgkm0();
    __builtin_amdgcn_s_barrier();

    if (w < 4) {
        __builtin_amdgcn_s_setprio(3);
        const int s0 = b * 8;
        const u32 tag = ld_sc1_4(gf + GF_EPOCH) + 1u;
        lds_arrive(&lf[LF_UARR], lane);
        float gval[2];
#pragma unroll
        for (int i = 0; i < 2; i++) {
            const int r = 2 * w + i;
            float x[8];
#pragma unroll
            for (int j = 0; j < 4; j++) { x[2 * j] = lo_f(xv[i][j]); x[2 * j + 1] = hi_f(xv[i][j]); }
            float sum = 0.f, gs = 0.f;
#pragma unroll
            for (int j = 0; j < 4; j++) {
                sum += x[2 * j] + x[2 * j + 1];
                u32 p = pk2bf(x[2 * j] * lo_f(gv[j]), x[2 * j + 1] * hi_f(gv[j]));
                gs += lo_f(p) + hi_f(p);
            }
            sum = wave_sum(sum);
            gs = wave_sum(gs);
            const float mean = sum * (1.f / D);
            float sq = 0.f;
#pragma unroll
            for (int j = 0; j < 8; j++) { float dl = x[j] - mean; sq = __builtin_fmaf(dl, dl, sq); }
            sq = wave_sum(sq);
            const float var = rbf(sq * (1.f / D));
            const float m = rbf(mean);
            const float sd = rbf(__builtin_sqrtf(rbf(var + 1e-5f)));
            const float rs = __builtin_amdgcn_rcpf(sd);
            u32x4 hv;
#pragma unroll
            for (int j = 0; j < 4; j++) {
                u32 c = pk2bf(x[2 * j] - m, x[2 * j + 1] - m);
                hv[j] = pk2bf(div_nr(lo_f(c), sd, rs), div_nr(hi_f(c), sd, rs));
            }
            const int c16 = lane ^ ((r & 3) << 2);
            *(u32x4*)(smem + STG + r * 1024 + c16 * 16) = hv;
            *(u32x4*)(smem + STG + 8192 + r * 1024 + c16 * 16) = xv[i];
            gval[i] = rbf(__builtin_amdgcn_rcpf(1.f + fast_exp(-rbf(gs))));
        }
        lds_arrive(&lf[LF_LN], lane);
        lds_wait(&lf[LF_LN], 4);
        u32x4 ukeep[2];
        size_t uoff[2];
        {
            const int nt = lane >> 4, q = (lane & 15) >> 2, p = lane & 3;
            const int ks = b >> 2, kq = b & 3;
#pragma unroll
            for (int i = 0; i < 2; i++) {
                const int dt = w * 2 + i;
                const int cb = dt * 4 + nt;
                const unsigned o0 = q * 1024 + ((cb ^ (q << 1)) << 5) + p * 8;
                const unsigned o1 = o0 + 4096;
                u32x2 h0 = tr_read(smem + STG, o0), h1 = tr_read(smem + STG, o1);
                u32x2 u0 = tr_read(smem + STG + 8192, o0), u1 = tr_read(smem + STG + 8192, o1);
                const size_t off = ((size_t)((dt * 64 + ks) * 4 + nt) * 64 + kq * 16 + (lane & 15)) * 8;
                st_sc1_16(hF + off, u32x4{h0[0], h0[1], h1[0], h1[1]});
                ukeep[i] = u32x4{u0[0], u0[1], u1[0], u1[1]};
                uoff[i] = off;
            }
        }
        wait_vm0<0>();
        lds_arrive(&lf[LF_LNPUB], lane);
        if (w == 0) {
            lds_wait(&lf[LF_LNPUB], 4);
            if (lane == 0) st_sc1_4(gf + GF_H + b, tag);
            lds_arrive(&lf[LF_LNPUB], lane);
        } else {
            lds_wait(&lf[LF_LNPUB], 5);
        }
        st_sc1_16(uF + uoff[0], ukeep[0]);
        st_sc1_16(uF + uoff[1], ukeep[1]);
        if (lane < 2) st_sc1_4(g + s0 + 2 * w + lane, __builtin_bit_cast(u32, lane ? gval[1] : gval[0]));
        lds_arrive(&lf[LF_LNST], lane);
        __builtin_amdgcn_s_setprio(0);
        const bool has1 = li < 28;
        const int ftl = li % 7, dtl = li / 7;
        const int ft = (xcd >> 1) * 7 + ftl, dt = (xcd & 1) * 4 + dtl;
        u32x2 bb[4];
        GemmCtx<16, F * 2, true> c1;
        wait_vm0<0>();
        lds_arrive(&lf[LF_LNPUB2], lane);
        if (w == 1) {
            lds_wait(&lf[LF_LNPUB2], 4);
            if (lane == 0) st_sc1_4(gf + GF_H2 + b, tag);
            lds_arrive(&lf[LF_LNPUB2], lane);
        } else {
            lds_wait(&lf[LF_LNPUB2], 5);
        }
        if (has1) {
#pragma unroll
            for (int mt = 0; mt < 4; mt++) gload_early8(bb[mt], b1 + ft * 64 + mt * 16 + (lane >> 4) * 4);
            gemm_begin(c1, smem, (const char*)w1 + ((size_t)ft * 64) * 2, (size_t)512 * F * 2,
                       (const char*)hF + ((size_t)(dt * 64 + w * 16)) * 4096, RING, w, ROT1, lane);
        }
        if (w == 0) {
            poll_flags256(gf + GF_H, tag, lane);
            if (lane == 0) __hip_atomic_store(&lf[LF_H], 1u, __ATOMIC_RELAXED, __HIP_MEMORY_SCOPE_WORKGROUP);
        } else {
            lds_wait(&lf[LF_H], 1);
        }
        if (has1) {
            gemm_run(c1, &lf[LF_W0], 1u, w);
            lds_arrive(&lf[LF_G1DONE], lane);
#pragma unroll
            for (int mt = 0; mt < 4; mt++) tie(bb[mt]);
            f32x4 own[4];
            {
                char* mine = smem + RING + w * 16384;
#define XCH(k) if (w == k) { \
                _Pragma("unroll") for (int nt = 0; nt < 4; nt++) _Pragma("unroll") for (int mt = 0; mt < 4; mt++) \
                    if (nt != k) *(f32x4*)(mine + (nt * 4 + mt) * 1024 + lane * 16) = c1.acc[mt][nt]; \
                _Pragma("unroll") for (int mt = 0; mt < 4; mt++) { own[mt] = c1.acc[mt][k]; asm volatile("" : "+v"(own[mt])); } }
                XCH(0) XCH(1) XCH(2) XCH(3)
#undef XCH
            }
            lds_arrive(&lf[LF_X1], lane);
            lds_wait(&lf[LF_X1], 4);
#pragma unroll
            for (int o = 1; o < 4; o++) {
                const int ow = (w + o) & 3;
#pragma unroll
                for (int mt = 0; mt < 4; mt++)
                    own[mt] += *(const f32x4*)(smem + RING + ow * 16384 + (w * 4 + mt) * 1024 + lane * 16);
            }
            lds_arrive(&lf[LF_X1R], lane);
            const int mq = lane >> 4, n = lane & 15;
#pragma unroll
            for (int mt = 0; mt < 4; mt++) {
                float v[4];
                {
                    u32 p0 = pk2bf(own[mt][0], own[mt][1]), p1 = pk2bf(own[mt][2], own[mt][3]);
                    u32 s0_ = pk2bf(lo_f(p0) + lo_f(bb[mt][0]), hi_f(p0) + hi_f(bb[mt][0]));
                    u32 s1_ = pk2bf(lo_f(p1) + lo_f(bb[mt][1]), hi_f(p1) + hi_f(bb[mt][1]));
                    v[0] = lo_f(s0_); v[1] = hi_f(s0_); v[2] = lo_f(s1_); v[3] = hi_f(s1_);
                }
#pragma unroll
                for (int i = 0; i < 4; i++) {
                    const float x = v[i];
                    const float p = __builtin_fmaf(x * x, -2.f * 0.7978845608028654f * 0.044715f * 1.4426950408889634f,
                                                   -2.f * 0.7978845608028654f * 1.4426950408889634f);
                    const float e = __builtin_amdgcn_exp2f(x * p);
                    v[i] = x * __builtin_amdgcn_rcpf(1.f + e);
                }
                u32x2 o = {pk2bf(v[0], v[1]), pk2bf(v[2], v[3])};
                const int ks = ft * 2 + (mt >> 1), kq = 2 * (mt & 1) + (mq >> 1);
                char* dst = (char*)UF + ((size_t)((dt * 56 + ks) * 4 + w) * 64 + kq * 16 + n) * 16 + (mq & 1) * 8;
                st_sc1_8(dst, o);
            }
            wait_vm0<0>();
            lds_arrive(&lf[LF_UPUB], lane);
            if (w == 0) {
                lds_wait(&lf[LF_UPUB], 4);
                if (lane == 0) st_sc1_4(gf + GF_U + dt * 32 + ft, tag);
                lds_arrive(&lf[LF_UPUB], lane);
            } else {
                lds_wait(&lf[LF_UPUB], 5);
            }
            lds_wait(&lf[LF_X1R], 4);
        }
        if (!has1) lds_arrive(&lf[LF_G1DONE], lane);
        GemmCtx<14, L * 2, false> c2;
        gemm_begin(c2, smem, (const char*)w2 + ((size_t)st * 64) * 2, (size_t)448 * L * 2,
                   (const char*)UF + ((size_t)(dt2 * 56 + w * 14)) * 4096, RING, w, ROT2, lane);
        if (w == 0) {
            poll_flags(gf + GF_U + dt2 * 32, 28, tag, lane);
            if (lane == 0) {
                __hip_atomic_store(&lf[LF_U], 1u, __ATOMIC_RELAXED, __HIP_MEMORY_SCOPE_WORKGROUP);
                if (b == 0) st_sc1_4(gf + GF_EPOCH, tag);
            }
        } else {
            lds_wait(&lf[LF_U], 1);
        }
        gemm_run(c2, &lf[LF_W0], 2u, w);
        f32x4 own[4];
        {
            char* mine = smem + RING + w * 16384;
#define XCH(k) if (w == k) { \
            _Pragma("unroll") for (int nt = 0; nt < 4; nt++) _Pragma("unroll") for (int mt = 0; mt < 4; mt++) \
                if (nt != k) *(f32x4*)(mine + (nt * 4 + mt) * 1024 + lane * 16) = c2.acc[mt][nt]; \
            _Pragma("unroll") for (int mt = 0; mt < 4; mt++) { own[mt] = c2.acc[mt][k]; asm volatile("" : "+v"(own[mt])); } }
            XCH(0) XCH(1) XCH(2) XCH(3)
#undef XCH
        }
        lds_arrive(&lf[LF_X2], lane);
        lds_wait(&lf[LF_X2], 4);
#pragma unroll
        for (int o = 1; o < 4; o++) {
            const int ow = (w + o) & 3;
#pragma unroll
            for (int mt = 0; mt < 4; mt++)
                own[mt] += *(const f32x4*)(smem + RING + ow * 16384 + (w * 4 + mt) * 1024 + lane * 16);
        }
        lds_wait(&lf[LF_EPI], 4);
        {
            const int i = lane & 15, mq = lane >> 4, q = i >> 2, p = lane & 3;
            const float gg = *(const float*)(smem + STG + 8192 + (w * 16 + i) * 4);
            const float bias = *(const float*)(smem + STG + 8192 + 256 + (w * 16 + i) * 4);
            const float og = rbf(1.f - gg);
            u16* dst = out + (size_t)(st * 64 + w * 16 + i) * D + dt2 * 64 + mq * 4;
#pragma unroll
            for (int mt = 0; mt < 4; mt++) {
                const int r0 = 16 * mt + 4 * mq + q;
                const int f0 = ((r0 >> 1) & 1) | (((r0 >> 3) & 1) << 1);
                const u32x2 av = tr_read(smem, STG + r0 * 128 + ((w ^ f0) << 5) + p * 8);
                u32x2 o;
#pragma unroll
                for (int e = 0; e < 2; e++) {
                    u32 y = pk2bf(own[mt][2 * e], own[mt][2 * e + 1]);
                    y = pk2bf(lo_f(y) + bias, hi_f(y) + bias);
                    u32 t1 = pk2bf(gg * lo_f(av[e]), gg * hi_f(av[e]));
                    u32 t2 = pk2bf(og * lo_f(y), og * hi_f(y));
                    o[e] = pk2bf(lo_f(t1) + lo_f(t2), hi_f(t1) + hi_f(t2));
                }
                *(u32x2*)(dst + mt * 16) = o;
            }
        }
        return;
    }

    const int wv = w - 4, ch = wv >> 1, hf = wv & 1;
    const int d = xcd * 64 + li * 2 + ch;
    const int n = hf * 64 + lane;
    u32 r_are, r_aim, r_bre, r_bim, r_cre, r_cim, r_ldt, r_dsk, r_ep;
    {
        const unsigned po = (unsigned)(d * NS + n) * 2u;
        asm volatile("global_load_ushort %0, %9, %10\n\tglobal_load_ushort %1, %9, %11\n\t"
                     "global_load_ushort %2, %9, %12\n\tglobal_load_ushort %3, %9, %13\n\t"
                     "global_load_ushort %4, %9, %14\n\tglobal_load_ushort %5, %9, %15\n\t"
                     "global_load_ushort %6, %16, off\n\tglobal_load_ushort %7, %17, off\n\t"
                     "global_load_dword %8, %18, off sc1"
                     : "=&v"(r_are), "=&v"(r_aim), "=&v"(r_bre), "=&v"(r_bim), "=&v"(r_cre), "=&v"(r_cim), "=&v"(r_ldt), "=&v"(r_dsk), "=&v"(r_ep)
                     : "v"(po), "s"(Are), "s"(Aim), "s"(Bre), "s"(Bim), "s"(Cre), "s"(Cim), "v"(logdt + d), "v"(Dskip + d), "v"(gf + GF_EPOCH)
                     : "memory");
    }
    u32 dummy = 0;
    {
        const char* base = (const char*)w1 + ((size_t)(li * 64) * F + (xcd >> 1) * 448) * 2;
#pragma unroll
        for (int i = 0; i < T1; i++) {
            int idx = (i * 4 + wv) * 64 + lane;
            idx = idx < 448 ? idx : 447;
            const int row = idx / 7, sec = idx - row * 7;
            touch(dummy, (unsigned)(row * (F * 2) + sec * 128), base);
        }
    }
    asm volatile("s_waitcnt vmcnt(1)" : "+v"(r_are), "+v"(r_aim), "+v"(r_bre), "+v"(r_bim), "+v"(r_cre), "+v"(r_cim), "+v"(r_ldt), "+v"(r_dsk), "+v"(r_ep) :: "memory");
    const u32 tag = r_ep + 1u;
    const unsigned sb_off = SSM0 + ch * SSM_SIZE;
    char* sb = smem + sb_off;
    unsigned* fl = lf + LF_SSM + ch * 4;
    const float dt = rbf(fast_exp(lo_f(r_ldt)));
    const float zr = dt * lo_f(r_are), zi = dt * lo_f(r_aim);
    const float dsk = lo_f(r_dsk);
    f32x2 W;
    {
        const f32x2 B = {dt * lo_f(r_bre), dt * lo_f(r_bim)};
        const f32x2 C = {lo_f(r_cre), lo_f(r_cim)};
        W = cmul(C, B);
    }
    u32x4 kix[2][2];
#pragma unroll
    for (int i = 0; i < 2; i++) {
        const u32x4* pt = (const u32x4*)(KIDX.v + (hf * 128 + i * 64 + lane) * 16);
        kix[i][0] = pt[0]; kix[i][1] = pt[1];
    }
    char* tbl = sb + SSM_COPY + hf * TBL_SIZE;
    char* tw = tbl + lane * 8;
    const char* trd = tbl + (lane & 15) * TBL_PITCH + (lane >> 4) * 16;
    u32x4 Bh[8], Bl[8];
    f32x4 kacc[3];
    {
        f32x2 E8 = cexp_scaled(zr, zi, 8.f);
        E8.y = -E8.y;
        f32x2 pw[16];
        pw[0] = f32x2{1.f, 0.f};
        pw[1] = E8;
#pragma unroll
        for (int q = 2; q < 16; q++) pw[q] = (q & 1) ? cmul(pw[q - 1], E8) : cmul(pw[q / 2], pw[q / 2]);
#pragma unroll
        for (int q = 0; q < 16; q++) *(u32x2*)(tw + q * TBL_PITCH) = split_bf(pw[q]);
        wait_lgkm0();
#pragma unroll
        for (int T = 0; T < 8; T++) {
            u32x4 v = *(const u32x4*)(trd + T * 64);
            Bh[T] = u32x4{v[0], v[0], v[2], v[2]};
            Bl[T] = u32x4{v[1], v[1], v[3], v[3]};
        }
        wait_lgkm0();
    }
    {
        lds_wait(&lf[LF_LNST], 4);
        const char* base = (const char*)w1 + ((size_t)(li * 64) * F + (xcd >> 1) * 448) * 2;
#pragma unroll
        for (int i = T1; i < 2; i++) {
            int idx = (i * 4 + wv) * 64 + lane;
            idx = idx < 448 ? idx : 447;
            const int row = idx / 7, sec = idx - row * 7;
            touch(dummy, (unsigned)(row * (F * 2) + sec * 128), base);
        }
    }
    {
        const f32x2 E1 = cexp_scaled(zr, zi, 1.f);
        const f32x2 E128 = cexp_scaled(zr, zi, 128.f);
        f32x2 WY[8];
        {
            f32x2 Y[8];
            Y[0] = f32x2{1.f, 0.f}; Y[1] = E1;
#pragma unroll
            for (int c = 2; c < 8; c++) Y[c] = (c & 1) ? cmul(Y[c - 1], E1) : cmul(Y[c / 2], Y[c / 2]);
            WY[0] = W;
#pragma unroll
            for (int c = 1; c < 8; c++) WY[c] = cmul(W, Y[c]);
        }
        f32x2 Xp[17];
        Xp[0] = f32x2{1.f, 0.f}; Xp[1] = E128;
#pragma unroll
        for (int a = 2; a < 17; a++) Xp[a] = (a & 1) ? cmul(Xp[a - 1], E128) : cmul(Xp[a / 2], Xp[a / 2]);
#pragma unroll
        for (int tile = 0; tile < 3; tile++) {
#pragma unroll
            for (int r = 0; r < 16; r++) {
                f32x2 v;
                bool valid = true;
                if (tile == 0) { v = (r < 8) ? WY[r] : cmul(WY[r - 8], Xp[1]); }
                else if (tile == 1) {
                    if (r < 8) v = cmul(WY[2 * (r & 3)], Xp[2 + (r >> 2)]);
                    else v = cmul(WY[4 * (r & 1)], Xp[4 + ((r - 8) >> 1)]);
                } else {
                    if (r < 8) v = cmul(W, Xp[8 + r]);
                    else if (r == 8) v = cmul(W, Xp[16]);
                    else valid = false;
                }
                if (valid) *(u32x2*)(tw + r * TBL_PITCH) = split_bf(v);
            }
            wait_lgkm0();
            f32x4 a4 = {0.f, 0.f, 0.f, 0.f};
#pragma unroll
            for (int T = 0; T < 8; T++) {
                u32x4 av = *(const u32x4*)(trd + T * 64);
                a4 = mfma16(av, Bh[T], a4);
                a4 = mfma16(av, Bl[T], a4);
            }
            kacc[tile] = a4;
            wait_lgkm0();
        }
    }
    {
        float* P = (float*)(sb + SSM_P + hf * 3456);
        const int mq = lane >> 4, col = lane & 15;
#pragma unroll
        for (int tile = 0; tile < 3; tile++)
#pragma unroll
            for (int r = 0; r < 4; r++)
                if (!(tile == 2 && r == 1 && 0)) P[(tile * 16 + mq * 4 + r) * 18 + col] = kacc[tile][r];
        wait_lgkm0();
        if (lane == 0) P[KC_ZERO * 18] = 0.f;
    }
    if (hf == 0 && lane < 32) *(u32x4*)(sb + SSM_U + lane * 16) = u32x4{0, 0, 0, 0};
    lds_arrive(&fl[0], lane);
    touch_done(dummy);
    tie(cdummy);
    if (wv == 0) {
        poll_flags256(gf + GF_H2, tag, lane);
        if (lane == 0) __hip_atomic_store(&lf[LF_SSM + 9], 1u, __ATOMIC_RELAXED, __HIP_MEMORY_SCOPE_WORKGROUP);
    } else {
        lds_wait(&lf[LF_SSM + 9], 1);
    }
    u32x4 ug0, ug1;
    const int up0 = hf * 128 + lane, up1 = up0 + 64;
    {
        const int dt_ = d >> 6, nt = (d >> 4) & 3, m = d & 15;
        const char* src = (const char*)uF + ((size_t)(dt_ * 64 * 4 + nt) * 64 + m) * 16;
        const char* pa = src + (up0 >> 2) * 4096 + (up0 & 3) * 256;
        const char* pb = src + (up1 >> 2) * 4096 + (up1 & 3) * 256;
        asm volatile("global_load_dwordx4 %0, %2, off sc1\n\tglobal_load_dwordx4 %1, %3, off sc1"
                     : "=&v"(ug0), "=&v"(ug1) : "v"(pa), "v"(pb) : "memory");
    }
    lds_wait(&fl[0], 2);
    {
        const char* P0 = sb + SSM_P;
        const char* P1 = sb + SSM_P + 3456;
#pragma unroll
        for (int i = 0; i < 2; i++) {
            const int ub = hf * 128 + i * 64 + lane;
            float v[11];
#pragma unroll
            for (int j = 0; j < 11; j++) {
                const u32 word = kix[i][j >> 3][(j >> 1) & 3];
                const unsigned off = (j & 1) ? (word >> 16) : (word & 0xffffu);
                v[j] = *(const float*)(P0 + off) + *(const float*)(P1 + off);
            }
            u32 e[5], o[5];
#pragma unroll
            for (int k = 0; k < 5; k++) { e[k] = pk2bf(v[2 * k], v[2 * k + 1]); o[k] = pk2bf(v[2 * k + 1], v[2 * k + 2]); }
            char* dst = sb + SSM_COPY + ub * 16;
            *(u32x4*)(dst) = u32x4{e[0], e[1], e[2], e[3]};
            *(u32x4*)(dst + CP_STRIDE) = u32x4{o[0], o[1], o[2], o[3]};
            *(u32x4*)(dst + 2 * CP_STRIDE) = u32x4{e[1], e[2], e[3], e[4]};
            *(u32x4*)(dst + 3 * CP_STRIDE) = u32x4{o[1], o[2], o[3], o[4]};
        }
        if (hf == 0 && lane < 8) {
            *(u32x4*)(sb + SSM_COPY + (lane >> 1) * CP_STRIDE + (256 + (lane & 1)) * 16) = u32x4{0, 0, 0, 0};
        }
    }
    asm volatile("s_waitcnt vmcnt(0)" : "+v"(ug0), "+v"(ug1) :: "memory");
    *(u32x4*)(sb + SSM_U + 512 + up0 * 16) = ug0;
    *(u32x4*)(sb + SSM_U + 512 + up1 * 16) = ug1;
    lds_arrive(&fl[1], lane);
    lds_wait(&fl[1], 2);
    f32x4 cacc[8];
#pragma unroll
    for (int i = 0; i < 8; i++) cacc[i] = f32x4{0.f, 0.f, 0.f, 0.f};
    {
        const int m = lane & 15, q = lane >> 4;
        const int gq = m >> 2, e = m & 3, c = 3 - e;
        const char* abase = sb + SSM_COPY + c * CP_STRIDE + 4104 - 8 * gq - 16 * q - 64 * 63 + 64 * (7 - hf * 4 - 3);
        const char* bbase = sb + SSM_U + 528 + 32 * m - 16 * q - 64 * 7 + 64 * (7 - hf * 4 - 3);
#pragma unroll
        for (int rr = 0; rr < 4; rr++) {
            const char* ab = abase + 64 * (3 - rr);
            const char* bb = bbase + 64 * (3 - rr);
            u32x4 A[8], B[8];
#pragma unroll
            for (int pp = 0; pp < 8; pp++) {
                const u32x2 a0 = *(const u32x2*)(ab + 64 * (56 - 8 * pp));
                const u32x2 a1 = *(const u32x2*)(ab + 64 * (56 - 8 * pp) + 8);
                A[pp] = u32x4{a0[0], a0[1], a1[0], a1[1]};
                B[pp] = *(const u32x4*)(bb + 512 * pp);
            }
#pragma unroll
            for (int i = 0; i < 8; i++)
#pragma unroll
                for (int pp = 0; pp <= i; pp++) cacc[i] = mfma16(A[pp], B[i - pp], cacc[i]);
            if (rr == 1)
    {
        const char* base = (const char*)w2 + ((size_t)(li * 56) * L + (xcd >> 1) * 512) * 2;
#pragma unroll
        for (int i = 0; i < 2; i++) {
            const int idx = (wv * 2 + i) * 64 + lane;
            if (idx < 448) touch(dummy, (unsigned)((idx >> 3) * (L * 2) + (idx & 7) * 128), base);
        }
    }
        }
    }
    f32x4 keep[4];
    {
        char* xo = sb + SSM_X + hf * 4096;
        if (hf) {
#pragma unroll
            for (int i = 0; i < 4; i++) { *(f32x4*)(xo + i * 1024 + lane * 16) = cacc[i]; keep[i] = cacc[4 + i]; asm volatile("" : "+v"(keep[i])); }
        } else {
#pragma unroll
            for (int i = 0; i < 4; i++) { *(f32x4*)(xo + i * 1024 + lane * 16) = cacc[4 + i]; keep[i] = cacc[i]; asm volatile("" : "+v"(keep[i])); }
        }
    }
    lds_arrive(&fl[2], lane);
    lds_wait(&fl[2], 2);
    {
        const char* xi = sb + SSM_X + (hf ^ 1) * 4096;
        const int nn = lane & 15, mq = lane >> 4;
#pragma unroll
        for (int i = 0; i < 4; i++) {
            f32x4 v = keep[i];
            v += *(const f32x4*)(xi + i * 1024 + lane * 16);
            const int t = 256 * (hf * 4 + i) + 16 * nn + 4 * mq;
            const u32x2 uu = *(const u32x2*)(sb + SSM_U + 512 + t * 2);
            u32 y0 = pk2bf(v[0], v[1]), y1 = pk2bf(v[2], v[3]);
            u32 s0 = pk2bf(lo_f(uu[0]) * dsk, hi_f(uu[0]) * dsk), s1 = pk2bf(lo_f(uu[1]) * dsk, hi_f(uu[1]) * dsk);
            u32x2 o = {pk2bf(lo_f(y0) + lo_f(s0), hi_f(y0) + hi_f(s0)), pk2bf(lo_f(y1) + lo_f(s1), hi_f(y1) + hi_f(s1))};
            st_sc1_8(aT + (size_t)d * L + t, o);
        }
    }
    wait_vm0<0>();
    lds_arrive(&lf[LF_APUB], lane);
    if (wv == 0) {
        lds_wait(&lf[LF_APUB], 4);
        if (lane == 0) st_sc1_4(gf + GF_A + xcd * 32 + li, tag);
    }
    if (wv == 0) {
        poll_flags(gf + GF_A + dt2 * 32, 32, tag, lane);
        if (lane == 0) __hip_atomic_store(&lf[LF_SSM + 8], 1u, __ATOMIC_RELAXED, __HIP_MEMORY_SCOPE_WORKGROUP);
    } else {
        lds_wait(&lf[LF_SSM + 8], 1);
    }
    {
        const int r = 16 * wv + (lane >> 2);
        const int j0 = (lane & 3) * 2, j1 = j0 + 1;
        const char* src = (const char*)aT + ((size_t)(dt2 * 64 + r) * L + st * 64) * 2;
        u32x4 a, c;
        ld2_sc1_16(a, c, src + j0 * 16, src + j1 * 16);
        const int f = ((r >> 1) & 1) | (((r >> 3) & 1) << 1);
        *(u32x4*)(smem + STG + r * 128 + ((j0 ^ (f << 1)) << 4)) = a;
        *(u32x4*)(smem + STG + r * 128 + ((j1 ^ (f << 1)) << 4)) = c;
        if (lane < 16) {
            const int s = st * 64 + wv * 16 + lane;
            const u32 gv = ld_sc1_4(g + s);
            *(u32*)(smem + STG + 8192 + (wv * 16 + lane) * 4) = gv;
            *(float*)(smem + STG + 8192 + 256 + (wv * 16 + lane) * 4) = bf2f(b2[s]);
        }
    }
    lds_arrive(&lf[LF_EPI], lane);
    touch_done(dummy);
}

extern "C" void kernel_launch(void* const* d_in, const int* in_sizes, int n_in,
                              void* d_out, int out_size, void* d_ws, size_t ws_size,
                              hipStream_t stream) {
    const u16* u = (const u16*)d_in[0];
    const u16* Are = (const u16*)d_in[1];
    const u16* Aim = (const u16*)d_in[2];
    const u16* Bre = (const u16*)d_in[3];
    const u16* Bim = (const u16*)d_in[4];
    const u16* Cre = (const u16*)d_in[5];
    const u16* Cim = (const u16*)d_in[6];
    const u16* logdt = (const u16*)d_in[7];
    const u16* Dskip = (const u16*)d_in[8];
    const u16* w1 = (const u16*)d_in[10];
    const u16* b1 = (const u16*)d_in[11];
    const u16* w2 = (const u16*)d_in[12];
    const u16* b2 = (const u16*)d_in[13];
    const u16* gw = (const u16*)d_in[14];
    k_fused<<<256, 512, 0, stream>>>(u, gw, Are, Aim, Bre, Bim, Cre, Cim, logdt, Dskip, w1, b1, w2, b2, (char*)d_ws, (u16*)d_out);
}
```
